```python
import torch

def _softmax(x, axis=-1):
    return torch.softmax(x, dim=axis)
def _gelu(x):
    return torch.nn.functional.gelu(x, approximate="tanh")
def _silu(x):
    return torch.nn.functional.silu(x)
def _sigmoid(x):
    return torch.sigmoid(x)
def _layernorm(x, axis=-1, eps=1e-5):
    m = x.mean(dim=axis, keepdim=True)
    v = x.var(dim=axis, keepdim=True, unbiased=False)
    return (x - m) / torch.sqrt(v + eps)
def _scatter_add(tgt, idx, src, axis=0):
    return tgt.clone().index_add_(axis, idx, src)
def _segment_sum(src, seg, n):
    out = torch.zeros((n,) + src.shape[1:], dtype=src.dtype)
    return out.index_add_(0, seg, src)
def _pad(x, pw):
    flat = tuple(v for pair in reversed(pw) for v in pair)
    return torch.nn.functional.pad(x, flat)

kernel_name = 'recurrent_scan__mha_block_cached__dense_activation_squared_relu__decode'

def setup_inputs():

    _w = (lambda a: (lambda t: t.to(torch.bfloat16) if t.is_floating_point() else t)(torch.as_tensor(a)))
    def _A_setup():
        B, Sq, D = 2, 1, 640
        H, Sk, Dh = 32, 8192, 128
        return {
            "x": _w((lambda *s: torch.randn(*s, dtype=torch.bfloat16) * 0.05)(B, Sq, D)),
            "k_cache": _w((lambda *s: torch.randn(*s, dtype=torch.bfloat16) * 0.8)(B, H, Sk, Dh)),
            "v_cache": _w((lambda *s: torch.randn(*s, dtype=torch.bfloat16) * 0.2)(B, H, Sk, Dh)),
            "wq": _w((lambda *s: torch.randn(*s, dtype=torch.bfloat16) * 0.8)(D, H * Dh) * (D ** -0.5)),
            "wo": _w((lambda *s: torch.randn(*s, dtype=torch.bfloat16) * 0.2)(H * Dh, D) * ((H * Dh) ** -0.5)),
        }
    _A_in = _A_setup()

    def _B_setup():
        B, S, D, F = 2, 1, 640, 1728
        return {
            "x": _w((lambda *s: torch.randn(*s, dtype=torch.bfloat16) * 0.05)(B, S, D)),
            "w1": _w((lambda *s: torch.randn(*s, dtype=torch.bfloat16) * 0.1)(D, F)),
            "b1": _w((lambda *s: torch.randn(*s, dtype=torch.bfloat16) * 0.1)(F)),
            "w2": _w((lambda *s: torch.randn(*s, dtype=torch.bfloat16) * 0.1)(F, D)),
            "b2": _w((lambda *s: torch.randn(*s, dtype=torch.bfloat16) * 0.1)(D)),
        }
    _B_in = _B_setup()

    return {
        "A_x": _A_in["x"],
        "A_k_cache": _A_in["k_cache"],
        "A_v_cache": _A_in["v_cache"],
        "A_wq": _A_in["wq"],
        "A_wo": _A_in["wo"],
        "B_x": _B_in["x"],
        "B_w1": _B_in["w1"],
        "B_b1": _B_in["b1"],
        "B_w2": _B_in["w2"],
        "B_b2": _B_in["b2"],
    }

def reference(A_x, A_k_cache, A_v_cache, A_wq, A_wo, B_x, B_w1, B_b1, B_w2, B_b2):
    def _A_ref(x, k_cache, v_cache, wq, wo):
        B, Sq, D = x.shape
        H, Dh = 32, 128
        q = (lambda x, *d: x.permute(*d))((x @ wq).reshape(B, Sq, H, Dh), 0, 2, 1, 3)
        scores = torch.einsum("bhqd,bhkd->bhqk", q, k_cache) * (Dh ** -0.5)
        p = _softmax(scores, axis=-1)
        o = torch.einsum("bhqk,bhkd->bhqd", p, v_cache)
        return (lambda x, *d: x.permute(*d))(o, 0, 2, 1, 3).reshape(B, Sq, H * Dh) @ wo

    def _B_ref(x, w1, b1, w2, b2):
        h = torch.einsum("bsd,df->bsf", x, w1) + b1
        zero = torch.zeros_like(h)
        r = torch.where(h > zero, h, zero)
        h = r * r
        out = torch.einsum("bsf,fd->bsd", h, w2) + b2
        return out

    x = A_x
    xn = _layernorm(x, axis=-1)
    s = _A_ref(xn, A_k_cache, A_v_cache, A_wq, A_wo)
    g = _silu(_B_ref(xn, B_w1, B_b1, B_w2, B_b2))
    return x + s * g
```

```cpp
#define K_N0 18
#define Q_DELAY 40
#define POLL_SLEEP 16
#include <hip/hip_runtime.h>
#include <hip/hip_bf16.h>
#include <stdint.h>

namespace {

typedef unsigned u32;
typedef unsigned long long u64;
typedef unsigned short u16;
typedef u32 u32x4 __attribute__((ext_vector_type(4)));
typedef u32 u32x2 __attribute__((ext_vector_type(2)));
typedef float f32x4 __attribute__((ext_vector_type(4)));
typedef float f32x2 __attribute__((ext_vector_type(2)));
typedef short s16x4 __attribute__((ext_vector_type(4)));
typedef __bf16 bf16x2 __attribute__((ext_vector_type(2)));
typedef __bf16 bf16x8 __attribute__((ext_vector_type(8)));
typedef __amdgpu_buffer_rsrc_t rsrc_t;
#define GAS __attribute__((address_space(1)))
#define LAS __attribute__((address_space(3)))

constexpr int DM = 640, NH = 32, SK = 8192, DH = 128, FF = 1728;
constexpr int NWG = 256, NTH = 768;
constexpr int NFFN = FF / 8;
constexpr int CONS0 = 96;
constexpr u32 WS_E = 0;
constexpr u32 WS_ML = 4096;
constexpr u32 WS_Q = 8192;
constexpr u32 WS_H = WS_Q + NH * 8 * 2 * 64 * 16;
constexpr u32 WS_F = WS_H + NFFN * 64 * 16;
constexpr u32 WS_Y = WS_F + NFFN * 80 * 16;
constexpr u32 WS_END = WS_Y + NWG * 320 * 16;
constexpr u32 OOB = 0x7ffffff0u;
constexpr int AUX_NT = 2, AUX_SC1 = 16;
constexpr u32 SPIN_CAP = 1u << 16;

#define WQ_AUX 2
#define W_AUX 2
#define X_AUX 0
#define Y_SLEEP 2
#define VSIG 40
#define K_PRE 3
#define K_A 5
#define PACE 3
#define RING 3
#define BUMP 3
#define RING_END RING
#define RAMP_AT 96
#define RAMP_STEP 2
constexpr int N0 = K_N0, R = RING;
constexpr int target(int i) {
  int t = i + 1 + R;
  const int top = N0 + (i + 1 < R ? i + 1 : R);
  if (t < top) t = top;
  if (i >= 64 - BUMP && i < 64) t = i + 1 + R + (i - (64 - BUMP) + 1);
  if (i >= 64 && t < 64 + R + BUMP) t = 64 + R + BUMP;
  if (i >= RAMP_AT) { int rr = R + (i - RAMP_AT) / RAMP_STEP + 1; if (rr > RING_END) rr = RING_END; if (t < i + 1 + rr) t = i + 1 + rr; }
  return t > 128 ? 128 : t;
}

struct Params {
  const void* x; const void* k; const void* v; const void* wq; const void* wo;
  const void* w1; const void* b1; const void* w2; const void* b2; void* out; void* ws;
};

__device__ __forceinline__ rsrc_t mk_rsrc(const void* p, u32 bytes) { return __builtin_amdgcn_make_buffer_rsrc((void*)p, 0, (int)bytes, 0x00020000); }
template <int AUX> __device__ __forceinline__ u32x4 bld128(rsrc_t r, u32 voff, u32 soff) { return __builtin_amdgcn_raw_buffer_load_b128(r, (int)voff, (int)soff, AUX); }
template <int AUX> __device__ __forceinline__ u32 bld32(rsrc_t r, u32 voff, u32 soff) { return __builtin_amdgcn_raw_buffer_load_b32(r, (int)voff, (int)soff, AUX); }
__device__ __forceinline__ u32 bld16(rsrc_t r, u32 voff) { return (u32)__builtin_amdgcn_raw_buffer_load_b16(r, (int)voff, 0, 2); }
__device__ __forceinline__ float bflo(u32 x) { return __builtin_bit_cast(float, x << 16); }
__device__ __forceinline__ float bfhi(u32 x) { return __builtin_bit_cast(float, x & 0xffff0000u); }
__device__ __forceinline__ u32 pkbf(float lo, float hi) { bf16x2 v = {(__bf16)lo, (__bf16)hi}; return __builtin_bit_cast(u32, v); }
__device__ __forceinline__ float rbf(float x) { return bflo(pkbf(x, 0.f) & 0xffffu); }
__device__ __forceinline__ float dot2(u32 a, u32 b, float c) { return __builtin_amdgcn_fdot2_f32_bf16(__builtin_bit_cast(bf16x2, a), __builtin_bit_cast(bf16x2, b), c, false); }
template <int CTRL> __device__ __forceinline__ float dpp(float x) { return __builtin_bit_cast(float, __builtin_amdgcn_mov_dpp(__builtin_bit_cast(int, x), CTRL, 0xf, 0xf, true)); }
constexpr int XOR1 = 0xB1, XOR2 = 0x4E, ROR4 = 0x124, ROR8 = 0x128, ROR2 = 0x122, ROR1 = 0x121, BCAST = 0x150;
__device__ __forceinline__ float u2f(u32 x) { return __builtin_bit_cast(float, x); }
__device__ __forceinline__ u32 f2u(float x) { return __builtin_bit_cast(u32, x); }
__device__ __forceinline__ float tr32(float a, float b) { auto s = __builtin_amdgcn_permlane32_swap(f2u(a), f2u(b), false, false); return u2f(s[0]) + u2f(s[1]); }
__device__ __forceinline__ float tr16(float a, float b) { auto s = __builtin_amdgcn_permlane16_swap(f2u(a), f2u(b), false, false); return u2f(s[0]) + u2f(s[1]); }
__device__ __forceinline__ float rows_sum(float x) { x = tr16(x, x); return tr32(x, x); }
__device__ __forceinline__ float rows_max(float x) {
  auto s = __builtin_amdgcn_permlane16_swap(f2u(x), f2u(x), false, false); x = fmaxf(u2f(s[0]), u2f(s[1]));
  auto t = __builtin_amdgcn_permlane32_swap(f2u(x), f2u(x), false, false); return fmaxf(u2f(t[0]), u2f(t[1]));
}
__device__ __forceinline__ float row16_sum(float x) { x += dpp<ROR8>(x); x += dpp<ROR4>(x); x += dpp<ROR2>(x); x += dpp<ROR1>(x); return x; }
__device__ __forceinline__ float row16_max(float x) { x = fmaxf(x, dpp<ROR8>(x)); x = fmaxf(x, dpp<ROR4>(x)); x = fmaxf(x, dpp<ROR2>(x)); x = fmaxf(x, dpp<ROR1>(x)); return x; }
__device__ __forceinline__ float wave_sum(float x) { return rows_sum(row16_sum(x)); }
__device__ __forceinline__ float wave_max(float x) { return rows_max(row16_max(x)); }
template <int K> __device__ __forceinline__ float merge(float lo, float hi, int c) {
  const bool bit = (c >> K) & 1;
  const float keep = bit ? hi : lo, send = bit ? lo : hi;
  constexpr int CTRL = K == 0 ? XOR1 : K == 1 ? XOR2 : K == 2 ? ROR4 : ROR8;
  return keep + dpp<CTRL>(send);
}
template <int T> __device__ __forceinline__ float bcast(float x) { return dpp<BCAST + T>(x); }
#define SB() __builtin_amdgcn_sched_barrier(0)
template <int I> struct IC { static constexpr int value = I; };
template <int B, int E, class F> __device__ __forceinline__ void sfor(F&& f) { if constexpr (B < E) { f(IC<B>{}); sfor<B + 1, E>(f); } }
#define INL __attribute__((always_inline))

__device__ __forceinline__ bool tags_ok(const u32x4& g, u32 tA, u32 tB) { return g.y == tA && g.w == tB; }
template <int SLEEP = 2> __device__ __forceinline__ u32x4 wait_granule(u32x4 g, rsrc_t rs, u32 off, u32 tA, u32 tB, bool need) {
  bool ok = !need || tags_ok(g, tA, tB);
  if (!__all(ok)) {
#pragma unroll 1
    for (u32 spin = 0; spin < SPIN_CAP; ++spin) {
      if (!ok) { g = bld128<AUX_SC1>(rs, off, 0); ok = tags_ok(g, tA, tB); }
      if (__all(ok)) break;
      __builtin_amdgcn_s_sleep(SLEEP);
    }
  }
  return g;
}

template <int N> __device__ __forceinline__ void wait_granules(u32x4 (&g)[N], rsrc_t rs, u32 off, u32 stride, u32 tA, u32 tB) {
  bool ok = true;
#pragma unroll
  for (int k = 0; k < N; ++k) ok = ok && tags_ok(g[k], tA, tB);
  if (!__all(ok)) {
#pragma unroll 1
    for (u32 spin = 0; spin < SPIN_CAP; ++spin) {
#pragma unroll
      for (int k = 0; k < N; ++k) if (!tags_ok(g[k], tA, tB)) g[k] = bld128<AUX_SC1>(rs, off, k * stride);
      ok = true;
#pragma unroll
      for (int k = 0; k < N; ++k) ok = ok && tags_ok(g[k], tA, tB);
      if (__all(ok)) break;
      __builtin_amdgcn_s_sleep(POLL_SLEEP);
    }
  }
}
__device__ __forceinline__ void lds_wait(LAS unsigned char* word, u32 want) {
#pragma unroll 1
  for (u32 spin = 0; spin < (SPIN_CAP << 4); ++spin) {
    if (*(volatile LAS u32*)word >= want) break;
    __builtin_amdgcn_s_sleep(2);
  }
  asm volatile("" ::: "memory");
}

constexpr int L_XN = 0;
constexpr int L_QP = 2560;
constexpr int L_Q = L_QP + 4096;
constexpr int L_ML = L_Q + 256;
constexpr int L_CNT = L_ML + 64;
constexpr int L_TAG = L_CNT + 16;
constexpr int L_FW = L_CNT + 32;
constexpr int L_FP = L_FW + 64;
constexpr int L_YP = L_FP + 32;
constexpr int L_H2 = L_YP + 256;
constexpr int L_FR = L_H2 + 512;
constexpr int L_OA = L_FR + 3840;
constexpr int L_WO = L_OA + 16 * 272;
constexpr int L_TRASH = L_WO + 8 * 5120;
constexpr int L_END = L_TRASH + 1024;
static_assert(L_OA % 16 == 0 && L_WO % 16 == 0 && L_FW % 16 == 0 && L_FR % 16 == 0 && L_QP % 16 == 0, "LDS alignment");

#define STAMP(n) do {} while (0)

__global__ void __launch_bounds__(NTH, 3) fused_block(Params p) {
  __shared__ __attribute__((aligned(16))) unsigned char lds[L_END];
  const int tid = threadIdx.x, lane = tid & 63, w = __builtin_amdgcn_readfirstlane(tid >> 6), g4 = lane >> 4, c = lane & 15;
  const int bid = blockIdx.x, xc = bid & 7, rr = bid >> 3;
#define HMAP 0
  const int h = HMAP == 0 ? (rr >> 3) * 8 + xc : HMAP == 1 ? (rr >> 3) * 8 + ((xc + 2 * (rr >> 3)) & 7) : (rr >> 3) * 8 + ((xc + 3 * (rr >> 3)) & 7), b = (rr >> 2) & 1, s = rr & 3;
  const int pair = b * NH + h, prod = pair * 4 + s;
  const int fu = xc + 8 * (rr >> 3), fv = rr & 7;
  const bool is_ffn = fu < 27;
  const bool is_cons = bid >= CONS0;
  const int cj = bid - CONS0, cb = is_cons ? cj / 80 : 0, coct = is_cons ? cj % 80 : 0;
  STAMP(0);

  const rsrc_t rsWS = mk_rsrc(p.ws, WS_END);
  const rsrc_t rsK = mk_rsrc((const char*)p.k + (size_t)pair * (SK * DH * 2) + (size_t)s * (2048 * DH * 2), 2048 * DH * 2);
  const rsrc_t rsV = mk_rsrc((const char*)p.v + (size_t)pair * (SK * DH * 2) + (size_t)s * (2048 * DH * 2), 2048 * DH * 2);
  const rsrc_t rsX = mk_rsrc(p.x, 2 * DM * 2);
  const rsrc_t rsWQ = mk_rsrc(p.wq, DM * NH * DH * 2);
  const rsrc_t rsWO = mk_rsrc((const char*)p.wo + (size_t)h * (DH * DM * 2), DH * DM * 2);
  const rsrc_t rsW1 = mk_rsrc(p.w1, is_ffn ? DM * FF * 2 : 0);
  const rsrc_t rsW2 = mk_rsrc(p.w2, is_ffn ? FF * DM * 2 : 0);
  const rsrc_t rsB1 = mk_rsrc(p.b1, is_ffn ? FF * 2 : 0);
  const rsrc_t rsB2 = mk_rsrc(p.b2, DM * 2);

  auto ln_stats = [&](const u32x4& ra, u32 rb, float& mb, float& inv) INL {
    float v[10];
#pragma unroll
    for (int k = 0; k < 4; ++k) { v[2 * k] = bflo(ra[k]); v[2 * k + 1] = bfhi(ra[k]); }
    v[8] = bflo(rb); v[9] = bfhi(rb);
    float sum = 0.f;
#pragma unroll
    for (int k = 0; k < 10; ++k) sum += v[k];
    const float mean = wave_sum(sum) * (1.f / DM);
    float sq = 0.f;
#pragma unroll
    for (int k = 0; k < 10; ++k) { const float d = v[k] - mean; sq = fmaf(d, d, sq); }
    const float var = wave_sum(sq) * (1.f / DM);
    mb = rbf(mean);
    inv = 1.f / rbf(sqrtf(rbf(rbf(var) + 1e-5f)));
  };
  auto ln_row = [&](const u32x4& ra, u32 rb, float mb, float inv, int dst) INL {
#pragma unroll
    for (int k = 0; k < 5; ++k) {
      const u32 r = k < 4 ? ra[k & 3] : rb;
      const u32 d = pkbf(bflo(r) - mb, bfhi(r) - mb);
      *(u32*)(lds + dst + lane * 20 + 4 * k) = pkbf(bflo(d) * inv, bfhi(d) * inv);
    }
  };

  if (w >= 8) {
    const int hw = w - 8;
    if (hw == 0) { *(volatile LAS u32*)(LAS unsigned char*)(lds + L_CNT) = 0u; *(volatile LAS u32*)(LAS unsigned char*)(lds + L_CNT + 4) = 0u; *(volatile LAS u32*)(LAS unsigned char*)(lds + L_CNT + 8) = 0u; }
    __syncthreads();
    const u64 E = __hip_atomic_load((GAS u64*)p.ws, __ATOMIC_RELAXED, __HIP_MEMORY_SCOPE_AGENT);
    const u32x4 x0a = bld128<X_AUX>(rsX, lane * 20, 0), x1a = bld128<X_AUX>(rsX, 1280 + lane * 20, 0);
    const u32 x0b = bld32<X_AUX>(rsX, lane * 20 + 16, 0), x1b = bld32<X_AUX>(rsX, 1280 + lane * 20 + 16, 0);
    const int qrow = 80 * (4 * b + s) + 20 * hw;
    u32x4 WQ[5]; u32 xr0[5], xr1[5];
#pragma unroll
    for (int k = 0; k < 5; ++k) WQ[k] = bld128<WQ_AUX>(rsWQ, g4 * 8192 + c * 16, (qrow + 4 * k) * 8192 + h * 256);
#pragma unroll
    for (int k = 0; k < 5; ++k) { xr0[k] = __builtin_amdgcn_raw_buffer_load_b16(rsX, (qrow + 4 * k + g4) * 2, 0, X_AUX); xr1[k] = __builtin_amdgcn_raw_buffer_load_b16(rsX, 1280 + (qrow + 4 * k + g4) * 2, 0, X_AUX); }
    float mb0, inv0, mb1, inv1;
    ln_stats(x0a, x0b, mb0, inv0);
    ln_stats(x1a, x1b, mb1, inv1);
    STAMP(9);
    {
      float a[16];
#pragma unroll
      for (int k = 0; k < 16; ++k) a[k] = 0.f;
#pragma unroll
      for (int k = 0; k < 5; ++k) {
        const float x0 = rbf(rbf(bflo(xr0[k]) - mb0) * inv0), x1 = rbf(rbf(bflo(xr1[k]) - mb1) * inv1);
#pragma unroll
        for (int u = 0; u < 4; ++u) {
          const float lo = bflo(WQ[k][u]), hi = bfhi(WQ[k][u]);
          a[2 * u] = fmaf(x0, lo, a[2 * u]); a[2 * u + 1] = fmaf(x0, hi, a[2 * u + 1]);
          a[8 + 2 * u] = fmaf(x1, lo, a[8 + 2 * u]); a[8 + 2 * u + 1] = fmaf(x1, hi, a[8 + 2 * u + 1]);
        }
      }
#pragma unroll
      for (int e = 0; e < 8; ++e) a[e] = tr32(a[e], a[8 + e]);
      const f32x4 z = {tr16(a[0], a[4]), tr16(a[1], a[5]), tr16(a[2], a[6]), tr16(a[3], a[7])};
      *(f32x4*)(lds + L_QP + (hw * 256 + (g4 >> 1) * 128 + 8 * c + 4 * (g4 & 1)) * 4) = z;
      asm volatile("s_waitcnt lgkmcnt(0)" ::: "memory");
      if (lane == 0) __hip_atomic_fetch_add((LAS u32*)(LAS unsigned char*)(lds + L_CNT + 4), 1u, __ATOMIC_RELAXED, __HIP_MEMORY_SCOPE_WORKGROUP);
    }
    if (hw != 0) return;
    const u32 tA = (u32)E + 0x9E3779B9u, tB = (u32)(E >> 32) + 0x85EBCA6Bu;
    *(u32x2*)(lds + (lane == 0 ? L_TAG : L_TRASH + lane * 8)) = u32x2{tA, tB};
    lds_wait((LAS unsigned char*)(lds + L_CNT + 4), 4u);
    STAMP(7);
    {
      f32x2 t0 = {0.f, 0.f}, t1 = {0.f, 0.f};
#pragma unroll
      for (int k = 0; k < 4; ++k) { t0 += *(const f32x2*)(lds + L_QP + (k * 256 + 2 * lane) * 4); t1 += *(const f32x2*)(lds + L_QP + (k * 256 + 128 + 2 * lane) * 4); }
      const u32 offP = WS_Q + (((h * 8 + b * 4 + s) * 2) * 64 + lane) * 16;
      __builtin_amdgcn_raw_buffer_store_b128(u32x4{f2u(t0.x), tA, f2u(t0.y), tB}, rsWS, (int)offP, 0, AUX_SC1);
      __builtin_amdgcn_raw_buffer_store_b128(u32x4{f2u(t1.x), tA, f2u(t1.y), tB}, rsWS, (int)offP, 64 * 16, AUX_SC1);
      const u32 offQ = WS_Q + ((h * 8 * 2 + b) * 64 + lane) * 16;
      u32x4 g[8];
      if (Q_DELAY) __builtin_amdgcn_s_sleep(Q_DELAY);
#pragma unroll
      for (int k = 0; k < 8; ++k) g[k] = bld128<AUX_SC1>(rsWS, offQ, k * 2 * 64 * 16);
      STAMP(6);
      wait_granules<8>(g, rsWS, offQ, 2 * 64 * 16, tA, tB);
      float q0 = 0.f, q1 = 0.f;
#pragma unroll
      for (int k = 0; k < 8; ++k) { q0 += u2f(g[k].x); q1 += u2f(g[k].z); }
      *(u32*)(lds + L_Q + lane * 4) = pkbf(q0, q1);
    }
    STAMP(1);
    __syncthreads();
    const u32 B2v = bld16(rsB2, (8 * coct + (lane & 7)) * 2), XV = bld16(rsX, cb * 1280 + (8 * coct + (lane & 7)) * 2);
    ln_row(x0a, x0b, mb0, inv0, L_XN);
    ln_row(x1a, x1b, mb1, inv1, L_XN + 1280);
    if (is_ffn) {
      u32x4 W1[10];
#pragma unroll
      for (int k = 0; k < 10; ++k) W1[k] = bld128<W_AUX>(rsW1, (lane >> 3) * (FF * 2) + (lane & 7) * 16, (80 * fv + 8 * k) * (FF * 2) + fu * 128);
      SB();
      float hv[16];
#pragma unroll
      for (int k = 0; k < 16; ++k) hv[k] = 0.f;
#pragma unroll
      for (int k = 0; k < 10; ++k) {
        const int row = 80 * fv + 8 * k + (lane >> 3);
        const float x0 = bflo(*(const u16*)(lds + L_XN + row * 2)), x1 = bflo(*(const u16*)(lds + L_XN + 1280 + row * 2));
#pragma unroll
        for (int u = 0; u < 4; ++u) {
          const float lo = bflo(W1[k][u]), hi = bfhi(W1[k][u]);
          hv[2 * u] = fmaf(x0, lo, hv[2 * u]); hv[2 * u + 1] = fmaf(x0, hi, hv[2 * u + 1]);
          hv[8 + 2 * u] = fmaf(x1, lo, hv[8 + 2 * u]); hv[8 + 2 * u + 1] = fmaf(x1, hi, hv[8 + 2 * u + 1]);
        }
      }
      SB();
#pragma unroll
      for (int e = 0; e < 8; ++e) hv[e] = merge<3>(hv[e], hv[8 + e], c);
#pragma unroll
      for (int e = 0; e < 4; ++e) hv[e] = tr16(hv[e], hv[e + 4]);
      const float h0 = tr32(hv[0], hv[2]), h1 = tr32(hv[1], hv[3]);
      const u32 gi = ((lane >> 3) & 1) * 32 + 4 * (lane & 7) + 2 * ((lane >> 4) & 1) + (lane >> 5);
      __builtin_amdgcn_raw_buffer_store_b128(u32x4{f2u(h0), tA, f2u(h1), tB}, rsWS, (int)(WS_H + ((fu * 8 + fv) * 64 + gi) * 16), 0, AUX_SC1);
    }
    STAMP(2);
      lds_wait((LAS unsigned char*)(lds + L_CNT), 8u);
      const f32x2 ml = *(const f32x2*)(lds + L_ML + (lane & 7) * 8);
      const float mg = wave_max(ml.x);
      const float lg = row16_sum(c < 8 ? ml.y * __builtin_amdgcn_exp2f(ml.x - mg) : 0.f);
      __builtin_amdgcn_raw_buffer_store_b128(u32x4{f2u(mg), tA, f2u(lg), tB}, rsWS, (int)(lane == 0 ? WS_ML + prod * 16 : OOB), 0, AUX_SC1);
      STAMP(3);
    if (is_ffn) {
      u32x4 W2[11];
      const u32 B1v = bld32<W_AUX>(rsB1, (lane & 31) * 4, fu * 128);
      const int rg = lane / 10, ch = lane - 10 * rg;
#pragma unroll
      for (int k = 0; k < 11; ++k) W2[k] = bld128<W_AUX>(rsW2, (lane < 60 && 6 * k + rg < 64) ? rg * (DM * 2) + ch * 16 : OOB, (64 * fu + 6 * k) * (DM * 2) + fv * 160);
      SB();
      const u32 offH = WS_H + (fu * 8 * 64 + lane) * 16;
      u32x4 g[8];
#pragma unroll
      for (int k = 0; k < 8; ++k) g[k] = bld128<AUX_SC1>(rsWS, offH, k * 64 * 16);
      wait_granules<8>(g, rsWS, offH, 64 * 16, tA, tB);
      float s0 = 0.f, s1 = 0.f;
#pragma unroll
      for (int k = 0; k < 8; ++k) { s0 += u2f(g[k].x); s1 += u2f(g[k].z); }
      s0 = rbf(rbf(s0) + bflo(B1v)); s1 = rbf(rbf(s1) + bfhi(B1v));
      s0 = s0 > 0.f ? s0 : 0.f; s1 = s1 > 0.f ? s1 : 0.f;
      *(f32x2*)(lds + L_H2 + lane * 8) = f32x2{rbf(s0 * s0), rbf(s1 * s1)};
      float ov[16];
#pragma unroll
      for (int k = 0; k < 16; ++k) ov[k] = 0.f;
#pragma unroll
      for (int k = 0; k < 11; ++k) {
        const int r = (6 * k + rg) & 63;
        const float a0 = *(const float*)(lds + L_H2 + r * 4), a1 = *(const float*)(lds + L_H2 + 256 + r * 4);
#pragma unroll
        for (int u = 0; u < 4; ++u) {
          const float lo = bflo(W2[k][u]), hi = bfhi(W2[k][u]);
          ov[2 * u] = fmaf(a0, lo, ov[2 * u]); ov[2 * u + 1] = fmaf(a0, hi, ov[2 * u + 1]);
          ov[8 + 2 * u] = fmaf(a1, lo, ov[8 + 2 * u]); ov[8 + 2 * u + 1] = fmaf(a1, hi, ov[8 + 2 * u + 1]);
        }
      }
      unsigned char* fr = lds + (lane < 60 ? L_FR + lane * 64 : L_TRASH + (lane & 3) * 64);
#pragma unroll
      for (int k = 0; k < 4; ++k) *(f32x4*)(fr + 16 * k) = f32x4{ov[4 * k], ov[4 * k + 1], ov[4 * k + 2], ov[4 * k + 3]};
#pragma unroll
      for (int j = 0; j < 2; ++j) {
        const int gq = lane + 64 * j, gb = gq >= 40 ? 1 : 0, cp = (gq - 40 * gb) % 40;
        f32x2 t = {0.f, 0.f};
#pragma unroll
        for (int k = 0; k < 6; ++k) t += *(const f32x2*)(lds + L_FR + (k * 10 + (cp >> 2)) * 64 + (gb * 8 + 2 * (cp & 3)) * 4);
        __builtin_amdgcn_raw_buffer_store_b128(u32x4{f2u(t.x), tA, f2u(t.y), tB}, rsWS, (int)(gq < 80 ? WS_F + ((fu * 8 + fv) * 80 + gq) * 16 : OOB), 0, AUX_SC1);
      }
    }
    {
      const u32 offML = WS_ML + (pair * 4 + (lane & 3)) * 16;
      u32x4 g = bld128<AUX_SC1>(rsWS, offML, 0);
      g = wait_granule(g, rsWS, offML, tA, tB, true);
      const float mv = u2f(g.x), M = row16_max(mv);
      const float L = row16_sum(c < 4 ? u2f(g.z) * __builtin_amdgcn_exp2f(mv - M) : 0.f);
      *(float*)(lds + (lane < 16 ? L_FW : L_TRASH) + lane * 4) = __builtin_amdgcn_exp2f(ml.x - M) / L;
    }
    STAMP(4);
    float gate = 0.f;
    if (is_cons) {
      lds_wait((LAS unsigned char*)(lds + L_CNT + 8), 1u);
      u32x4 FG[2]; u32 offF[2];
      const int cv = coct / 10, cp0 = (coct - 10 * cv) * 4;
#pragma unroll
      for (int k = 0; k < 2; ++k) {
        const int idx = lane + 64 * k;
        offF[k] = idx < 108 ? WS_F + (((idx >> 2) * 8 + cv) * 80 + cb * 40 + cp0 + (idx & 3)) * 16 : OOB;
        FG[k] = bld128<AUX_SC1>(rsWS, offF[k], 0);
      }
      FG[0] = wait_granule(FG[0], rsWS, offF[0], tA, tB, true);
      FG[1] = wait_granule(FG[1], rsWS, offF[1], tA, tB, lane < 44);
      float f0 = u2f(FG[0].x) + (lane < 44 ? u2f(FG[1].x) : 0.f), f1 = u2f(FG[0].z) + (lane < 44 ? u2f(FG[1].z) : 0.f);
      f0 += dpp<ROR4>(f0); f1 += dpp<ROR4>(f1); f0 += dpp<ROR8>(f0); f1 += dpp<ROR8>(f1);
      f0 = rows_sum(f0); f1 = rows_sum(f1);
      *(f32x2*)(lds + (lane < 4 ? L_FP : L_TRASH) + lane * 8) = f32x2{f0, f1};
      const float fs = *(const float*)(lds + L_FP + (lane & 7) * 4);
      const float o = rbf(rbf(fs) + bflo(B2v));
      gate = rbf(o / (1.f + __expf(-o)));
    }
    STAMP(5);
    __syncthreads();
    if (!is_cons) return;
    u32x4 og;
    const u32 offO = WS_Y + ((128 * (cb ^ 1) + lane) * 320) * 16;
    __syncthreads();
    {
      float ys = 0.f;
#pragma unroll
      for (int k = 0; k < 8; ++k) ys += *(const float*)(lds + L_YP + (k * 8 + (lane & 7)) * 4);
      const float res = bflo(XV) + rbf(rbf(ys) * gate);
      if (lane < 8) ((GAS u16*)p.out)[cb * DM + 8 * coct + lane] = (u16)(pkbf(res, 0.f) & 0xffffu);
    }
    STAMP(8);
    if (bid == CONS0) {
      og = bld128<AUX_SC1>(rsWS, offO, 0);
      og = wait_granule(og, rsWS, offO, tA, tB, true);
      og = bld128<AUX_SC1>(rsWS, offO + 64 * 320 * 16, 0);
      og = wait_granule(og, rsWS, offO + 64 * 320 * 16, tA, tB, true);
      if (lane == 0) __hip_atomic_store((GAS u64*)p.ws, (u64)tA | ((u64)tB << 32), __ATOMIC_RELAXED, __HIP_MEMORY_SCOPE_AGENT);
    }
    return;
  }

  const u32 vKV = w * 4096 + lane * 16;
  u32x4 S[128];
  auto ldS = [&](int i) INL -> u32x4 { const int t = i & 63; return bld128<AUX_NT>(i < 64 ? rsK : rsV, vKV + (t & 3) * 1024, (t >> 2) * 32768); };
  sfor<0, PACE>([&](auto I) INL { S[decltype(I)::value] = ldS(decltype(I)::value); });
  SB();
  __syncthreads();
  sfor<PACE, N0>([&](auto I) INL {
    constexpr int n = decltype(I)::value;
    asm volatile("" :: "v"(S[n - PACE][0]));
    SB();
    S[n] = ldS(n);
    SB();
    if constexpr (n == 6) STAMP(9);
    if constexpr (n == 10) STAMP(10);
    if constexpr (n == 14) STAMP(11);
  });
  STAMP(6);
  __syncthreads();
  u32 q[4];
  { const u32x4 qq = *(const u32x4*)(lds + L_Q + c * 16); q[0] = qq[0]; q[1] = qq[1]; q[2] = qq[2]; q[3] = qq[3]; }
  STAMP(2);

  float sc[4], d0 = 0.f, m1a = 0.f, m2a = 0.f, m3a = 0.f, m1 = 0.f, m2 = 0.f;
  sfor<0, 64>([&](auto I) INL {
    constexpr int i = decltype(I)::value;
    float d = dot2(S[i][0], q[0], 0.f);
    asm volatile("" : "+v"(d));
    SB();
    sfor<(i == 0 ? N0 : target(i - 1)), target(i)>([&](auto N) INL { S[decltype(N)::value] = ldS(decltype(N)::value); });
    SB();
    d = dot2(S[i][1], q[1], d); d = dot2(S[i][2], q[2], d); d = dot2(S[i][3], q[3], d);
    asm volatile("" : "+v"(d));
    constexpr int t = i & 15;
    if ((t & 1) == 0) d0 = d;
    else {
      const float a1 = merge<0>(d0, d, c);
      if ((t & 2) == 0) m1a = a1;
      else {
        m1 = merge<1>(m1a, a1, c);
        if ((t & 4) == 0) m2a = m1;
        else {
          m2 = merge<2>(m2a, m1, c);
          if ((t & 8) == 0) m3a = m2;
          else sc[i >> 4] = merge<3>(m3a, m2, c);
        }
      }
    }
    SB();
  });
  STAMP(3);
  constexpr float C2 = 0.08838834764831845f * 1.4426950408889634f;
  float pr[4];
  {
#pragma unroll
    for (int j = 0; j < 4; ++j) sc[j] *= C2;
    const float mw = wave_max(fmaxf(fmaxf(sc[0], sc[1]), fmaxf(sc[2], sc[3])));
#pragma unroll
    for (int j = 0; j < 4; ++j) pr[j] = __builtin_amdgcn_exp2f(sc[j] - mw);
    const float lw = wave_sum((pr[0] + pr[1]) + (pr[2] + pr[3]));
    *(f32x2*)(lds + (lane == 0 ? L_ML + w * 8 : L_TRASH + lane * 8)) = f32x2{mw, lw};
    asm volatile("s_waitcnt lgkmcnt(0)" ::: "memory");
    if (lane == 0) __hip_atomic_fetch_add((LAS u32*)(LAS unsigned char*)(lds + L_CNT), 1u, __ATOMIC_RELAXED, __HIP_MEMORY_SCOPE_WORKGROUP);
  }
  SB();

  float acc[8];
#pragma unroll
  for (int k = 0; k < 8; ++k) acc[k] = 0.f;
  u32x4 WO[5], BF[4][5];
  u32 vWO[5];
#pragma unroll
  for (int k = 0; k < 5; ++k) { const int it = lane + 64 * k; vWO[k] = (it / 10) * (DM * 2) + (it % 10) * 16 + w * 160; }
  sfor<64, 128>([&](auto I) INL {
    constexpr int i = decltype(I)::value;
    constexpr int vi = i - 64, t = vi & 15;
    const u32x4 V = S[i];
    float v0 = bflo(V[0]);
    asm volatile("" : "+v"(v0));
    SB();
    sfor<target(i - 1), target(i)>([&](auto N) INL { S[decltype(N)::value] = ldS(decltype(N)::value); });
    if constexpr (vi == VSIG) { if (tid == 0) __hip_atomic_fetch_add((LAS u32*)(LAS unsigned char*)(lds + L_CNT + 8), 1u, __ATOMIC_RELAXED, __HIP_MEMORY_SCOPE_WORKGROUP); }
    if constexpr (vi >= 2 && vi < 50 && (vi - 2) % 12 < 10 && (vi - 2) % 2 == 0) { const int rho = (vi - 2) / 12, k = ((vi - 2) % 12) / 2;
 WO[k] = bld128<W_AUX>(rsWO, vWO[k], rho * 32 * DM * 2);
 }
    SB();
    {
      const float pb = bcast<t>(pr[vi >> 4]);
      acc[0] = fmaf(pb, v0, acc[0]); acc[1] = fmaf(pb, bfhi(V[0]), acc[1]);
#pragma unroll
      for (int u = 1; u < 4; ++u) { acc[2 * u] = fmaf(pb, bflo(V[u]), acc[2 * u]); acc[2 * u + 1] = fmaf(pb, bfhi(V[u]), acc[2 * u + 1]); }
      asm volatile("" : "+v"(acc[0]), "+v"(acc[1]), "+v"(acc[2]), "+v"(acc[3]), "+v"(acc[4]), "+v"(acc[5]), "+v"(acc[6]), "+v"(acc[7]));
    }
    if constexpr (vi >= 13 && vi <= 49 && (vi - 13) % 12 == 0) {
      unsigned char* st = lds + L_WO + w * 5120;
#pragma unroll
      for (int k = 0; k < 5; ++k) *(u32x4*)(st + (lane + 64 * k) * 16) = WO[k];
    }
    if constexpr (vi >= 14 && vi <= 50 && (vi - 14) % 12 == 0) {
      constexpr int rho = (vi - 14) / 12;
      LAS unsigned char* st = (LAS unsigned char*)(lds + L_WO + w * 5120);
#pragma unroll
      for (int n = 0; n < 5; ++n) {
        LAS unsigned char* a = st + (8 * g4 + (c >> 2)) * 160 + 32 * n + 8 * (c & 3);
        const u32x2 lo = __builtin_bit_cast(u32x2, __builtin_amdgcn_ds_read_tr16_b64_v4i16((LAS s16x4*)a));
        const u32x2 hi = __builtin_bit_cast(u32x2, __builtin_amdgcn_ds_read_tr16_b64_v4i16((LAS s16x4*)(a + 4 * 160)));
        BF[rho][n] = u32x4{lo.x, lo.y, hi.x, hi.y};
      }
    }
    if constexpr (vi >= 16 && vi <= 52 && (vi - 16) % 12 == 0) {
      constexpr int rho = (vi - 16) / 12;
#pragma unroll
      for (int n = 0; n < 5; ++n) asm volatile("" : "+v"(BF[rho][n]));
    }
    SB();
  });
  STAMP(4);

  {
    const float z0 = tr16(tr32(acc[0], acc[4]), tr32(acc[2], acc[6])), z1 = tr16(tr32(acc[1], acc[5]), tr32(acc[3], acc[7]));
    const u32 hi = pkbf(z0, z1), lo = pkbf(z0 - bflo(hi), z1 - bfhi(hi));
    *(u32*)(lds + L_OA + w * 272 + 16 * c + 4 * g4) = hi;
    *(u32*)(lds + L_OA + (8 + w) * 272 + 16 * c + 4 * g4) = lo;
  }
  __syncthreads();
  const u32x2 tg = *(const u32x2*)(lds + L_TAG);
  const u32 tA = tg.x, tB = tg.y;
  {
    f32x4 D[5];
#pragma unroll
    for (int n = 0; n < 5; ++n) D[n] = f32x4{0.f, 0.f, 0.f, 0.f};
    const f32x4 fr = *(const f32x4*)(lds + L_FW + 16 * g4);
#pragma unroll
    for (int rho = 0; rho < 4; ++rho) {
      const u32x4 A = *(const u32x4*)(lds + L_OA + c * 272 + 64 * rho + 16 * g4);
#pragma unroll
      for (int n = 0; n < 5; ++n) D[n] = __builtin_amdgcn_mfma_f32_16x16x32_bf16(__builtin_bit_cast(bf16x8, A), __builtin_bit_cast(bf16x8, BF[rho][n]), D[n], 0, 0, 0);
    }
    float e[5];
#pragma unroll
    for (int n = 0; n < 5; ++n) e[n] = fmaf(D[n][0], fr[0], D[n][1] * fr[1]) + fmaf(D[n][2], fr[2], D[n][3] * fr[3]);
    const float ya = tr16(tr32(e[0], e[2]), tr32(e[1], e[3]));
    const float yb = rows_sum(e[4]);
    const float yan = dpp<XOR1>(ya), ybn = dpp<XOR1>(yb);
    const bool odd = c & 1;
    const u32 cp = odd ? 40 * w + 32 + (c >> 1) : 40 * w + 8 * g4 + (c >> 1);
    const u32 off = (!odd || g4 == 0) ? WS_Y + (prod * 320 + cp) * 16 : OOB;
    const u32x4 gr = odd ? u32x4{f2u(ybn), tA, f2u(yb), tB} : u32x4{f2u(ya), tA, f2u(yan), tB};
    __builtin_amdgcn_raw_buffer_store_b128(gr, rsWS, (int)off, 0, AUX_SC1);
  }
  STAMP(5);
  if (!is_cons) return;

  const u32 offY = WS_Y + ((cb * 128 + (tid >> 2)) * 320 + 4 * coct + (tid & 3)) * 16;
  u32x4 YG = bld128<AUX_SC1>(rsWS, offY, 0);
  YG = wait_granule<Y_SLEEP>(YG, rsWS, offY, tA, tB, true);
  {
    float y0 = u2f(YG.x), y1 = u2f(YG.z);
    y0 += dpp<ROR4>(y0); y1 += dpp<ROR4>(y1); y0 += dpp<ROR8>(y0); y1 += dpp<ROR8>(y1);
    y0 = rows_sum(y0); y1 = rows_sum(y1);
    *(f32x2*)(lds + (lane < 4 ? L_YP + w * 32 : L_TRASH) + lane * 8) = f32x2{y0, y1};
  }
  __syncthreads();
  STAMP(8);
}

}

extern "C" void kernel_launch(void* const* d_in, const int* in_sizes, int n_in, void* d_out, int out_size, void* d_ws, size_t ws_size, hipStream_t stream) {
  (void)in_sizes; (void)n_in; (void)out_size; (void)ws_size;
  Params p;
  p.x = d_in[0]; p.k = d_in[1]; p.v = d_in[2]; p.wq = d_in[3]; p.wo = d_in[4];
  p.w1 = d_in[6]; p.b1 = d_in[7]; p.w2 = d_in[8]; p.b2 = d_in[9]; p.out = d_out;
  p.ws = d_ws;
  hipLaunchKernelGGL(fused_block, dim3(NWG), dim3(NTH), 0, stream, p);
}
```

```cpp
#define K_N0 18 Q_DELAY
#define W_AUX 0
#define WQ_AUX 0
#include <hip/hip_runtime.h>
#include <hip/hip_bf16.h>
#include <stdint.h>

namespace {

typedef unsigned u32;
typedef unsigned long long u64;
typedef unsigned short u16;
typedef u32 u32x4 __attribute__((ext_vector_type(4)));
typedef u32 u32x2 __attribute__((ext_vector_type(2)));
typedef float f32x4 __attribute__((ext_vector_type(4)));
typedef float f32x2 __attribute__((ext_vector_type(2)));
typedef short s16x4 __attribute__((ext_vector_type(4)));
typedef __bf16 bf16x2 __attribute__((ext_vector_type(2)));
typedef __bf16 bf16x8 __attribute__((ext_vector_type(8)));
typedef __amdgpu_buffer_rsrc_t rsrc_t;
#define GAS __attribute__((address_space(1)))
#define LAS __attribute__((address_space(3)))

constexpr int DM = 640, NH = 32, SK = 8192, DH = 128, FF = 1728;
constexpr int NWG = 256, NTH = 768;
constexpr int NFFN = FF / 8;
constexpr int CONS0 = 96;
constexpr u32 WS_E = 0;
constexpr u32 WS_ML = 4096;
constexpr u32 WS_Q = 8192;
constexpr u32 WS_H = WS_Q + NH * 8 * 2 * 64 * 16;
constexpr u32 WS_F = WS_H + NFFN * 64 * 16;
constexpr u32 WS_Y = WS_F + NFFN * 80 * 16;
constexpr u32 WS_END = WS_Y + NWG * 320 * 16;
constexpr u32 OOB = 0x7ffffff0u;
constexpr int AUX_NT = 2, AUX_SC1 = 16;
constexpr u32 SPIN_CAP = 1u << 16;

#define X_AUX 0
#define K_AUX 2
#define V_AUX 2
#define Y_SLEEP 2
#define VSIG 40
#define POLL_SLEEP 1
#define Q_DELAY 0
#define K_PRE 3
#define K_A 5
#define PACE 3
#define RING 3
#define BUMP 3
#define RING_END RING
#define RAMP_AT 96
#define RAMP_STEP 2
constexpr int N0 = K_N0, R = RING;
constexpr int target(int i) {
  int t = i + 1 + R;
  const int top = N0 + (i + 1 < R ? i + 1 : R);
  if (t < top) t = top;
  if (i >= 64 - BUMP && i < 64) t = i + 1 + R + (i - (64 - BUMP) + 1);
  if (i >= 64 && t < 64 + R + BUMP) t = 64 + R + BUMP;
  if (i >= RAMP_AT) { int rr = R + (i - RAMP_AT) / RAMP_STEP + 1; if (rr > RING_END) rr = RING_END; if (t < i + 1 + rr) t = i + 1 + rr; }
  return t > 128 ? 128 : t;
}

struct Params {
  const void* x; const void* k; const void* v; const void* wq; const void* wo;
  const void* w1; const void* b1; const void* w2; const void* b2; void* out; void* ws;
};

__device__ __forceinline__ rsrc_t mk_rsrc(const void* p, u32 bytes) { return __builtin_amdgcn_make_buffer_rsrc((void*)p, 0, (int)bytes, 0x00020000); }
template <int AUX> __device__ __forceinline__ u32x4 bld128(rsrc_t r, u32 voff, u32 soff) { return __builtin_amdgcn_raw_buffer_load_b128(r, (int)voff, (int)soff, AUX); }
template <int AUX> __device__ __forceinline__ u32 bld32(rsrc_t r, u32 voff, u32 soff) { return __builtin_amdgcn_raw_buffer_load_b32(r, (int)voff, (int)soff, AUX); }
__device__ __forceinline__ u32 bld16(rsrc_t r, u32 voff) { return (u32)__builtin_amdgcn_raw_buffer_load_b16(r, (int)voff, 0, 2); }
__device__ __forceinline__ float bflo(u32 x) { return __builtin_bit_cast(float, x << 16); }
__device__ __forceinline__ float bfhi(u32 x) { return __builtin_bit_cast(float, x & 0xffff0000u); }
__device__ __forceinline__ u32 pkbf(float lo, float hi) { bf16x2 v = {(__bf16)lo, (__bf16)hi}; return __builtin_bit_cast(u32, v); }
__device__ __forceinline__ float rbf(float x) { return bflo(pkbf(x, 0.f) & 0xffffu); }
__device__ __forceinline__ float dot2(u32 a, u32 b, float c) { return __builtin_amdgcn_fdot2_f32_bf16(__builtin_bit_cast(bf16x2, a), __builtin_bit_cast(bf16x2, b), c, false); }
template <int CTRL> __device__ __forceinline__ float dpp(float x) { return __builtin_bit_cast(float, __builtin_amdgcn_mov_dpp(__builtin_bit_cast(int, x), CTRL, 0xf, 0xf, true)); }
constexpr int XOR1 = 0xB1, XOR2 = 0x4E, ROR4 = 0x124, ROR8 = 0x128, ROR2 = 0x122, ROR1 = 0x121, BCAST = 0x150;
__device__ __forceinline__ float u2f(u32 x) { return __builtin_bit_cast(float, x); }
__device__ __forceinline__ u32 f2u(float x) { return __builtin_bit_cast(u32, x); }
__device__ __forceinline__ float tr32(float a, float b) { auto s = __builtin_amdgcn_permlane32_swap(f2u(a), f2u(b), false, false); return u2f(s[0]) + u2f(s[1]); }
__device__ __forceinline__ float tr16(float a, float b) { auto s = __builtin_amdgcn_permlane16_swap(f2u(a), f2u(b), false, false); return u2f(s[0]) + u2f(s[1]); }
__device__ __forceinline__ float rows_sum(float x) { x = tr16(x, x); return tr32(x, x); }
__device__ __forceinline__ float rows_max(float x) {
  auto s = __builtin_amdgcn_permlane16_swap(f2u(x), f2u(x), false, false); x = fmaxf(u2f(s[0]), u2f(s[1]));
  auto t = __builtin_amdgcn_permlane32_swap(f2u(x), f2u(x), false, false); return fmaxf(u2f(t[0]), u2f(t[1]));
}
__device__ __forceinline__ float row16_sum(float x) { x += dpp<ROR8>(x); x += dpp<ROR4>(x); x += dpp<ROR2>(x); x += dpp<ROR1>(x); return x; }
__device__ __forceinline__ float row16_max(float x) { x = fmaxf(x, dpp<ROR8>(x)); x = fmaxf(x, dpp<ROR4>(x)); x = fmaxf(x, dpp<ROR2>(x)); x = fmaxf(x, dpp<ROR1>(x)); return x; }
__device__ __forceinline__ float wave_sum(float x) { return rows_sum(row16_sum(x)); }
__device__ __forceinline__ float wave_max(float x) { return rows_max(row16_max(x)); }
template <int K> __device__ __forceinline__ float merge(float lo, float hi, int c) {
  const bool bit = (c >> K) & 1;
  const float keep = bit ? hi : lo, send = bit ? lo : hi;
  constexpr int CTRL = K == 0 ? XOR1 : K == 1 ? XOR2 : K == 2 ? ROR4 : ROR8;
  return keep + dpp<CTRL>(send);
}
template <int T> __device__ __forceinline__ float bcast(float x) { return dpp<BCAST + T>(x); }
#define SB() __builtin_amdgcn_sched_barrier(0)
template <int I> struct IC { static constexpr int value = I; };
template <int B, int E, class F> __device__ __forceinline__ void sfor(F&& f) { if constexpr (B < E) { f(IC<B>{}); sfor<B + 1, E>(f); } }
#define INL __attribute__((always_inline))

__device__ __forceinline__ bool tags_ok(const u32x4& g, u32 tA, u32 tB) { return g.y == tA && g.w == tB; }
template <int SLEEP = 2> __device__ __forceinline__ u32x4 wait_granule(u32x4 g, rsrc_t rs, u32 off, u32 tA, u32 tB, bool need) {
  bool ok = !need || tags_ok(g, tA, tB);
  if (!__all(ok)) {
#pragma unroll 1
    for (u32 spin = 0; spin < SPIN_CAP; ++spin) {
      if (!ok) { g = bld128<AUX_SC1>(rs, off, 0); ok = tags_ok(g, tA, tB); }
      if (__all(ok)) break;
      __builtin_amdgcn_s_sleep(SLEEP);
    }
  }
  return g;
}

template <int N> __device__ __forceinline__ void wait_granules(u32x4 (&g)[N], rsrc_t rs, u32 off, u32 stride, u32 tA, u32 tB) {
  bool ok = true;
#pragma unroll
  for (int k = 0; k < N; ++k) ok = ok && tags_ok(g[k], tA, tB);
  if (!__all(ok)) {
#pragma unroll 1
    for (u32 spin = 0; spin < SPIN_CAP; ++spin) {
#pragma unroll
      for (int k = 0; k < N; ++k) if (!tags_ok(g[k], tA, tB)) g[k] = bld128<AUX_SC1>(rs, off, k * stride);
      ok = true;
#pragma unroll
      for (int k = 0; k < N; ++k) ok = ok && tags_ok(g[k], tA, tB);
      if (__all(ok)) break;
      __builtin_amdgcn_s_sleep(POLL_SLEEP);
    }
  }
}
__device__ __forceinline__ void lds_wait(LAS unsigned char* word, u32 want) {
#pragma unroll 1
  for (u32 spin = 0; spin < (SPIN_CAP << 4); ++spin) {
    if (*(volatile LAS u32*)word >= want) break;
    __builtin_amdgcn_s_sleep(2);
  }
  asm volatile("" ::: "memory");
}

constexpr int L_XN = 0;
constexpr int L_QP = 2560;
constexpr int L_Q = L_QP + 4096;
constexpr int L_ML = L_Q + 256;
constexpr int L_CNT = L_ML + 64;
constexpr int L_TAG = L_CNT + 16;
constexpr int L_FW = L_CNT + 32;
constexpr int L_FP = L_FW + 64;
constexpr int L_YP = L_FP + 32;
constexpr int L_H2 = L_YP + 256;
constexpr int L_FR = L_H2 + 512;
constexpr int L_OA = L_FR + 3840;
constexpr int L_WO = L_OA + 16 * 272;
constexpr int L_TRASH = L_WO + 8 * 5120;
constexpr int L_END = L_TRASH + 1024;
static_assert(L_OA % 16 == 0 && L_WO % 16 == 0 && L_FW % 16 == 0 && L_FR % 16 == 0 && L_QP % 16 == 0, "LDS alignment");

#define STAMP(n) do {} while (0)

__global__ void __launch_bounds__(NTH, 3) fused_block(Params p) {
  __shared__ __attribute__((aligned(16))) unsigned char lds[L_END];
  const int tid = threadIdx.x, lane = tid & 63, w = __builtin_amdgcn_readfirstlane(tid >> 6), g4 = lane >> 4, c = lane & 15;
  const int bid = blockIdx.x, xc = bid & 7, rr = bid >> 3;
#define HMAP 0
  const int h = HMAP == 0 ? (rr >> 3) * 8 + xc : HMAP == 1 ? (rr >> 3) * 8 + ((xc + 2 * (rr >> 3)) & 7) : (rr >> 3) * 8 + ((xc + 3 * (rr >> 3)) & 7), b = (rr >> 2) & 1, s = rr & 3;
  const int pair = b * NH + h, prod = pair * 4 + s;
  const int fu = xc + 8 * (rr >> 3), fv = rr & 7;
  const bool is_ffn = fu < 27;
  const bool is_cons = bid >= CONS0;
  const int cj = bid - CONS0, cb = is_cons ? cj / 80 : 0, coct = is_cons ? cj % 80 : 0;
  STAMP(0);

  const rsrc_t rsWS = mk_rsrc(p.ws, WS_END);
  const rsrc_t rsK = mk_rsrc((const char*)p.k + (size_t)pair * (SK * DH * 2) + (size_t)s * (2048 * DH * 2), 2048 * DH * 2);
  const rsrc_t rsV = mk_rsrc((const char*)p.v + (size_t)pair * (SK * DH * 2) + (size_t)s * (2048 * DH * 2), 2048 * DH * 2);
  const rsrc_t rsX = mk_rsrc(p.x, 2 * DM * 2);
  const rsrc_t rsWQ = mk_rsrc(p.wq, DM * NH * DH * 2);
  const rsrc_t rsWO = mk_rsrc((const char*)p.wo + (size_t)h * (DH * DM * 2), DH * DM * 2);
  const rsrc_t rsW1 = mk_rsrc(p.w1, is_ffn ? DM * FF * 2 : 0);
  const rsrc_t rsW2 = mk_rsrc(p.w2, is_ffn ? FF * DM * 2 : 0);
  const rsrc_t rsB1 = mk_rsrc(p.b1, is_ffn ? FF * 2 : 0);
  const rsrc_t rsB2 = mk_rsrc(p.b2, DM * 2);

  auto ln_stats = [&](const u32x4& ra, u32 rb, float& mb, float& inv) INL {
    float v[10];
#pragma unroll
    for (int k = 0; k < 4; ++k) { v[2 * k] = bflo(ra[k]); v[2 * k + 1] = bfhi(ra[k]); }
    v[8] = bflo(rb); v[9] = bfhi(rb);
    float sum = 0.f;
#pragma unroll
    for (int k = 0; k < 10; ++k) sum += v[k];
    const float mean = wave_sum(sum) * (1.f / DM);
    float sq = 0.f;
#pragma unroll
    for (int k = 0; k < 10; ++k) { const float d = v[k] - mean; sq = fmaf(d, d, sq); }
    const float var = wave_sum(sq) * (1.f / DM);
    mb = rbf(mean);
    inv = 1.f / rbf(sqrtf(rbf(rbf(var) + 1e-5f)));
  };
  auto ln_row = [&](const u32x4& ra, u32 rb, float mb, float inv, int dst) INL {
#pragma unroll
    for (int k = 0; k < 5; ++k) {
      const u32 r = k < 4 ? ra[k & 3] : rb;
      const u32 d = pkbf(bflo(r) - mb, bfhi(r) - mb);
      *(u32*)(lds + dst + lane * 20 + 4 * k) = pkbf(bflo(d) * inv, bfhi(d) * inv);
    }
  };

  if (w >= 8) {
    const int hw = w - 8;
    if (hw == 0) { *(volatile LAS u32*)(LAS unsigned char*)(lds + L_CNT) = 0u; *(volatile LAS u32*)(LAS unsigned char*)(lds + L_CNT + 4) = 0u; *(volatile LAS u32*)(LAS unsigned char*)(lds + L_CNT + 8) = 0u; }
    __syncthreads();
    const u64 E = __hip_atomic_load((GAS u64*)p.ws, __ATOMIC_RELAXED, __HIP_MEMORY_SCOPE_AGENT);
    const u32x4 x0a = bld128<X_AUX>(rsX, lane * 20, 0), x1a = bld128<X_AUX>(rsX, 1280 + lane * 20, 0);
    const u32 x0b = bld32<X_AUX>(rsX, lane * 20 + 16, 0), x1b = bld32<X_AUX>(rsX, 1280 + lane * 20 + 16, 0);
    const int qrow = 80 * (4 * b + s) + 20 * hw;
    u32x4 WQ[5]; u32 xr0[5], xr1[5];
#pragma unroll
    for (int k = 0; k < 5; ++k) WQ[k] = bld128<WQ_AUX>(rsWQ, g4 * 8192 + c * 16, (qrow + 4 * k) * 8192 + h * 256);
#pragma unroll
    for (int k = 0; k < 5; ++k) { xr0[k] = __builtin_amdgcn_raw_buffer_load_b16(rsX, (qrow + 4 * k + g4) * 2, 0, X_AUX); xr1[k] = __builtin_amdgcn_raw_buffer_load_b16(rsX, 1280 + (qrow + 4 * k + g4) * 2, 0, X_AUX); }
    float mb0, inv0, mb1, inv1;
    ln_stats(x0a, x0b, mb0, inv0);
    ln_stats(x1a, x1b, mb1, inv1);
    STAMP(9);
    {
      float a[16];
#pragma unroll
      for (int k = 0; k < 16; ++k) a[k] = 0.f;
#pragma unroll
      for (int k = 0; k < 5; ++k) {
        const float x0 = rbf(rbf(bflo(xr0[k]) - mb0) * inv0), x1 = rbf(rbf(bflo(xr1[k]) - mb1) * inv1);
#pragma unroll
        for (int u = 0; u < 4; ++u) {
          const float lo = bflo(WQ[k][u]), hi = bfhi(WQ[k][u]);
          a[2 * u] = fmaf(x0, lo, a[2 * u]); a[2 * u + 1] = fmaf(x0, hi, a[2 * u + 1]);
          a[8 + 2 * u] = fmaf(x1, lo, a[8 + 2 * u]); a[8 + 2 * u + 1] = fmaf(x1, hi, a[8 + 2 * u + 1]);
        }
      }
#pragma unroll
      for (int e = 0; e < 8; ++e) a[e] = tr32(a[e], a[8 + e]);
      const f32x4 z = {tr16(a[0], a[4]), tr16(a[1], a[5]), tr16(a[2], a[6]), tr16(a[3], a[7])};
      *(f32x4*)(lds + L_QP + (hw * 256 + (g4 >> 1) * 128 + 8 * c + 4 * (g4 & 1)) * 4) = z;
      asm volatile("s_waitcnt lgkmcnt(0)" ::: "memory");
      if (lane == 0) __hip_atomic_fetch_add((LAS u32*)(LAS unsigned char*)(lds + L_CNT + 4), 1u, __ATOMIC_RELAXED, __HIP_MEMORY_SCOPE_WORKGROUP);
    }
    if (hw != 0) return;
    const u32 tA = (u32)E + 0x9E3779B9u, tB = (u32)(E >> 32) + 0x85EBCA6Bu;
    *(u32x2*)(lds + (lane == 0 ? L_TAG : L_TRASH + lane * 8)) = u32x2{tA, tB};
    lds_wait((LAS unsigned char*)(lds + L_CNT + 4), 4u);
    STAMP(7);
    {
      f32x2 t0 = {0.f, 0.f}, t1 = {0.f, 0.f};
#pragma unroll
      for (int k = 0; k < 4; ++k) { t0 += *(const f32x2*)(lds + L_QP + (k * 256 + 2 * lane) * 4); t1 += *(const f32x2*)(lds + L_QP + (k * 256 + 128 + 2 * lane) * 4); }
      const u32 offP = WS_Q + (((h * 8 + b * 4 + s) * 2) * 64 + lane) * 16;
      __builtin_amdgcn_raw_buffer_store_b128(u32x4{f2u(t0.x), tA, f2u(t0.y), tB}, rsWS, (int)offP, 0, AUX_SC1);
      __builtin_amdgcn_raw_buffer_store_b128(u32x4{f2u(t1.x), tA, f2u(t1.y), tB}, rsWS, (int)offP, 64 * 16, AUX_SC1);
      const u32 offQ = WS_Q + ((h * 8 * 2 + b) * 64 + lane) * 16;
      u32x4 g[8];
      if (Q_DELAY) __builtin_amdgcn_s_sleep(Q_DELAY);
#pragma unroll
      for (int k = 0; k < 8; ++k) g[k] = bld128<AUX_SC1>(rsWS, offQ, k * 2 * 64 * 16);
      STAMP(6);
      wait_granules<8>(g, rsWS, offQ, 2 * 64 * 16, tA, tB);
      float q0 = 0.f, q1 = 0.f;
#pragma unroll
      for (int k = 0; k < 8; ++k) { q0 += u2f(g[k].x); q1 += u2f(g[k].z); }
      *(u32*)(lds + L_Q + lane * 4) = pkbf(q0, q1);
    }
    STAMP(1);
    __syncthreads();
    const u32 B2v = bld16(rsB2, (8 * coct + (lane & 7)) * 2), XV = bld16(rsX, cb * 1280 + (8 * coct + (lane & 7)) * 2);
    ln_row(x0a, x0b, mb0, inv0, L_XN);
    ln_row(x1a, x1b, mb1, inv1, L_XN + 1280);
    if (is_ffn) {
      u32x4 W1[10];
#pragma unroll
      for (int k = 0; k < 10; ++k) W1[k] = bld128<W_AUX>(rsW1, (lane >> 3) * (FF * 2) + (lane & 7) * 16, (80 * fv + 8 * k) * (FF * 2) + fu * 128);
      SB();
      float hv[16];
#pragma unroll
      for (int k = 0; k < 16; ++k) hv[k] = 0.f;
#pragma unroll
      for (int k = 0; k < 10; ++k) {
        const int row = 80 * fv + 8 * k + (lane >> 3);
        const float x0 = bflo(*(const u16*)(lds + L_XN + row * 2)), x1 = bflo(*(const u16*)(lds + L_XN + 1280 + row * 2));
#pragma unroll
        for (int u = 0; u < 4; ++u) {
          const float lo = bflo(W1[k][u]), hi = bfhi(W1[k][u]);
          hv[2 * u] = fmaf(x0, lo, hv[2 * u]); hv[2 * u + 1] = fmaf(x0, hi, hv[2 * u + 1]);
          hv[8 + 2 * u] = fmaf(x1, lo, hv[8 + 2 * u]); hv[8 + 2 * u + 1] = fmaf(x1, hi, hv[8 + 2 * u + 1]);
        }
      }
      SB();
#pragma unroll
      for (int e = 0; e < 8; ++e) hv[e] = merge<3>(hv[e], hv[8 + e], c);
#pragma unroll
      for (int e = 0; e < 4; ++e) hv[e] = tr16(hv[e], hv[e + 4]);
      const float h0 = tr32(hv[0], hv[2]), h1 = tr32(hv[1], hv[3]);
      const u32 gi = ((lane >> 3) & 1) * 32 + 4 * (lane & 7) + 2 * ((lane >> 4) & 1) + (lane >> 5);
      __builtin_amdgcn_raw_buffer_store_b128(u32x4{f2u(h0), tA, f2u(h1), tB}, rsWS, (int)(WS_H + ((fu * 8 + fv) * 64 + gi) * 16), 0, AUX_SC1);
    }
    STAMP(2);
      lds_wait((LAS unsigned char*)(lds + L_CNT), 8u);
      const f32x2 ml = *(const f32x2*)(lds + L_ML + (lane & 7) * 8);
      const float mg = wave_max(ml.x);
      const float lg = row16_sum(c < 8 ? ml.y * __builtin_amdgcn_exp2f(ml.x - mg) : 0.f);
      __builtin_amdgcn_raw_buffer_store_b128(u32x4{f2u(mg), tA, f2u(lg), tB}, rsWS, (int)(lane == 0 ? WS_ML + prod * 16 : OOB), 0, AUX_SC1);
      STAMP(3);
    if (is_ffn) {
      u32x4 W2[11];
      const u32 B1v = bld32<W_AUX>(rsB1, (lane & 31) * 4, fu * 128);
      const int rg = lane / 10, ch = lane - 10 * rg;
#pragma unroll
      for (int k = 0; k < 11; ++k) W2[k] = bld128<W_AUX>(rsW2, (lane < 60 && 6 * k + rg < 64) ? rg * (DM * 2) + ch * 16 : OOB, (64 * fu + 6 * k) * (DM * 2) + fv * 160);
      SB();
      const u32 offH = WS_H + (fu * 8 * 64 + lane) * 16;
      u32x4 g[8];
#pragma unroll
      for (int k = 0; k < 8; ++k) g[k] = bld128<AUX_SC1>(rsWS, offH, k * 64 * 16);
      wait_granules<8>(g, rsWS, offH, 64 * 16, tA, tB);
      float s0 = 0.f, s1 = 0.f;
#pragma unroll
      for (int k = 0; k < 8; ++k) { s0 += u2f(g[k].x); s1 += u2f(g[k].z); }
      s0 = rbf(rbf(s0) + bflo(B1v)); s1 = rbf(rbf(s1) + bfhi(B1v));
      s0 = s0 > 0.f ? s0 : 0.f; s1 = s1 > 0.f ? s1 : 0.f;
      *(f32x2*)(lds + L_H2 + lane * 8) = f32x2{rbf(s0 * s0), rbf(s1 * s1)};
      float ov[16];
#pragma unroll
      for (int k = 0; k < 16; ++k) ov[k] = 0.f;
#pragma unroll
      for (int k = 0; k < 11; ++k) {
        const int r = (6 * k + rg) & 63;
        const float a0 = *(const float*)(lds + L_H2 + r * 4), a1 = *(const float*)(lds + L_H2 + 256 + r * 4);
#pragma unroll
        for (int u = 0; u < 4; ++u) {
          const float lo = bflo(W2[k][u]), hi = bfhi(W2[k][u]);
          ov[2 * u] = fmaf(a0, lo, ov[2 * u]); ov[2 * u + 1] = fmaf(a0, hi, ov[2 * u + 1]);
          ov[8 + 2 * u] = fmaf(a1, lo, ov[8 + 2 * u]); ov[8 + 2 * u + 1] = fmaf(a1, hi, ov[8 + 2 * u + 1]);
        }
      }
      unsigned char* fr = lds + (lane < 60 ? L_FR + lane * 64 : L_TRASH + (lane & 3) * 64);
#pragma unroll
      for (int k = 0; k < 4; ++k) *(f32x4*)(fr + 16 * k) = f32x4{ov[4 * k], ov[4 * k + 1], ov[4 * k + 2], ov[4 * k + 3]};
#pragma unroll
      for (int j = 0; j < 2; ++j) {
        const int gq = lane + 64 * j, gb = gq >= 40 ? 1 : 0, cp = (gq - 40 * gb) % 40;
        f32x2 t = {0.f, 0.f};
#pragma unroll
        for (int k = 0; k < 6; ++k) t += *(const f32x2*)(lds + L_FR + (k * 10 + (cp >> 2)) * 64 + (gb * 8 + 2 * (cp & 3)) * 4);
        __builtin_amdgcn_raw_buffer_store_b128(u32x4{f2u(t.x), tA, f2u(t.y), tB}, rsWS, (int)(gq < 80 ? WS_F + ((fu * 8 + fv) * 80 + gq) * 16 : OOB), 0, AUX_SC1);
      }
    }
    {
      const u32 offML = WS_ML + (pair * 4 + (lane & 3)) * 16;
      u32x4 g = bld128<AUX_SC1>(rsWS, offML, 0);
      g = wait_granule(g, rsWS, offML, tA, tB, true);
      const float mv = u2f(g.x), M = row16_max(mv);
      const float L = row16_sum(c < 4 ? u2f(g.z) * __builtin_amdgcn_exp2f(mv - M) : 0.f);
      *(float*)(lds + (lane < 16 ? L_FW : L_TRASH) + lane * 4) = __builtin_amdgcn_exp2f(ml.x - M) / L;
    }
    STAMP(4);
    float gate = 0.f;
    if (is_cons) {
      lds_wait((LAS unsigned char*)(lds + L_CNT + 8), 1u);
      u32x4 FG[2]; u32 offF[2];
      const int cv = coct / 10, cp0 = (coct - 10 * cv) * 4;
#pragma unroll
      for (int k = 0; k < 2; ++k) {
        const int idx = lane + 64 * k;
        offF[k] = idx < 108 ? WS_F + (((idx >> 2) * 8 + cv) * 80 + cb * 40 + cp0 + (idx & 3)) * 16 : OOB;
        FG[k] = bld128<AUX_SC1>(rsWS, offF[k], 0);
      }
      FG[0] = wait_granule(FG[0], rsWS, offF[0], tA, tB, true);
      FG[1] = wait_granule(FG[1], rsWS, offF[1], tA, tB, lane < 44);
      float f0 = u2f(FG[0].x) + (lane < 44 ? u2f(FG[1].x) : 0.f), f1 = u2f(FG[0].z) + (lane < 44 ? u2f(FG[1].z) : 0.f);
      f0 += dpp<ROR4>(f0); f1 += dpp<ROR4>(f1); f0 += dpp<ROR8>(f0); f1 += dpp<ROR8>(f1);
      f0 = rows_sum(f0); f1 = rows_sum(f1);
      *(f32x2*)(lds + (lane < 4 ? L_FP : L_TRASH) + lane * 8) = f32x2{f0, f1};
      const float fs = *(const float*)(lds + L_FP + (lane & 7) * 4);
      const float o = rbf(rbf(fs) + bflo(B2v));
      gate = rbf(o / (1.f + __expf(-o)));
    }
    STAMP(5);
    __syncthreads();
    if (!is_cons) return;
    u32x4 og;
    const u32 offO = WS_Y + ((128 * (cb ^ 1) + lane) * 320) * 16;
    __syncthreads();
    {
      float ys = 0.f;
#pragma unroll
      for (int k = 0; k < 8; ++k) ys += *(const float*)(lds + L_YP + (k * 8 + (lane & 7)) * 4);
      const float res = bflo(XV) + rbf(rbf(ys) * gate);
      if (lane < 8) ((GAS u16*)p.out)[cb * DM + 8 * coct + lane] = (u16)(pkbf(res, 0.f) & 0xffffu);
    }
    STAMP(8);
    if (bid == CONS0) {
      og = bld128<AUX_SC1>(rsWS, offO, 0);
      og = wait_granule(og, rsWS, offO, tA, tB, true);
      og = bld128<AUX_SC1>(rsWS, offO + 64 * 320 * 16, 0);
      og = wait_granule(og, rsWS, offO + 64 * 320 * 16, tA, tB, true);
      if (lane == 0) __hip_atomic_store((GAS u64*)p.ws, (u64)tA | ((u64)tB << 32), __ATOMIC_RELAXED, __HIP_MEMORY_SCOPE_AGENT);
    }
    return;
  }

  const u32 vKV = w * 4096 + lane * 16;
  u32x4 S[128];
  auto ldS = [&](int i) INL -> u32x4 { const int t = i & 63; return i < 64 ? bld128<K_AUX>(rsK, vKV + (t & 3) * 1024, (t >> 2) * 32768) : bld128<V_AUX>(rsV, vKV + (t & 3) * 1024, (t >> 2) * 32768); };
  sfor<0, PACE>([&](auto I) INL { S[decltype(I)::value] = ldS(decltype(I)::value); });
  SB();
  __syncthreads();
  sfor<PACE, N0>([&](auto I) INL {
    constexpr int n = decltype(I)::value;
    asm volatile("" :: "v"(S[n - PACE][0]));
    SB();
    S[n] = ldS(n);
    SB();
    if constexpr (n == 6) STAMP(9);
    if constexpr (n == 10) STAMP(10);
    if constexpr (n == 14) STAMP(11);
  });
  STAMP(6);
  __syncthreads();
  u32 q[4];
  { const u32x4 qq = *(const u32x4*)(lds + L_Q + c * 16); q[0] = qq[0]; q[1] = qq[1]; q[2] = qq[2]; q[3] = qq[3]; }
  STAMP(2);

  float sc[4], d0 = 0.f, m1a = 0.f, m2a = 0.f, m3a = 0.f, m1 = 0.f, m2 = 0.f;
  sfor<0, 64>([&](auto I) INL {
    constexpr int i = decltype(I)::value;
    float d = dot2(S[i][0], q[0], 0.f);
    asm volatile("" : "+v"(d));
    SB();
    sfor<(i == 0 ? N0 : target(i - 1)), target(i)>([&](auto N) INL { S[decltype(N)::value] = ldS(decltype(N)::value); });
    SB();
    d = dot2(S[i][1], q[1], d); d = dot2(S[i][2], q[2], d); d = dot2(S[i][3], q[3], d);
    asm volatile("" : "+v"(d));
    constexpr int t = i & 15;
    if ((t & 1) == 0) d0 = d;
    else {
      const float a1 = merge<0>(d0, d, c);
      if ((t & 2) == 0) m1a = a1;
      else {
        m1 = merge<1>(m1a, a1, c);
        if ((t & 4) == 0) m2a = m1;
        else {
          m2 = merge<2>(m2a, m1, c);
          if ((t & 8) == 0) m3a = m2;
          else sc[i >> 4] = merge<3>(m3a, m2, c);
        }
      }
    }
    SB();
  });
  STAMP(3);
  constexpr float C2 = 0.08838834764831845f * 1.4426950408889634f;
  float pr[4];
  {
#pragma unroll
    for (int j = 0; j < 4; ++j) sc[j] *= C2;
    const float mw = wave_max(fmaxf(fmaxf(sc[0], sc[1]), fmaxf(sc[2], sc[3])));
#pragma unroll
    for (int j = 0; j < 4; ++j) pr[j] = __builtin_amdgcn_exp2f(sc[j] - mw);
    const float lw = wave_sum((pr[0] + pr[1]) + (pr[2] + pr[3]));
    *(f32x2*)(lds + (lane == 0 ? L_ML + w * 8 : L_TRASH + lane * 8)) = f32x2{mw, lw};
    asm volatile("s_waitcnt lgkmcnt(0)" ::: "memory");
    if (lane == 0) __hip_atomic_fetch_add((LAS u32*)(LAS unsigned char*)(lds + L_CNT), 1u, __ATOMIC_RELAXED, __HIP_MEMORY_SCOPE_WORKGROUP);
  }
  SB();

  float acc[8];
#pragma unroll
  for (int k = 0; k < 8; ++k) acc[k] = 0.f;
  u32x4 WO[5], BF[4][5];
  u32 vWO[5];
#pragma unroll
  for (int k = 0; k < 5; ++k) { const int it = lane + 64 * k; vWO[k] = (it / 10) * (DM * 2) + (it % 10) * 16 + w * 160; }
  sfor<64, 128>([&](auto I) INL {
    constexpr int i = decltype(I)::value;
    constexpr int vi = i - 64, t = vi & 15;
    const u32x4 V = S[i];
    float v0 = bflo(V[0]);
    asm volatile("" : "+v"(v0));
    SB();
    sfor<target(i - 1), target(i)>([&](auto N) INL { S[decltype(N)::value] = ldS(decltype(N)::value); });
    if constexpr (vi == VSIG) { if (tid == 0) __hip_atomic_fetch_add((LAS u32*)(LAS unsigned char*)(lds + L_CNT + 8), 1u, __ATOMIC_RELAXED, __HIP_MEMORY_SCOPE_WORKGROUP); }
    if constexpr (vi >= 2 && vi < 50 && (vi - 2) % 12 < 10 && (vi - 2) % 2 == 0) { const int rho = (vi - 2) / 12, k = ((vi - 2) % 12) / 2;
 WO[k] = bld128<W_AUX>(rsWO, vWO[k], rho * 32 * DM * 2);
 }
    SB();
    {
      const float pb = bcast<t>(pr[vi >> 4]);
      acc[0] = fmaf(pb, v0, acc[0]); acc[1] = fmaf(pb, bfhi(V[0]), acc[1]);
#pragma unroll
      for (int u = 1; u < 4; ++u) { acc[2 * u] = fmaf(pb, bflo(V[u]), acc[2 * u]); acc[2 * u + 1] = fmaf(pb, bfhi(V[u]), acc[2 * u + 1]); }
      asm volatile("" : "+v"(acc[0]), "+v"(acc[1]), "+v"(acc[2]), "+v"(acc[3]), "+v"(acc[4]), "+v"(acc[5]), "+v"(acc[6]), "+v"(acc[7]));
    }
    if constexpr (vi >= 13 && vi <= 49 && (vi - 13) % 12 == 0) {
      unsigned char* st = lds + L_WO + w * 5120;
#pragma unroll
      for (int k = 0; k < 5; ++k) *(u32x4*)(st + (lane + 64 * k) * 16) = WO[k];
    }
    if constexpr (vi >= 14 && vi <= 50 && (vi - 14) % 12 == 0) {
      constexpr int rho = (vi - 14) / 12;
      LAS unsigned char* st = (LAS unsigned char*)(lds + L_WO + w * 5120);
#pragma unroll
      for (int n = 0; n < 5; ++n) {
        LAS unsigned char* a = st + (8 * g4 + (c >> 2)) * 160 + 32 * n + 8 * (c & 3);
        const u32x2 lo = __builtin_bit_cast(u32x2, __builtin_amdgcn_ds_read_tr16_b64_v4i16((LAS s16x4*)a));
        const u32x2 hi = __builtin_bit_cast(u32x2, __builtin_amdgcn_ds_read_tr16_b64_v4i16((LAS s16x4*)(a + 4 * 160)));
        BF[rho][n] = u32x4{lo.x, lo.y, hi.x, hi.y};
      }
    }
    if constexpr (vi >= 16 && vi <= 52 && (vi - 16) % 12 == 0) {
      constexpr int rho = (vi - 16) / 12;
#pragma unroll
      for (int n = 0; n < 5; ++n) asm volatile("" : "+v"(BF[rho][n]));
    }
    SB();
  });
  STAMP(4);

  {
    const float z0 = tr16(tr32(acc[0], acc[4]), tr32(acc[2], acc[6])), z1 = tr16(tr32(acc[1], acc[5]), tr32(acc[3], acc[7]));
    const u32 hi = pkbf(z0, z1), lo = pkbf(z0 - bflo(hi), z1 - bfhi(hi));
    *(u32*)(lds + L_OA + w * 272 + 16 * c + 4 * g4) = hi;
    *(u32*)(lds + L_OA + (8 + w) * 272 + 16 * c + 4 * g4) = lo;
  }
  __syncthreads();
  const u32x2 tg = *(const u32x2*)(lds + L_TAG);
  const u32 tA = tg.x, tB = tg.y;
  {
    f32x4 D[5];
#pragma unroll
    for (int n = 0; n < 5; ++n) D[n] = f32x4{0.f, 0.f, 0.f, 0.f};
    const f32x4 fr = *(const f32x4*)(lds + L_FW + 16 * g4);
#pragma unroll
    for (int rho = 0; rho < 4; ++rho) {
      const u32x4 A = *(const u32x4*)(lds + L_OA + c * 272 + 64 * rho + 16 * g4);
#pragma unroll
      for (int n = 0; n < 5; ++n) D[n] = __builtin_amdgcn_mfma_f32_16x16x32_bf16(__builtin_bit_cast(bf16x8, A), __builtin_bit_cast(bf16x8, BF[rho][n]), D[n], 0, 0, 0);
    }
    float e[5];
#pragma unroll
    for (int n = 0; n < 5; ++n) e[n] = fmaf(D[n][0], fr[0], D[n][1] * fr[1]) + fmaf(D[n][2], fr[2], D[n][3] * fr[3]);
    const float ya = tr16(tr32(e[0], e[2]), tr32(e[1], e[3]));
    const float yb = rows_sum(e[4]);
    const float yan = dpp<XOR1>(ya), ybn = dpp<XOR1>(yb);
    const bool odd = c & 1;
    const u32 cp = odd ? 40 * w + 32 + (c >> 1) : 40 * w + 8 * g4 + (c >> 1);
    const u32 off = (!odd || g4 == 0) ? WS_Y + (prod * 320 + cp) * 16 : OOB;
    const u32x4 gr = odd ? u32x4{f2u(ybn), tA, f2u(yb), tB} : u32x4{f2u(ya), tA, f2u(yan), tB};
    __builtin_amdgcn_raw_buffer_store_b128(gr, rsWS, (int)off, 0, AUX_SC1);
  }
  STAMP(5);
  if (!is_cons) return;

  const u32 offY = WS_Y + ((cb * 128 + (tid >> 2)) * 320 + 4 * coct + (tid & 3)) * 16;
  u32x4 YG = bld128<AUX_SC1>(rsWS, offY, 0);
  YG = wait_granule<Y_SLEEP>(YG, rsWS, offY, tA, tB, true);
  {
    float y0 = u2f(YG.x), y1 = u2f(YG.z);
    y0 += dpp<ROR4>(y0); y1 += dpp<ROR4>(y1); y0 += dpp<ROR8>(y0); y1 += dpp<ROR8>(y1);
    y0 = rows_sum(y0); y1 = rows_sum(y1);
    *(f32x2*)(lds + (lane < 4 ? L_YP + w * 32 : L_TRASH) + lane * 8) = f32x2{y0, y1};
  }
  __syncthreads();
  STAMP(8);
}

}

extern "C" void kernel_launch(void* const* d_in, const int* in_sizes, int n_in, void* d_out, int out_size, void* d_ws, size_t ws_size, hipStream_t stream) {
  (void)in_sizes; (void)n_in; (void)out_size; (void)ws_size;
  Params p;
  p.x = d_in[0]; p.k = d_in[1]; p.v = d_in[2]; p.wq = d_in[3]; p.wo = d_in[4];
  p.w1 = d_in[6]; p.b1 = d_in[7]; p.w2 = d_in[8]; p.b2 = d_in[9]; p.out = d_out;
  p.ws = d_ws;
  hipLaunchKernelGGL(fused_block, dim3(NWG), dim3(NTH), 0, stream, p);
}
```

```cpp
#define K_N0 18 Q_DELAY
#define WS_AT_END 1
#include <hip/hip_runtime.h>
#include <hip/hip_bf16.h>
#include <stdint.h>

namespace {

typedef unsigned u32;
typedef unsigned long long u64;
typedef unsigned short u16;
typedef u32 u32x4 __attribute__((ext_vector_type(4)));
typedef u32 u32x2 __attribute__((ext_vector_type(2)));
typedef float f32x4 __attribute__((ext_vector_type(4)));
typedef float f32x2 __attribute__((ext_vector_type(2)));
typedef short s16x4 __attribute__((ext_vector_type(4)));
typedef __bf16 bf16x2 __attribute__((ext_vector_type(2)));
typedef __bf16 bf16x8 __attribute__((ext_vector_type(8)));
typedef __amdgpu_buffer_rsrc_t rsrc_t;
#define GAS __attribute__((address_space(1)))
#define LAS __attribute__((address_space(3)))

constexpr int DM = 640, NH = 32, SK = 8192, DH = 128, FF = 1728;
constexpr int NWG = 256, NTH = 768;
constexpr int NFFN = FF / 8;
constexpr int CONS0 = 96;
constexpr u32 WS_E = 0;
constexpr u32 WS_ML = 4096;
constexpr u32 WS_Q = 8192;
constexpr u32 WS_H = WS_Q + NH * 8 * 2 * 64 * 16;
constexpr u32 WS_F = WS_H + NFFN * 64 * 16;
constexpr u32 WS_Y = WS_F + NFFN * 80 * 16;
constexpr u32 WS_END = WS_Y + NWG * 320 * 16;
constexpr u32 OOB = 0x7ffffff0u;
constexpr int AUX_NT = 2, AUX_SC1 = 16;
constexpr u32 SPIN_CAP = 1u << 16;

#define WQ_AUX 2
#define W_AUX 2
#define X_AUX 0
#define K_AUX 2
#define V_AUX 2
#define Y_SLEEP 2
#define VSIG 40
#define POLL_SLEEP 1
#define Q_DELAY 0
#define K_PRE 3
#define K_A 5
#define PACE 3
#define RING 3
#define BUMP 3
#define RING_END RING
#define RAMP_AT 96
#define RAMP_STEP 2
constexpr int N0 = K_N0, R = RING;
constexpr int target(int i) {
  int t = i + 1 + R;
  const int top = N0 + (i + 1 < R ? i + 1 : R);
  if (t < top) t = top;
  if (i >= 64 - BUMP && i < 64) t = i + 1 + R + (i - (64 - BUMP) + 1);
  if (i >= 64 && t < 64 + R + BUMP) t = 64 + R + BUMP;
  if (i >= RAMP_AT) { int rr = R + (i - RAMP_AT) / RAMP_STEP + 1; if (rr > RING_END) rr = RING_END; if (t < i + 1 + rr) t = i + 1 + rr; }
  return t > 128 ? 128 : t;
}

struct Params {
  const void* x; const void* k; const void* v; const void* wq; const void* wo;
  const void* w1; const void* b1; const void* w2; const void* b2; void* out; void* ws;
};

__device__ __forceinline__ rsrc_t mk_rsrc(const void* p, u32 bytes) { return __builtin_amdgcn_make_buffer_rsrc((void*)p, 0, (int)bytes, 0x00020000); }
template <int AUX> __device__ __forceinline__ u32x4 bld128(rsrc_t r, u32 voff, u32 soff) { return __builtin_amdgcn_raw_buffer_load_b128(r, (int)voff, (int)soff, AUX); }
template <int AUX> __device__ __forceinline__ u32 bld32(rsrc_t r, u32 voff, u32 soff) { return __builtin_amdgcn_raw_buffer_load_b32(r, (int)voff, (int)soff, AUX); }
__device__ __forceinline__ u32 bld16(rsrc_t r, u32 voff) { return (u32)__builtin_amdgcn_raw_buffer_load_b16(r, (int)voff, 0, 2); }
__device__ __forceinline__ float bflo(u32 x) { return __builtin_bit_cast(float, x << 16); }
__device__ __forceinline__ float bfhi(u32 x) { return __builtin_bit_cast(float, x & 0xffff0000u); }
__device__ __forceinline__ u32 pkbf(float lo, float hi) { bf16x2 v = {(__bf16)lo, (__bf16)hi}; return __builtin_bit_cast(u32, v); }
__device__ __forceinline__ float rbf(float x) { return bflo(pkbf(x, 0.f) & 0xffffu); }
__device__ __forceinline__ float dot2(u32 a, u32 b, float c) { return __builtin_amdgcn_fdot2_f32_bf16(__builtin_bit_cast(bf16x2, a), __builtin_bit_cast(bf16x2, b), c, false); }
template <int CTRL> __device__ __forceinline__ float dpp(float x) { return __builtin_bit_cast(float, __builtin_amdgcn_mov_dpp(__builtin_bit_cast(int, x), CTRL, 0xf, 0xf, true)); }
constexpr int XOR1 = 0xB1, XOR2 = 0x4E, ROR4 = 0x124, ROR8 = 0x128, ROR2 = 0x122, ROR1 = 0x121, BCAST = 0x150;
__device__ __forceinline__ float u2f(u32 x) { return __builtin_bit_cast(float, x); }
__device__ __forceinline__ u32 f2u(float x) { return __builtin_bit_cast(u32, x); }
__device__ __forceinline__ float tr32(float a, float b) { auto s = __builtin_amdgcn_permlane32_swap(f2u(a), f2u(b), false, false); return u2f(s[0]) + u2f(s[1]); }
__device__ __forceinline__ float tr16(float a, float b) { auto s = __builtin_amdgcn_permlane16_swap(f2u(a), f2u(b), false, false); return u2f(s[0]) + u2f(s[1]); }
__device__ __forceinline__ float rows_sum(float x) { x = tr16(x, x); return tr32(x, x); }
__device__ __forceinline__ float rows_max(float x) {
  auto s = __builtin_amdgcn_permlane16_swap(f2u(x), f2u(x), false, false); x = fmaxf(u2f(s[0]), u2f(s[1]));
  auto t = __builtin_amdgcn_permlane32_swap(f2u(x), f2u(x), false, false); return fmaxf(u2f(t[0]), u2f(t[1]));
}
__device__ __forceinline__ float row16_sum(float x) { x += dpp<ROR8>(x); x += dpp<ROR4>(x); x += dpp<ROR2>(x); x += dpp<ROR1>(x); return x; }
__device__ __forceinline__ float row16_max(float x) { x = fmaxf(x, dpp<ROR8>(x)); x = fmaxf(x, dpp<ROR4>(x)); x = fmaxf(x, dpp<ROR2>(x)); x = fmaxf(x, dpp<ROR1>(x)); return x; }
__device__ __forceinline__ float wave_sum(float x) { return rows_sum(row16_sum(x)); }
__device__ __forceinline__ float wave_max(float x) { return rows_max(row16_max(x)); }
template <int K> __device__ __forceinline__ float merge(float lo, float hi, int c) {
  const bool bit = (c >> K) & 1;
  const float keep = bit ? hi : lo, send = bit ? lo : hi;
  constexpr int CTRL = K == 0 ? XOR1 : K == 1 ? XOR2 : K == 2 ? ROR4 : ROR8;
  return keep + dpp<CTRL>(send);
}
template <int T> __device__ __forceinline__ float bcast(float x) { return dpp<BCAST + T>(x); }
#define SB() __builtin_amdgcn_sched_barrier(0)
template <int I> struct IC { static constexpr int value = I; };
template <int B, int E, class F> __device__ __forceinline__ void sfor(F&& f) { if constexpr (B < E) { f(IC<B>{}); sfor<B + 1, E>(f); } }
#define INL __attribute__((always_inline))

__device__ __forceinline__ bool tags_ok(const u32x4& g, u32 tA, u32 tB) { return g.y == tA && g.w == tB; }
template <int SLEEP = 2> __device__ __forceinline__ u32x4 wait_granule(u32x4 g, rsrc_t rs, u32 off, u32 tA, u32 tB, bool need) {
  bool ok = !need || tags_ok(g, tA, tB);
  if (!__all(ok)) {
#pragma unroll 1
    for (u32 spin = 0; spin < SPIN_CAP; ++spin) {
      if (!ok) { g = bld128<AUX_SC1>(rs, off, 0); ok = tags_ok(g, tA, tB); }
      if (__all(ok)) break;
      __builtin_amdgcn_s_sleep(SLEEP);
    }
  }
  return g;
}

template <int N> __device__ __forceinline__ void wait_granules(u32x4 (&g)[N], rsrc_t rs, u32 off, u32 stride, u32 tA, u32 tB) {
  bool ok = true;
#pragma unroll
  for (int k = 0; k < N; ++k) ok = ok && tags_ok(g[k], tA, tB);
  if (!__all(ok)) {
#pragma unroll 1
    for (u32 spin = 0; spin < SPIN_CAP; ++spin) {
#pragma unroll
      for (int k = 0; k < N; ++k) if (!tags_ok(g[k], tA, tB)) g[k] = bld128<AUX_SC1>(rs, off, k * stride);
      ok = true;
#pragma unroll
      for (int k = 0; k < N; ++k) ok = ok && tags_ok(g[k], tA, tB);
      if (__all(ok)) break;
      __builtin_amdgcn_s_sleep(POLL_SLEEP);
    }
  }
}
__device__ __forceinline__ void lds_wait(LAS unsigned char* word, u32 want) {
#pragma unroll 1
  for (u32 spin = 0; spin < (SPIN_CAP << 4); ++spin) {
    if (*(volatile LAS u32*)word >= want) break;
    __builtin_amdgcn_s_sleep(2);
  }
  asm volatile("" ::: "memory");
}

constexpr int L_XN = 0;
constexpr int L_QP = 2560;
constexpr int L_Q = L_QP + 4096;
constexpr int L_ML = L_Q + 256;
constexpr int L_CNT = L_ML + 64;
constexpr int L_TAG = L_CNT + 16;
constexpr int L_FW = L_CNT + 32;
constexpr int L_FP = L_FW + 64;
constexpr int L_YP = L_FP + 32;
constexpr int L_H2 = L_YP + 256;
constexpr int L_FR = L_H2 + 512;
constexpr int L_OA = L_FR + 3840;
constexpr int L_WO = L_OA + 16 * 272;
constexpr int L_TRASH = L_WO + 8 * 5120;
constexpr int L_END = L_TRASH + 1024;
static_assert(L_OA % 16 == 0 && L_WO % 16 == 0 && L_FW % 16 == 0 && L_FR % 16 == 0 && L_QP % 16 == 0, "LDS alignment");

#define STAMP(n) do {} while (0)

__global__ void __launch_bounds__(NTH, 3) fused_block(Params p) {
  __shared__ __attribute__((aligned(16))) unsigned char lds[L_END];
  const int tid = threadIdx.x, lane = tid & 63, w = __builtin_amdgcn_readfirstlane(tid >> 6), g4 = lane >> 4, c = lane & 15;
  const int bid = blockIdx.x, xc = bid & 7, rr = bid >> 3;
#define HMAP 0
  const int h = HMAP == 0 ? (rr >> 3) * 8 + xc : HMAP == 1 ? (rr >> 3) * 8 + ((xc + 2 * (rr >> 3)) & 7) : (rr >> 3) * 8 + ((xc + 3 * (rr >> 3)) & 7), b = (rr >> 2) & 1, s = rr & 3;
  const int pair = b * NH + h, prod = pair * 4 + s;
  const int fu = xc + 8 * (rr >> 3), fv = rr & 7;
  const bool is_ffn = fu < 27;
  const bool is_cons = bid >= CONS0;
  const int cj = bid - CONS0, cb = is_cons ? cj / 80 : 0, coct = is_cons ? cj % 80 : 0;
  STAMP(0);

  const rsrc_t rsWS = mk_rsrc(p.ws, WS_END);
  const rsrc_t rsK = mk_rsrc((const char*)p.k + (size_t)pair * (SK * DH * 2) + (size_t)s * (2048 * DH * 2), 2048 * DH * 2);
  const rsrc_t rsV = mk_rsrc((const char*)p.v + (size_t)pair * (SK * DH * 2) + (size_t)s * (2048 * DH * 2), 2048 * DH * 2);
  const rsrc_t rsX = mk_rsrc(p.x, 2 * DM * 2);
  const rsrc_t rsWQ = mk_rsrc(p.wq, DM * NH * DH * 2);
  const rsrc_t rsWO = mk_rsrc((const char*)p.wo + (size_t)h * (DH * DM * 2), DH * DM * 2);
  const rsrc_t rsW1 = mk_rsrc(p.w1, is_ffn ? DM * FF * 2 : 0);
  const rsrc_t rsW2 = mk_rsrc(p.w2, is_ffn ? FF * DM * 2 : 0);
  const rsrc_t rsB1 = mk_rsrc(p.b1, is_ffn ? FF * 2 : 0);
  const rsrc_t rsB2 = mk_rsrc(p.b2, DM * 2);

  auto ln_stats = [&](const u32x4& ra, u32 rb, float& mb, float& inv) INL {
    float v[10];
#pragma unroll
    for (int k = 0; k < 4; ++k) { v[2 * k] = bflo(ra[k]); v[2 * k + 1] = bfhi(ra[k]); }
    v[8] = bflo(rb); v[9] = bfhi(rb);
    float sum = 0.f;
#pragma unroll
    for (int k = 0; k < 10; ++k) sum += v[k];
    const float mean = wave_sum(sum) * (1.f / DM);
    float sq = 0.f;
#pragma unroll
    for (int k = 0; k < 10; ++k) { const float d = v[k] - mean; sq = fmaf(d, d, sq); }
    const float var = wave_sum(sq) * (1.f / DM);
    mb = rbf(mean);
    inv = 1.f / rbf(sqrtf(rbf(rbf(var) + 1e-5f)));
  };
  auto ln_row = [&](const u32x4& ra, u32 rb, float mb, float inv, int dst) INL {
#pragma unroll
    for (int k = 0; k < 5; ++k) {
      const u32 r = k < 4 ? ra[k & 3] : rb;
      const u32 d = pkbf(bflo(r) - mb, bfhi(r) - mb);
      *(u32*)(lds + dst + lane * 20 + 4 * k) = pkbf(bflo(d) * inv, bfhi(d) * inv);
    }
  };

  if (w >= 8) {
    const int hw = w - 8;
    if (hw == 0) { *(volatile LAS u32*)(LAS unsigned char*)(lds + L_CNT) = 0u; *(volatile LAS u32*)(LAS unsigned char*)(lds + L_CNT + 4) = 0u; *(volatile LAS u32*)(LAS unsigned char*)(lds + L_CNT + 8) = 0u; }
    __syncthreads();
    const u64 E = __hip_atomic_load((GAS u64*)p.ws, __ATOMIC_RELAXED, __HIP_MEMORY_SCOPE_AGENT);
    const u32x4 x0a = bld128<X_AUX>(rsX, lane * 20, 0), x1a = bld128<X_AUX>(rsX, 1280 + lane * 20, 0);
    const u32 x0b = bld32<X_AUX>(rsX, lane * 20 + 16, 0), x1b = bld32<X_AUX>(rsX, 1280 + lane * 20 + 16, 0);
    const int qrow = 80 * (4 * b + s) + 20 * hw;
    u32x4 WQ[5]; u32 xr0[5], xr1[5];
#pragma unroll
    for (int k = 0; k < 5; ++k) WQ[k] = bld128<WQ_AUX>(rsWQ, g4 * 8192 + c * 16, (qrow + 4 * k) * 8192 + h * 256);
#pragma unroll
    for (int k = 0; k < 5; ++k) { xr0[k] = __builtin_amdgcn_raw_buffer_load_b16(rsX, (qrow + 4 * k + g4) * 2, 0, X_AUX); xr1[k] = __builtin_amdgcn_raw_buffer_load_b16(rsX, 1280 + (qrow + 4 * k + g4) * 2, 0, X_AUX); }
    float mb0, inv0, mb1, inv1;
    ln_stats(x0a, x0b, mb0, inv0);
    ln_stats(x1a, x1b, mb1, inv1);
    STAMP(9);
    {
      float a[16];
#pragma unroll
      for (int k = 0; k < 16; ++k) a[k] = 0.f;
#pragma unroll
      for (int k = 0; k < 5; ++k) {
        const float x0 = rbf(rbf(bflo(xr0[k]) - mb0) * inv0), x1 = rbf(rbf(bflo(xr1[k]) - mb1) * inv1);
#pragma unroll
        for (int u = 0; u < 4; ++u) {
          const float lo = bflo(WQ[k][u]), hi = bfhi(WQ[k][u]);
          a[2 * u] = fmaf(x0, lo, a[2 * u]); a[2 * u + 1] = fmaf(x0, hi, a[2 * u + 1]);
          a[8 + 2 * u] = fmaf(x1, lo, a[8 + 2 * u]); a[8 + 2 * u + 1] = fmaf(x1, hi, a[8 + 2 * u + 1]);
        }
      }
#pragma unroll
      for (int e = 0; e < 8; ++e) a[e] = tr32(a[e], a[8 + e]);
      const f32x4 z = {tr16(a[0], a[4]), tr16(a[1], a[5]), tr16(a[2], a[6]), tr16(a[3], a[7])};
      *(f32x4*)(lds + L_QP + (hw * 256 + (g4 >> 1) * 128 + 8 * c + 4 * (g4 & 1)) * 4) = z;
      asm volatile("s_waitcnt lgkmcnt(0)" ::: "memory");
      if (lane == 0) __hip_atomic_fetch_add((LAS u32*)(LAS unsigned char*)(lds + L_CNT + 4), 1u, __ATOMIC_RELAXED, __HIP_MEMORY_SCOPE_WORKGROUP);
    }
    if (hw != 0) return;
    const u32 tA = (u32)E + 0x9E3779B9u, tB = (u32)(E >> 32) + 0x85EBCA6Bu;
    *(u32x2*)(lds + (lane == 0 ? L_TAG : L_TRASH + lane * 8)) = u32x2{tA, tB};
    lds_wait((LAS unsigned char*)(lds + L_CNT + 4), 4u);
    STAMP(7);
    {
      f32x2 t0 = {0.f, 0.f}, t1 = {0.f, 0.f};
#pragma unroll
      for (int k = 0; k < 4; ++k) { t0 += *(const f32x2*)(lds + L_QP + (k * 256 + 2 * lane) * 4); t1 += *(const f32x2*)(lds + L_QP + (k * 256 + 128 + 2 * lane) * 4); }
      const u32 offP = WS_Q + (((h * 8 + b * 4 + s) * 2) * 64 + lane) * 16;
      __builtin_amdgcn_raw_buffer_store_b128(u32x4{f2u(t0.x), tA, f2u(t0.y), tB}, rsWS, (int)offP, 0, AUX_SC1);
      __builtin_amdgcn_raw_buffer_store_b128(u32x4{f2u(t1.x), tA, f2u(t1.y), tB}, rsWS, (int)offP, 64 * 16, AUX_SC1);
      const u32 offQ = WS_Q + ((h * 8 * 2 + b) * 64 + lane) * 16;
      u32x4 g[8];
      if (Q_DELAY) __builtin_amdgcn_s_sleep(Q_DELAY);
#pragma unroll
      for (int k = 0; k < 8; ++k) g[k] = bld128<AUX_SC1>(rsWS, offQ, k * 2 * 64 * 16);
      STAMP(6);
      wait_granules<8>(g, rsWS, offQ, 2 * 64 * 16, tA, tB);
      float q0 = 0.f, q1 = 0.f;
#pragma unroll
      for (int k = 0; k < 8; ++k) { q0 += u2f(g[k].x); q1 += u2f(g[k].z); }
      *(u32*)(lds + L_Q + lane * 4) = pkbf(q0, q1);
    }
    STAMP(1);
    __syncthreads();
    const u32 B2v = bld16(rsB2, (8 * coct + (lane & 7)) * 2), XV = bld16(rsX, cb * 1280 + (8 * coct + (lane & 7)) * 2);
    ln_row(x0a, x0b, mb0, inv0, L_XN);
    ln_row(x1a, x1b, mb1, inv1, L_XN + 1280);
    if (is_ffn) {
      u32x4 W1[10];
#pragma unroll
      for (int k = 0; k < 10; ++k) W1[k] = bld128<W_AUX>(rsW1, (lane >> 3) * (FF * 2) + (lane & 7) * 16, (80 * fv + 8 * k) * (FF * 2) + fu * 128);
      SB();
      float hv[16];
#pragma unroll
      for (int k = 0; k < 16; ++k) hv[k] = 0.f;
#pragma unroll
      for (int k = 0; k < 10; ++k) {
        const int row = 80 * fv + 8 * k + (lane >> 3);
        const float x0 = bflo(*(const u16*)(lds + L_XN + row * 2)), x1 = bflo(*(const u16*)(lds + L_XN + 1280 + row * 2));
#pragma unroll
        for (int u = 0; u < 4; ++u) {
          const float lo = bflo(W1[k][u]), hi = bfhi(W1[k][u]);
          hv[2 * u] = fmaf(x0, lo, hv[2 * u]); hv[2 * u + 1] = fmaf(x0, hi, hv[2 * u + 1]);
          hv[8 + 2 * u] = fmaf(x1, lo, hv[8 + 2 * u]); hv[8 + 2 * u + 1] = fmaf(x1, hi, hv[8 + 2 * u + 1]);
        }
      }
      SB();
#pragma unroll
      for (int e = 0; e < 8; ++e) hv[e] = merge<3>(hv[e], hv[8 + e], c);
#pragma unroll
      for (int e = 0; e < 4; ++e) hv[e] = tr16(hv[e], hv[e + 4]);
      const float h0 = tr32(hv[0], hv[2]), h1 = tr32(hv[1], hv[3]);
      const u32 gi = ((lane >> 3) & 1) * 32 + 4 * (lane & 7) + 2 * ((lane >> 4) & 1) + (lane >> 5);
      __builtin_amdgcn_raw_buffer_store_b128(u32x4{f2u(h0), tA, f2u(h1), tB}, rsWS, (int)(WS_H + ((fu * 8 + fv) * 64 + gi) * 16), 0, AUX_SC1);
    }
    STAMP(2);
      lds_wait((LAS unsigned char*)(lds + L_CNT), 8u);
      const f32x2 ml = *(const f32x2*)(lds + L_ML + (lane & 7) * 8);
      const float mg = wave_max(ml.x);
      const float lg = row16_sum(c < 8 ? ml.y * __builtin_amdgcn_exp2f(ml.x - mg) : 0.f);
      __builtin_amdgcn_raw_buffer_store_b128(u32x4{f2u(mg), tA, f2u(lg), tB}, rsWS, (int)(lane == 0 ? WS_ML + prod * 16 : OOB), 0, AUX_SC1);
      STAMP(3);
    if (is_ffn) {
      u32x4 W2[11];
      const u32 B1v = bld32<W_AUX>(rsB1, (lane & 31) * 4, fu * 128);
      const int rg = lane / 10, ch = lane - 10 * rg;
#pragma unroll
      for (int k = 0; k < 11; ++k) W2[k] = bld128<W_AUX>(rsW2, (lane < 60 && 6 * k + rg < 64) ? rg * (DM * 2) + ch * 16 : OOB, (64 * fu + 6 * k) * (DM * 2) + fv * 160);
      SB();
      const u32 offH = WS_H + (fu * 8 * 64 + lane) * 16;
      u32x4 g[8];
#pragma unroll
      for (int k = 0; k < 8; ++k) g[k] = bld128<AUX_SC1>(rsWS, offH, k * 64 * 16);
      wait_granules<8>(g, rsWS, offH, 64 * 16, tA, tB);
      float s0 = 0.f, s1 = 0.f;
#pragma unroll
      for (int k = 0; k < 8; ++k) { s0 += u2f(g[k].x); s1 += u2f(g[k].z); }
      s0 = rbf(rbf(s0) + bflo(B1v)); s1 = rbf(rbf(s1) + bfhi(B1v));
      s0 = s0 > 0.f ? s0 : 0.f; s1 = s1 > 0.f ? s1 : 0.f;
      *(f32x2*)(lds + L_H2 + lane * 8) = f32x2{rbf(s0 * s0), rbf(s1 * s1)};
      float ov[16];
#pragma unroll
      for (int k = 0; k < 16; ++k) ov[k] = 0.f;
#pragma unroll
      for (int k = 0; k < 11; ++k) {
        const int r = (6 * k + rg) & 63;
        const float a0 = *(const float*)(lds + L_H2 + r * 4), a1 = *(const float*)(lds + L_H2 + 256 + r * 4);
#pragma unroll
        for (int u = 0; u < 4; ++u) {
          const float lo = bflo(W2[k][u]), hi = bfhi(W2[k][u]);
          ov[2 * u] = fmaf(a0, lo, ov[2 * u]); ov[2 * u + 1] = fmaf(a0, hi, ov[2 * u + 1]);
          ov[8 + 2 * u] = fmaf(a1, lo, ov[8 + 2 * u]); ov[8 + 2 * u + 1] = fmaf(a1, hi, ov[8 + 2 * u + 1]);
        }
      }
      unsigned char* fr = lds + (lane < 60 ? L_FR + lane * 64 : L_TRASH + (lane & 3) * 64);
#pragma unroll
      for (int k = 0; k < 4; ++k) *(f32x4*)(fr + 16 * k) = f32x4{ov[4 * k], ov[4 * k + 1], ov[4 * k + 2], ov[4 * k + 3]};
#pragma unroll
      for (int j = 0; j < 2; ++j) {
        const int gq = lane + 64 * j, gb = gq >= 40 ? 1 : 0, cp = (gq - 40 * gb) % 40;
        f32x2 t = {0.f, 0.f};
#pragma unroll
        for (int k = 0; k < 6; ++k) t += *(const f32x2*)(lds + L_FR + (k * 10 + (cp >> 2)) * 64 + (gb * 8 + 2 * (cp & 3)) * 4);
        __builtin_amdgcn_raw_buffer_store_b128(u32x4{f2u(t.x), tA, f2u(t.y), tB}, rsWS, (int)(gq < 80 ? WS_F + ((fu * 8 + fv) * 80 + gq) * 16 : OOB), 0, AUX_SC1);
      }
    }
    {
      const u32 offML = WS_ML + (pair * 4 + (lane & 3)) * 16;
      u32x4 g = bld128<AUX_SC1>(rsWS, offML, 0);
      g = wait_granule(g, rsWS, offML, tA, tB, true);
      const float mv = u2f(g.x), M = row16_max(mv);
      const float L = row16_sum(c < 4 ? u2f(g.z) * __builtin_amdgcn_exp2f(mv - M) : 0.f);
      *(float*)(lds + (lane < 16 ? L_FW : L_TRASH) + lane * 4) = __builtin_amdgcn_exp2f(ml.x - M) / L;
    }
    STAMP(4);
    float gate = 0.f;
    if (is_cons) {
      lds_wait((LAS unsigned char*)(lds + L_CNT + 8), 1u);
      u32x4 FG[2]; u32 offF[2];
      const int cv = coct / 10, cp0 = (coct - 10 * cv) * 4;
#pragma unroll
      for (int k = 0; k < 2; ++k) {
        const int idx = lane + 64 * k;
        offF[k] = idx < 108 ? WS_F + (((idx >> 2) * 8 + cv) * 80 + cb * 40 + cp0 + (idx & 3)) * 16 : OOB;
        FG[k] = bld128<AUX_SC1>(rsWS, offF[k], 0);
      }
      FG[0] = wait_granule(FG[0], rsWS, offF[0], tA, tB, true);
      FG[1] = wait_granule(FG[1], rsWS, offF[1], tA, tB, lane < 44);
      float f0 = u2f(FG[0].x) + (lane < 44 ? u2f(FG[1].x) : 0.f), f1 = u2f(FG[0].z) + (lane < 44 ? u2f(FG[1].z) : 0.f);
      f0 += dpp<ROR4>(f0); f1 += dpp<ROR4>(f1); f0 += dpp<ROR8>(f0); f1 += dpp<ROR8>(f1);
      f0 = rows_sum(f0); f1 = rows_sum(f1);
      *(f32x2*)(lds + (lane < 4 ? L_FP : L_TRASH) + lane * 8) = f32x2{f0, f1};
      const float fs = *(const float*)(lds + L_FP + (lane & 7) * 4);
      const float o = rbf(rbf(fs) + bflo(B2v));
      gate = rbf(o / (1.f + __expf(-o)));
    }
    STAMP(5);
    __syncthreads();
    if (!is_cons) return;
    u32x4 og;
    const u32 offO = WS_Y + ((128 * (cb ^ 1) + lane) * 320) * 16;
    __syncthreads();
    {
      float ys = 0.f;
#pragma unroll
      for (int k = 0; k < 8; ++k) ys += *(const float*)(lds + L_YP + (k * 8 + (lane & 7)) * 4);
      const float res = bflo(XV) + rbf(rbf(ys) * gate);
      if (lane < 8) ((GAS u16*)p.out)[cb * DM + 8 * coct + lane] = (u16)(pkbf(res, 0.f) & 0xffffu);
    }
    STAMP(8);
    if (bid == CONS0) {
      og = bld128<AUX_SC1>(rsWS, offO, 0);
      og = wait_granule(og, rsWS, offO, tA, tB, true);
      og = bld128<AUX_SC1>(rsWS, offO + 64 * 320 * 16, 0);
      og = wait_granule(og, rsWS, offO + 64 * 320 * 16, tA, tB, true);
      if (lane == 0) __hip_atomic_store((GAS u64*)p.ws, (u64)tA | ((u64)tB << 32), __ATOMIC_RELAXED, __HIP_MEMORY_SCOPE_AGENT);
    }
    return;
  }

  const u32 vKV = w * 4096 + lane * 16;
  u32x4 S[128];
  auto ldS = [&](int i) INL -> u32x4 { const int t = i & 63; return i < 64 ? bld128<K_AUX>(rsK, vKV + (t & 3) * 1024, (t >> 2) * 32768) : bld128<V_AUX>(rsV, vKV + (t & 3) * 1024, (t >> 2) * 32768); };
  sfor<0, PACE>([&](auto I) INL { S[decltype(I)::value] = ldS(decltype(I)::value); });
  SB();
  __syncthreads();
  sfor<PACE, N0>([&](auto I) INL {
    constexpr int n = decltype(I)::value;
    asm volatile("" :: "v"(S[n - PACE][0]));
    SB();
    S[n] = ldS(n);
    SB();
    if constexpr (n == 6) STAMP(9);
    if constexpr (n == 10) STAMP(10);
    if constexpr (n == 14) STAMP(11);
  });
  STAMP(6);
  __syncthreads();
  u32 q[4];
  { const u32x4 qq = *(const u32x4*)(lds + L_Q + c * 16); q[0] = qq[0]; q[1] = qq[1]; q[2] = qq[2]; q[3] = qq[3]; }
  STAMP(2);

  float sc[4], d0 = 0.f, m1a = 0.f, m2a = 0.f, m3a = 0.f, m1 = 0.f, m2 = 0.f;
  sfor<0, 64>([&](auto I) INL {
    constexpr int i = decltype(I)::value;
    float d = dot2(S[i][0], q[0], 0.f);
    asm volatile("" : "+v"(d));
    SB();
    sfor<(i == 0 ? N0 : target(i - 1)), target(i)>([&](auto N) INL { S[decltype(N)::value] = ldS(decltype(N)::value); });
    SB();
    d = dot2(S[i][1], q[1], d); d = dot2(S[i][2], q[2], d); d = dot2(S[i][3], q[3], d);
    asm volatile("" : "+v"(d));
    constexpr int t = i & 15;
    if ((t & 1) == 0) d0 = d;
    else {
      const float a1 = merge<0>(d0, d, c);
      if ((t & 2) == 0) m1a = a1;
      else {
        m1 = merge<1>(m1a, a1, c);
        if ((t & 4) == 0) m2a = m1;
        else {
          m2 = merge<2>(m2a, m1, c);
          if ((t & 8) == 0) m3a = m2;
          else sc[i >> 4] = merge<3>(m3a, m2, c);
        }
      }
    }
    SB();
  });
  STAMP(3);
  constexpr float C2 = 0.08838834764831845f * 1.4426950408889634f;
  float pr[4];
  {
#pragma unroll
    for (int j = 0; j < 4; ++j) sc[j] *= C2;
    const float mw = wave_max(fmaxf(fmaxf(sc[0], sc[1]), fmaxf(sc[2], sc[3])));
#pragma unroll
    for (int j = 0; j < 4; ++j) pr[j] = __builtin_amdgcn_exp2f(sc[j] - mw);
    const float lw = wave_sum((pr[0] + pr[1]) + (pr[2] + pr[3]));
    *(f32x2*)(lds + (lane == 0 ? L_ML + w * 8 : L_TRASH + lane * 8)) = f32x2{mw, lw};
    asm volatile("s_waitcnt lgkmcnt(0)" ::: "memory");
    if (lane == 0) __hip_atomic_fetch_add((LAS u32*)(LAS unsigned char*)(lds + L_CNT), 1u, __ATOMIC_RELAXED, __HIP_MEMORY_SCOPE_WORKGROUP);
  }
  SB();

  float acc[8];
#pragma unroll
  for (int k = 0; k < 8; ++k) acc[k] = 0.f;
  u32x4 WO[5], BF[4][5];
  u32 vWO[5];
#pragma unroll
  for (int k = 0; k < 5; ++k) { const int it = lane + 64 * k; vWO[k] = (it / 10) * (DM * 2) + (it % 10) * 16 + w * 160; }
  sfor<64, 128>([&](auto I) INL {
    constexpr int i = decltype(I)::value;
    constexpr int vi = i - 64, t = vi & 15;
    const u32x4 V = S[i];
    float v0 = bflo(V[0]);
    asm volatile("" : "+v"(v0));
    SB();
    sfor<target(i - 1), target(i)>([&](auto N) INL { S[decltype(N)::value] = ldS(decltype(N)::value); });
    if constexpr (vi == VSIG) { if (tid == 0) __hip_atomic_fetch_add((LAS u32*)(LAS unsigned char*)(lds + L_CNT + 8), 1u, __ATOMIC_RELAXED, __HIP_MEMORY_SCOPE_WORKGROUP); }
    if constexpr (vi >= 2 && vi < 50 && (vi - 2) % 12 < 10 && (vi - 2) % 2 == 0) { const int rho = (vi - 2) / 12, k = ((vi - 2) % 12) / 2;
 WO[k] = bld128<W_AUX>(rsWO, vWO[k], rho * 32 * DM * 2);
 }
    SB();
    {
      const float pb = bcast<t>(pr[vi >> 4]);
      acc[0] = fmaf(pb, v0, acc[0]); acc[1] = fmaf(pb, bfhi(V[0]), acc[1]);
#pragma unroll
      for (int u = 1; u < 4; ++u) { acc[2 * u] = fmaf(pb, bflo(V[u]), acc[2 * u]); acc[2 * u + 1] = fmaf(pb, bfhi(V[u]), acc[2 * u + 1]); }
      asm volatile("" : "+v"(acc[0]), "+v"(acc[1]), "+v"(acc[2]), "+v"(acc[3]), "+v"(acc[4]), "+v"(acc[5]), "+v"(acc[6]), "+v"(acc[7]));
    }
    if constexpr (vi >= 13 && vi <= 49 && (vi - 13) % 12 == 0) {
      unsigned char* st = lds + L_WO + w * 5120;
#pragma unroll
      for (int k = 0; k < 5; ++k) *(u32x4*)(st + (lane + 64 * k) * 16) = WO[k];
    }
    if constexpr (vi >= 14 && vi <= 50 && (vi - 14) % 12 == 0) {
      constexpr int rho = (vi - 14) / 12;
      LAS unsigned char* st = (LAS unsigned char*)(lds + L_WO + w * 5120);
#pragma unroll
      for (int n = 0; n < 5; ++n) {
        LAS unsigned char* a = st + (8 * g4 + (c >> 2)) * 160 + 32 * n + 8 * (c & 3);
        const u32x2 lo = __builtin_bit_cast(u32x2, __builtin_amdgcn_ds_read_tr16_b64_v4i16((LAS s16x4*)a));
        const u32x2 hi = __builtin_bit_cast(u32x2, __builtin_amdgcn_ds_read_tr16_b64_v4i16((LAS s16x4*)(a + 4 * 160)));
        BF[rho][n] = u32x4{lo.x, lo.y, hi.x, hi.y};
      }
    }
    if constexpr (vi >= 16 && vi <= 52 && (vi - 16) % 12 == 0) {
      constexpr int rho = (vi - 16) / 12;
#pragma unroll
      for (int n = 0; n < 5; ++n) asm volatile("" : "+v"(BF[rho][n]));
    }
    SB();
  });
  STAMP(4);

  {
    const float z0 = tr16(tr32(acc[0], acc[4]), tr32(acc[2], acc[6])), z1 = tr16(tr32(acc[1], acc[5]), tr32(acc[3], acc[7]));
    const u32 hi = pkbf(z0, z1), lo = pkbf(z0 - bflo(hi), z1 - bfhi(hi));
    *(u32*)(lds + L_OA + w * 272 + 16 * c + 4 * g4) = hi;
    *(u32*)(lds + L_OA + (8 + w) * 272 + 16 * c + 4 * g4) = lo;
  }
  __syncthreads();
  const u32x2 tg = *(const u32x2*)(lds + L_TAG);
  const u32 tA = tg.x, tB = tg.y;
  {
    f32x4 D[5];
#pragma unroll
    for (int n = 0; n < 5; ++n) D[n] = f32x4{0.f, 0.f, 0.f, 0.f};
    const f32x4 fr = *(const f32x4*)(lds + L_FW + 16 * g4);
#pragma unroll
    for (int rho = 0; rho < 4; ++rho) {
      const u32x4 A = *(const u32x4*)(lds + L_OA + c * 272 + 64 * rho + 16 * g4);
#pragma unroll
      for (int n = 0; n < 5; ++n) D[n] = __builtin_amdgcn_mfma_f32_16x16x32_bf16(__builtin_bit_cast(bf16x8, A), __builtin_bit_cast(bf16x8, BF[rho][n]), D[n], 0, 0, 0);
    }
    float e[5];
#pragma unroll
    for (int n = 0; n < 5; ++n) e[n] = fmaf(D[n][0], fr[0], D[n][1] * fr[1]) + fmaf(D[n][2], fr[2], D[n][3] * fr[3]);
    const float ya = tr16(tr32(e[0], e[2]), tr32(e[1], e[3]));
    const float yb = rows_sum(e[4]);
    const float yan = dpp<XOR1>(ya), ybn = dpp<XOR1>(yb);
    const bool odd = c & 1;
    const u32 cp = odd ? 40 * w + 32 + (c >> 1) : 40 * w + 8 * g4 + (c >> 1);
    const u32 off = (!odd || g4 == 0) ? WS_Y + (prod * 320 + cp) * 16 : OOB;
    const u32x4 gr = odd ? u32x4{f2u(ybn), tA, f2u(yb), tB} : u32x4{f2u(ya), tA, f2u(yan), tB};
    __builtin_amdgcn_raw_buffer_store_b128(gr, rsWS, (int)off, 0, AUX_SC1);
  }
  STAMP(5);
  if (!is_cons) return;

  const u32 offY = WS_Y + ((cb * 128 + (tid >> 2)) * 320 + 4 * coct + (tid & 3)) * 16;
  u32x4 YG = bld128<AUX_SC1>(rsWS, offY, 0);
  YG = wait_granule<Y_SLEEP>(YG, rsWS, offY, tA, tB, true);
  {
    float y0 = u2f(YG.x), y1 = u2f(YG.z);
    y0 += dpp<ROR4>(y0); y1 += dpp<ROR4>(y1); y0 += dpp<ROR8>(y0); y1 += dpp<ROR8>(y1);
    y0 = rows_sum(y0); y1 = rows_sum(y1);
    *(f32x2*)(lds + (lane < 4 ? L_YP + w * 32 : L_TRASH) + lane * 8) = f32x2{y0, y1};
  }
  __syncthreads();
  STAMP(8);
}

}

extern "C" void kernel_launch(void* const* d_in, const int* in_sizes, int n_in, void* d_out, int out_size, void* d_ws, size_t ws_size, hipStream_t stream) {
  (void)in_sizes; (void)n_in; (void)out_size; (void)ws_size;
  Params p;
  p.x = d_in[0]; p.k = d_in[1]; p.v = d_in[2]; p.wq = d_in[3]; p.wo = d_in[4];
  p.w1 = d_in[6]; p.b1 = d_in[7]; p.w2 = d_in[8]; p.b2 = d_in[9]; p.out = d_out;
  p.ws = (char*)d_ws + ((ws_size - (size_t)(4u << 20)) & ~(size_t)4095);
  hipLaunchKernelGGL(fused_block, dim3(NWG), dim3(NTH), 0, stream, p);
}
```

```cpp
#define K_N0 18 Q_DELAY
#include <hip/hip_runtime.h>
#include <hip/hip_bf16.h>
#include <stdint.h>

namespace {

typedef unsigned u32;
typedef unsigned long long u64;
typedef unsigned short u16;
typedef u32 u32x4 __attribute__((ext_vector_type(4)));
typedef u32 u32x2 __attribute__((ext_vector_type(2)));
typedef float f32x4 __attribute__((ext_vector_type(4)));
typedef float f32x2 __attribute__((ext_vector_type(2)));
typedef short s16x4 __attribute__((ext_vector_type(4)));
typedef __bf16 bf16x2 __attribute__((ext_vector_type(2)));
typedef __bf16 bf16x8 __attribute__((ext_vector_type(8)));
typedef __amdgpu_buffer_rsrc_t rsrc_t;
#define GAS __attribute__((address_space(1)))
#define LAS __attribute__((address_space(3)))

constexpr int DM = 640, NH = 32, SK = 8192, DH = 128, FF = 1728;
constexpr int NWG = 256, NTH = 768;
constexpr int NFFN = FF / 8;
constexpr int CONS0 = 96;
constexpr u32 WS_E = 0;
constexpr u32 WS_ML = 4096;
constexpr u32 WS_Q = 8192;
constexpr u32 WS_H = WS_Q + NH * 8 * 2 * 64 * 16;
constexpr u32 WS_F = WS_H + NFFN * 64 * 16;
constexpr u32 WS_Y = WS_F + NFFN * 80 * 16;
constexpr u32 WS_END = WS_Y + NWG * 320 * 16;
constexpr u32 OOB = 0x7ffffff0u;
constexpr int AUX_NT = 2, AUX_SC1 = 16;
constexpr u32 SPIN_CAP = 1u << 16;

#define WQ_AUX 2
#define W_AUX 2
#define X_AUX 0
#define K_AUX 2
#define V_AUX 2
#define Y_SLEEP 2
#define VSIG 40
#define POLL_SLEEP 1
#define Q_DELAY 0
#define K_PRE 3
#define K_A 5
#define PACE 3
#define RING 3
#define BUMP 3
#define RING_END RING
#define RAMP_AT 96
#define RAMP_STEP 2
constexpr int N0 = K_N0, R = RING;
constexpr int target(int i) {
  int t = i + 1 + R;
  const int top = N0 + (i + 1 < R ? i + 1 : R);
  if (t < top) t = top;
  if (i >= 64 - BUMP && i < 64) t = i + 1 + R + (i - (64 - BUMP) + 1);
  if (i >= 64 && t < 64 + R + BUMP) t = 64 + R + BUMP;
  if (i >= RAMP_AT) { int rr = R + (i - RAMP_AT) / RAMP_STEP + 1; if (rr > RING_END) rr = RING_END; if (t < i + 1 + rr) t = i + 1 + rr; }
  return t > 128 ? 128 : t;
}

struct Params {
  const void* x; const void* k; const void* v; const void* wq; const void* wo;
  const void* w1; const void* b1; const void* w2; const void* b2; void* out; void* ws;
};

__device__ __forceinline__ rsrc_t mk_rsrc(const void* p, u32 bytes) { return __builtin_amdgcn_make_buffer_rsrc((void*)p, 0, (int)bytes, 0x00020000); }
template <int AUX> __device__ __forceinline__ u32x4 bld128(rsrc_t r, u32 voff, u32 soff) { return __builtin_amdgcn_raw_buffer_load_b128(r, (int)voff, (int)soff, AUX); }
template <int AUX> __device__ __forceinline__ u32 bld32(rsrc_t r, u32 voff, u32 soff) { return __builtin_amdgcn_raw_buffer_load_b32(r, (int)voff, (int)soff, AUX); }
__device__ __forceinline__ u32 bld16(rsrc_t r, u32 voff) { return (u32)__builtin_amdgcn_raw_buffer_load_b16(r, (int)voff, 0, 2); }
__device__ __forceinline__ float bflo(u32 x) { return __builtin_bit_cast(float, x << 16); }
__device__ __forceinline__ float bfhi(u32 x) { return __builtin_bit_cast(float, x & 0xffff0000u); }
__device__ __forceinline__ u32 pkbf(float lo, float hi) { bf16x2 v = {(__bf16)lo, (__bf16)hi}; return __builtin_bit_cast(u32, v); }
__device__ __forceinline__ float rbf(float x) { return bflo(pkbf(x, 0.f) & 0xffffu); }
__device__ __forceinline__ float dot2(u32 a, u32 b, float c) { return __builtin_amdgcn_fdot2_f32_bf16(__builtin_bit_cast(bf16x2, a), __builtin_bit_cast(bf16x2, b), c, false); }
template <int CTRL> __device__ __forceinline__ float dpp(float x) { return __builtin_bit_cast(float, __builtin_amdgcn_mov_dpp(__builtin_bit_cast(int, x), CTRL, 0xf, 0xf, true)); }
constexpr int XOR1 = 0xB1, XOR2 = 0x4E, ROR4 = 0x124, ROR8 = 0x128, ROR2 = 0x122, ROR1 = 0x121, BCAST = 0x150;
__device__ __forceinline__ float u2f(u32 x) { return __builtin_bit_cast(float, x); }
__device__ __forceinline__ u32 f2u(float x) { return __builtin_bit_cast(u32, x); }
__device__ __forceinline__ float tr32(float a, float b) { auto s = __builtin_amdgcn_permlane32_swap(f2u(a), f2u(b), false, false); return u2f(s[0]) + u2f(s[1]); }
__device__ __forceinline__ float tr16(float a, float b) { auto s = __builtin_amdgcn_permlane16_swap(f2u(a), f2u(b), false, false); return u2f(s[0]) + u2f(s[1]); }
__device__ __forceinline__ float rows_sum(float x) { x = tr16(x, x); return tr32(x, x); }
__device__ __forceinline__ float rows_max(float x) {
  auto s = __builtin_amdgcn_permlane16_swap(f2u(x), f2u(x), false, false); x = fmaxf(u2f(s[0]), u2f(s[1]));
  auto t = __builtin_amdgcn_permlane32_swap(f2u(x), f2u(x), false, false); return fmaxf(u2f(t[0]), u2f(t[1]));
}
__device__ __forceinline__ float row16_sum(float x) { x += dpp<ROR8>(x); x += dpp<ROR4>(x); x += dpp<ROR2>(x); x += dpp<ROR1>(x); return x; }
__device__ __forceinline__ float row16_max(float x) { x = fmaxf(x, dpp<ROR8>(x)); x = fmaxf(x, dpp<ROR4>(x)); x = fmaxf(x, dpp<ROR2>(x)); x = fmaxf(x, dpp<ROR1>(x)); return x; }
__device__ __forceinline__ float wave_sum(float x) { return rows_sum(row16_sum(x)); }
__device__ __forceinline__ float wave_max(float x) { return rows_max(row16_max(x)); }
template <int K> __device__ __forceinline__ float merge(float lo, float hi, int c) {
  const bool bit = (c >> K) & 1;
  const float keep = bit ? hi : lo, send = bit ? lo : hi;
  constexpr int CTRL = K == 0 ? XOR1 : K == 1 ? XOR2 : K == 2 ? ROR4 : ROR8;
  return keep + dpp<CTRL>(send);
}
template <int T> __device__ __forceinline__ float bcast(float x) { return dpp<BCAST + T>(x); }
#define SB() __builtin_amdgcn_sched_barrier(0)
template <int I> struct IC { static constexpr int value = I; };
template <int B, int E, class F> __device__ __forceinline__ void sfor(F&& f) { if constexpr (B < E) { f(IC<B>{}); sfor<B + 1, E>(f); } }
#define INL __attribute__((always_inline))

__device__ __forceinline__ bool tags_ok(const u32x4& g, u32 tA, u32 tB) { return g.y == tA && g.w == tB; }
template <int SLEEP = 2> __device__ __forceinline__ u32x4 wait_granule(u32x4 g, rsrc_t rs, u32 off, u32 tA, u32 tB, bool need) {
  bool ok = !need || tags_ok(g, tA, tB);
  if (!__all(ok)) {
#pragma unroll 1
    for (u32 spin = 0; spin < SPIN_CAP; ++spin) {
      if (!ok) { g = bld128<AUX_SC1>(rs, off, 0); ok = tags_ok(g, tA, tB); }
      if (__all(ok)) break;
      __builtin_amdgcn_s_sleep(SLEEP);
    }
  }
  return g;
}

template <int N> __device__ __forceinline__ void wait_granules(u32x4 (&g)[N], rsrc_t rs, u32 off, u32 stride, u32 tA, u32 tB) {
  bool ok = true;
#pragma unroll
  for (int k = 0; k < N; ++k) ok = ok && tags_ok(g[k], tA, tB);
  if (!__all(ok)) {
#pragma unroll 1
    for (u32 spin = 0; spin < SPIN_CAP; ++spin) {
#pragma unroll
      for (int k = 0; k < N; ++k) if (!tags_ok(g[k], tA, tB)) g[k] = bld128<AUX_SC1>(rs, off, k * stride);
      ok = true;
#pragma unroll
      for (int k = 0; k < N; ++k) ok = ok && tags_ok(g[k], tA, tB);
      if (__all(ok)) break;
      __builtin_amdgcn_s_sleep(POLL_SLEEP);
    }
  }
}
__device__ __forceinline__ void lds_wait(LAS unsigned char* word, u32 want) {
#pragma unroll 1
  for (u32 spin = 0; spin < (SPIN_CAP << 4); ++spin) {
    if (*(volatile LAS u32*)word >= want) break;
    __builtin_amdgcn_s_sleep(2);
  }
  asm volatile("" ::: "memory");
}

constexpr int L_XN = 0;
constexpr int L_QP = 2560;
constexpr int L_Q = L_QP + 4096;
constexpr int L_ML = L_Q + 256;
constexpr int L_CNT = L_ML + 64;
constexpr int L_TAG = L_CNT + 16;
constexpr int L_FW = L_CNT + 32;
constexpr int L_FP = L_FW + 64;
constexpr int L_YP = L_FP + 32;
constexpr int L_H2 = L_YP + 256;
constexpr int L_FR = L_H2 + 512;
constexpr int L_OA = L_FR + 3840;
constexpr int L_WO = L_OA + 16 * 272;
constexpr int L_TRASH = L_WO + 8 * 5120;
constexpr int L_END = L_TRASH + 1024;
static_assert(L_OA % 16 == 0 && L_WO % 16 == 0 && L_FW % 16 == 0 && L_FR % 16 == 0 && L_QP % 16 == 0, "LDS alignment");

#define STAMP(n) do {} while (0)

template <int KA, int VA> __device__ __forceinline__ void body(const Params& p, unsigned char* lds) {
  const int tid = threadIdx.x, lane = tid & 63, w = __builtin_amdgcn_readfirstlane(tid >> 6), g4 = lane >> 4, c = lane & 15;
  const int bid = blockIdx.x, xc = bid & 7, rr = bid >> 3;
#define HMAP 0
  const int h = HMAP == 0 ? (rr >> 3) * 8 + xc : HMAP == 1 ? (rr >> 3) * 8 + ((xc + 2 * (rr >> 3)) & 7) : (rr >> 3) * 8 + ((xc + 3 * (rr >> 3)) & 7), b = (rr >> 2) & 1, s = rr & 3;
  const int pair = b * NH + h, prod = pair * 4 + s;
  const int fu = xc + 8 * (rr >> 3), fv = rr & 7;
  const bool is_ffn = fu < 27;
  const bool is_cons = bid >= CONS0;
  const int cj = bid - CONS0, cb = is_cons ? cj / 80 : 0, coct = is_cons ? cj % 80 : 0;
  STAMP(0);

  const rsrc_t rsWS = mk_rsrc(p.ws, WS_END);
  const rsrc_t rsK = mk_rsrc((const char*)p.k + (size_t)pair * (SK * DH * 2) + (size_t)s * (2048 * DH * 2), 2048 * DH * 2);
  const rsrc_t rsV = mk_rsrc((const char*)p.v + (size_t)pair * (SK * DH * 2) + (size_t)s * (2048 * DH * 2), 2048 * DH * 2);
  const rsrc_t rsX = mk_rsrc(p.x, 2 * DM * 2);
  const rsrc_t rsWQ = mk_rsrc(p.wq, DM * NH * DH * 2);
  const rsrc_t rsWO = mk_rsrc((const char*)p.wo + (size_t)h * (DH * DM * 2), DH * DM * 2);
  const rsrc_t rsW1 = mk_rsrc(p.w1, is_ffn ? DM * FF * 2 : 0);
  const rsrc_t rsW2 = mk_rsrc(p.w2, is_ffn ? FF * DM * 2 : 0);
  const rsrc_t rsB1 = mk_rsrc(p.b1, is_ffn ? FF * 2 : 0);
  const rsrc_t rsB2 = mk_rsrc(p.b2, DM * 2);

  auto ln_stats = [&](const u32x4& ra, u32 rb, float& mb, float& inv) INL {
    float v[10];
#pragma unroll
    for (int k = 0; k < 4; ++k) { v[2 * k] = bflo(ra[k]); v[2 * k + 1] = bfhi(ra[k]); }
    v[8] = bflo(rb); v[9] = bfhi(rb);
    float sum = 0.f;
#pragma unroll
    for (int k = 0; k < 10; ++k) sum += v[k];
    const float mean = wave_sum(sum) * (1.f / DM);
    float sq = 0.f;
#pragma unroll
    for (int k = 0; k < 10; ++k) { const float d = v[k] - mean; sq = fmaf(d, d, sq); }
    const float var = wave_sum(sq) * (1.f / DM);
    mb = rbf(mean);
    inv = 1.f / rbf(sqrtf(rbf(rbf(var) + 1e-5f)));
  };
  auto ln_row = [&](const u32x4& ra, u32 rb, float mb, float inv, int dst) INL {
#pragma unroll
    for (int k = 0; k < 5; ++k) {
      const u32 r = k < 4 ? ra[k & 3] : rb;
      const u32 d = pkbf(bflo(r) - mb, bfhi(r) - mb);
      *(u32*)(lds + dst + lane * 20 + 4 * k) = pkbf(bflo(d) * inv, bfhi(d) * inv);
    }
  };

  if (w >= 8) {
    const int hw = w - 8;
    if (hw == 0) { *(volatile LAS u32*)(LAS unsigned char*)(lds + L_CNT) = 0u; *(volatile LAS u32*)(LAS unsigned char*)(lds + L_CNT + 4) = 0u; *(volatile LAS u32*)(LAS unsigned char*)(lds + L_CNT + 8) = 0u; }
    __syncthreads();
    const u64 E = __hip_atomic_load((GAS u64*)p.ws, __ATOMIC_RELAXED, __HIP_MEMORY_SCOPE_AGENT);
    const u32x4 x0a = bld128<X_AUX>(rsX, lane * 20, 0), x1a = bld128<X_AUX>(rsX, 1280 + lane * 20, 0);
    const u32 x0b = bld32<X_AUX>(rsX, lane * 20 + 16, 0), x1b = bld32<X_AUX>(rsX, 1280 + lane * 20 + 16, 0);
    const int qrow = 80 * (4 * b + s) + 20 * hw;
    u32x4 WQ[5]; u32 xr0[5], xr1[5];
#pragma unroll
    for (int k = 0; k < 5; ++k) WQ[k] = bld128<WQ_AUX>(rsWQ, g4 * 8192 + c * 16, (qrow + 4 * k) * 8192 + h * 256);
#pragma unroll
    for (int k = 0; k < 5; ++k) { xr0[k] = __builtin_amdgcn_raw_buffer_load_b16(rsX, (qrow + 4 * k + g4) * 2, 0, X_AUX); xr1[k] = __builtin_amdgcn_raw_buffer_load_b16(rsX, 1280 + (qrow + 4 * k + g4) * 2, 0, X_AUX); }
    float mb0, inv0, mb1, inv1;
    ln_stats(x0a, x0b, mb0, inv0);
    ln_stats(x1a, x1b, mb1, inv1);
    STAMP(9);
    {
      float a[16];
#pragma unroll
      for (int k = 0; k < 16; ++k) a[k] = 0.f;
#pragma unroll
      for (int k = 0; k < 5; ++k) {
        const float x0 = rbf(rbf(bflo(xr0[k]) - mb0) * inv0), x1 = rbf(rbf(bflo(xr1[k]) - mb1) * inv1);
#pragma unroll
        for (int u = 0; u < 4; ++u) {
          const float lo = bflo(WQ[k][u]), hi = bfhi(WQ[k][u]);
          a[2 * u] = fmaf(x0, lo, a[2 * u]); a[2 * u + 1] = fmaf(x0, hi, a[2 * u + 1]);
          a[8 + 2 * u] = fmaf(x1, lo, a[8 + 2 * u]); a[8 + 2 * u + 1] = fmaf(x1, hi, a[8 + 2 * u + 1]);
        }
      }
#pragma unroll
      for (int e = 0; e < 8; ++e) a[e] = tr32(a[e], a[8 + e]);
      const f32x4 z = {tr16(a[0], a[4]), tr16(a[1], a[5]), tr16(a[2], a[6]), tr16(a[3], a[7])};
      *(f32x4*)(lds + L_QP + (hw * 256 + (g4 >> 1) * 128 + 8 * c + 4 * (g4 & 1)) * 4) = z;
      asm volatile("s_waitcnt lgkmcnt(0)" ::: "memory");
      if (lane == 0) __hip_atomic_fetch_add((LAS u32*)(LAS unsigned char*)(lds + L_CNT + 4), 1u, __ATOMIC_RELAXED, __HIP_MEMORY_SCOPE_WORKGROUP);
    }
    if (hw != 0) return;
    const u32 tA = (u32)E + 0x9E3779B9u, tB = (u32)(E >> 32) + 0x85EBCA6Bu;
    *(u32x2*)(lds + (lane == 0 ? L_TAG : L_TRASH + lane * 8)) = u32x2{tA, tB};
    lds_wait((LAS unsigned char*)(lds + L_CNT + 4), 4u);
    STAMP(7);
    {
      f32x2 t0 = {0.f, 0.f}, t1 = {0.f, 0.f};
#pragma unroll
      for (int k = 0; k < 4; ++k) { t0 += *(const f32x2*)(lds + L_QP + (k * 256 + 2 * lane) * 4); t1 += *(const f32x2*)(lds + L_QP + (k * 256 + 128 + 2 * lane) * 4); }
      const u32 offP = WS_Q + (((h * 8 + b * 4 + s) * 2) * 64 + lane) * 16;
      __builtin_amdgcn_raw_buffer_store_b128(u32x4{f2u(t0.x), tA, f2u(t0.y), tB}, rsWS, (int)offP, 0, AUX_SC1);
      __builtin_amdgcn_raw_buffer_store_b128(u32x4{f2u(t1.x), tA, f2u(t1.y), tB}, rsWS, (int)offP, 64 * 16, AUX_SC1);
      const u32 offQ = WS_Q + ((h * 8 * 2 + b) * 64 + lane) * 16;
      u32x4 g[8];
      if (Q_DELAY) __builtin_amdgcn_s_sleep(Q_DELAY);
#pragma unroll
      for (int k = 0; k < 8; ++k) g[k] = bld128<AUX_SC1>(rsWS, offQ, k * 2 * 64 * 16);
      STAMP(6);
      wait_granules<8>(g, rsWS, offQ, 2 * 64 * 16, tA, tB);
      float q0 = 0.f, q1 = 0.f;
#pragma unroll
      for (int k = 0; k < 8; ++k) { q0 += u2f(g[k].x); q1 += u2f(g[k].z); }
      *(u32*)(lds + L_Q + lane * 4) = pkbf(q0, q1);
    }
    STAMP(1);
    __syncthreads();
    const u32 B2v = bld16(rsB2, (8 * coct + (lane & 7)) * 2), XV = bld16(rsX, cb * 1280 + (8 * coct + (lane & 7)) * 2);
    ln_row(x0a, x0b, mb0, inv0, L_XN);
    ln_row(x1a, x1b, mb1, inv1, L_XN + 1280);
    if (is_ffn) {
      u32x4 W1[10];
#pragma unroll
      for (int k = 0; k < 10; ++k) W1[k] = bld128<W_AUX>(rsW1, (lane >> 3) * (FF * 2) + (lane & 7) * 16, (80 * fv + 8 * k) * (FF * 2) + fu * 128);
      SB();
      float hv[16];
#pragma unroll
      for (int k = 0; k < 16; ++k) hv[k] = 0.f;
#pragma unroll
      for (int k = 0; k < 10; ++k) {
        const int row = 80 * fv + 8 * k + (lane >> 3);
        const float x0 = bflo(*(const u16*)(lds + L_XN + row * 2)), x1 = bflo(*(const u16*)(lds + L_XN + 1280 + row * 2));
#pragma unroll
        for (int u = 0; u < 4; ++u) {
          const float lo = bflo(W1[k][u]), hi = bfhi(W1[k][u]);
          hv[2 * u] = fmaf(x0, lo, hv[2 * u]); hv[2 * u + 1] = fmaf(x0, hi, hv[2 * u + 1]);
          hv[8 + 2 * u] = fmaf(x1, lo, hv[8 + 2 * u]); hv[8 + 2 * u + 1] = fmaf(x1, hi, hv[8 + 2 * u + 1]);
        }
      }
      SB();
#pragma unroll
      for (int e = 0; e < 8; ++e) hv[e] = merge<3>(hv[e], hv[8 + e], c);
#pragma unroll
      for (int e = 0; e < 4; ++e) hv[e] = tr16(hv[e], hv[e + 4]);
      const float h0 = tr32(hv[0], hv[2]), h1 = tr32(hv[1], hv[3]);
      const u32 gi = ((lane >> 3) & 1) * 32 + 4 * (lane & 7) + 2 * ((lane >> 4) & 1) + (lane >> 5);
      __builtin_amdgcn_raw_buffer_store_b128(u32x4{f2u(h0), tA, f2u(h1), tB}, rsWS, (int)(WS_H + ((fu * 8 + fv) * 64 + gi) * 16), 0, AUX_SC1);
    }
    STAMP(2);
      lds_wait((LAS unsigned char*)(lds + L_CNT), 8u);
      const f32x2 ml = *(const f32x2*)(lds + L_ML + (lane & 7) * 8);
      const float mg = wave_max(ml.x);
      const float lg = row16_sum(c < 8 ? ml.y * __builtin_amdgcn_exp2f(ml.x - mg) : 0.f);
      __builtin_amdgcn_raw_buffer_store_b128(u32x4{f2u(mg), tA, f2u(lg), tB}, rsWS, (int)(lane == 0 ? WS_ML + prod * 16 : OOB), 0, AUX_SC1);
      STAMP(3);
    if (is_ffn) {
      u32x4 W2[11];
      const u32 B1v = bld32<W_AUX>(rsB1, (lane & 31) * 4, fu * 128);
      const int rg = lane / 10, ch = lane - 10 * rg;
#pragma unroll
      for (int k = 0; k < 11; ++k) W2[k] = bld128<W_AUX>(rsW2, (lane < 60 && 6 * k + rg < 64) ? rg * (DM * 2) + ch * 16 : OOB, (64 * fu + 6 * k) * (DM * 2) + fv * 160);
      SB();
      const u32 offH = WS_H + (fu * 8 * 64 + lane) * 16;
      u32x4 g[8];
#pragma unroll
      for (int k = 0; k < 8; ++k) g[k] = bld128<AUX_SC1>(rsWS, offH, k * 64 * 16);
      wait_granules<8>(g, rsWS, offH, 64 * 16, tA, tB);
      float s0 = 0.f, s1 = 0.f;
#pragma unroll
      for (int k = 0; k < 8; ++k) { s0 += u2f(g[k].x); s1 += u2f(g[k].z); }
      s0 = rbf(rbf(s0) + bflo(B1v)); s1 = rbf(rbf(s1) + bfhi(B1v));
      s0 = s0 > 0.f ? s0 : 0.f; s1 = s1 > 0.f ? s1 : 0.f;
      *(f32x2*)(lds + L_H2 + lane * 8) = f32x2{rbf(s0 * s0), rbf(s1 * s1)};
      float ov[16];
#pragma unroll
      for (int k = 0; k < 16; ++k) ov[k] = 0.f;
#pragma unroll
      for (int k = 0; k < 11; ++k) {
        const int r = (6 * k + rg) & 63;
        const float a0 = *(const float*)(lds + L_H2 + r * 4), a1 = *(const float*)(lds + L_H2 + 256 + r * 4);
#pragma unroll
        for (int u = 0; u < 4; ++u) {
          const float lo = bflo(W2[k][u]), hi = bfhi(W2[k][u]);
          ov[2 * u] = fmaf(a0, lo, ov[2 * u]); ov[2 * u + 1] = fmaf(a0, hi, ov[2 * u + 1]);
          ov[8 + 2 * u] = fmaf(a1, lo, ov[8 + 2 * u]); ov[8 + 2 * u + 1] = fmaf(a1, hi, ov[8 + 2 * u + 1]);
        }
      }
      unsigned char* fr = lds + (lane < 60 ? L_FR + lane * 64 : L_TRASH + (lane & 3) * 64);
#pragma unroll
      for (int k = 0; k < 4; ++k) *(f32x4*)(fr + 16 * k) = f32x4{ov[4 * k], ov[4 * k + 1], ov[4 * k + 2], ov[4 * k + 3]};
#pragma unroll
      for (int j = 0; j < 2; ++j) {
        const int gq = lane + 64 * j, gb = gq >= 40 ? 1 : 0, cp = (gq - 40 * gb) % 40;
        f32x2 t = {0.f, 0.f};
#pragma unroll
        for (int k = 0; k < 6; ++k) t += *(const f32x2*)(lds + L_FR + (k * 10 + (cp >> 2)) * 64 + (gb * 8 + 2 * (cp & 3)) * 4);
        __builtin_amdgcn_raw_buffer_store_b128(u32x4{f2u(t.x), tA, f2u(t.y), tB}, rsWS, (int)(gq < 80 ? WS_F + ((fu * 8 + fv) * 80 + gq) * 16 : OOB), 0, AUX_SC1);
      }
    }
    {
      const u32 offML = WS_ML + (pair * 4 + (lane & 3)) * 16;
      u32x4 g = bld128<AUX_SC1>(rsWS, offML, 0);
      g = wait_granule(g, rsWS, offML, tA, tB, true);
      const float mv = u2f(g.x), M = row16_max(mv);
      const float L = row16_sum(c < 4 ? u2f(g.z) * __builtin_amdgcn_exp2f(mv - M) : 0.f);
      *(float*)(lds + (lane < 16 ? L_FW : L_TRASH) + lane * 4) = __builtin_amdgcn_exp2f(ml.x - M) / L;
    }
    STAMP(4);
    float gate = 0.f;
    if (is_cons) {
      lds_wait((LAS unsigned char*)(lds + L_CNT + 8), 1u);
      u32x4 FG[2]; u32 offF[2];
      const int cv = coct / 10, cp0 = (coct - 10 * cv) * 4;
#pragma unroll
      for (int k = 0; k < 2; ++k) {
        const int idx = lane + 64 * k;
        offF[k] = idx < 108 ? WS_F + (((idx >> 2) * 8 + cv) * 80 + cb * 40 + cp0 + (idx & 3)) * 16 : OOB;
        FG[k] = bld128<AUX_SC1>(rsWS, offF[k], 0);
      }
      FG[0] = wait_granule(FG[0], rsWS, offF[0], tA, tB, true);
      FG[1] = wait_granule(FG[1], rsWS, offF[1], tA, tB, lane < 44);
      float f0 = u2f(FG[0].x) + (lane < 44 ? u2f(FG[1].x) : 0.f), f1 = u2f(FG[0].z) + (lane < 44 ? u2f(FG[1].z) : 0.f);
      f0 += dpp<ROR4>(f0); f1 += dpp<ROR4>(f1); f0 += dpp<ROR8>(f0); f1 += dpp<ROR8>(f1);
      f0 = rows_sum(f0); f1 = rows_sum(f1);
      *(f32x2*)(lds + (lane < 4 ? L_FP : L_TRASH) + lane * 8) = f32x2{f0, f1};
      const float fs = *(const float*)(lds + L_FP + (lane & 7) * 4);
      const float o = rbf(rbf(fs) + bflo(B2v));
      gate = rbf(o / (1.f + __expf(-o)));
    }
    STAMP(5);
    __syncthreads();
    if (!is_cons) return;
    u32x4 og;
    const u32 offO = WS_Y + ((128 * (cb ^ 1) + lane) * 320) * 16;
    __syncthreads();
    {
      float ys = 0.f;
#pragma unroll
      for (int k = 0; k < 8; ++k) ys += *(const float*)(lds + L_YP + (k * 8 + (lane & 7)) * 4);
      const float res = bflo(XV) + rbf(rbf(ys) * gate);
      if (lane < 8) ((GAS u16*)p.out)[cb * DM + 8 * coct + lane] = (u16)(pkbf(res, 0.f) & 0xffffu);
    }
    STAMP(8);
    if (bid == CONS0) {
      og = bld128<AUX_SC1>(rsWS, offO, 0);
      og = wait_granule(og, rsWS, offO, tA, tB, true);
      og = bld128<AUX_SC1>(rsWS, offO + 64 * 320 * 16, 0);
      og = wait_granule(og, rsWS, offO + 64 * 320 * 16, tA, tB, true);
      if (lane == 0) __hip_atomic_store((GAS u64*)p.ws, (u64)tA | ((u64)tB << 32), __ATOMIC_RELAXED, __HIP_MEMORY_SCOPE_AGENT);
    }
    return;
  }

  const u32 vKV = w * 4096 + lane * 16;
  u32x4 S[128];
  auto ldS = [&](int i) INL -> u32x4 { const int t = i & 63; return i < 64 ? bld128<KA>(rsK, vKV + (t & 3) * 1024, (t >> 2) * 32768) : bld128<VA>(rsV, vKV + (t & 3) * 1024, (t >> 2) * 32768); };
  sfor<0, PACE>([&](auto I) INL { S[decltype(I)::value] = ldS(decltype(I)::value); });
  SB();
  __syncthreads();
  sfor<PACE, N0>([&](auto I) INL {
    constexpr int n = decltype(I)::value;
    asm volatile("" :: "v"(S[n - PACE][0]));
    SB();
    S[n] = ldS(n);
    SB();
    if constexpr (n == 6) STAMP(9);
    if constexpr (n == 10) STAMP(10);
    if constexpr (n == 14) STAMP(11);
  });
  STAMP(6);
  __syncthreads();
  u32 q[4];
  { const u32x4 qq = *(const u32x4*)(lds + L_Q + c * 16); q[0] = qq[0]; q[1] = qq[1]; q[2] = qq[2]; q[3] = qq[3]; }
  STAMP(2);

  float sc[4], d0 = 0.f, m1a = 0.f, m2a = 0.f, m3a = 0.f, m1 = 0.f, m2 = 0.f;
  sfor<0, 64>([&](auto I) INL {
    constexpr int i = decltype(I)::value;
    float d = dot2(S[i][0], q[0], 0.f);
    asm volatile("" : "+v"(d));
    SB();
    sfor<(i == 0 ? N0 : target(i - 1)), target(i)>([&](auto N) INL { S[decltype(N)::value] = ldS(decltype(N)::value); });
    SB();
    d = dot2(S[i][1], q[1], d); d = dot2(S[i][2], q[2], d); d = dot2(S[i][3], q[3], d);
    asm volatile("" : "+v"(d));
    constexpr int t = i & 15;
    if ((t & 1) == 0) d0 = d;
    else {
      const float a1 = merge<0>(d0, d, c);
      if ((t & 2) == 0) m1a = a1;
      else {
        m1 = merge<1>(m1a, a1, c);
        if ((t & 4) == 0) m2a = m1;
        else {
          m2 = merge<2>(m2a, m1, c);
          if ((t & 8) == 0) m3a = m2;
          else sc[i >> 4] = merge<3>(m3a, m2, c);
        }
      }
    }
    SB();
  });
  STAMP(3);
  constexpr float C2 = 0.08838834764831845f * 1.4426950408889634f;
  float pr[4];
  {
#pragma unroll
    for (int j = 0; j < 4; ++j) sc[j] *= C2;
    const float mw = wave_max(fmaxf(fmaxf(sc[0], sc[1]), fmaxf(sc[2], sc[3])));
#pragma unroll
    for (int j = 0; j < 4; ++j) pr[j] = __builtin_amdgcn_exp2f(sc[j] - mw);
    const float lw = wave_sum((pr[0] + pr[1]) + (pr[2] + pr[3]));
    *(f32x2*)(lds + (lane == 0 ? L_ML + w * 8 : L_TRASH + lane * 8)) = f32x2{mw, lw};
    asm volatile("s_waitcnt lgkmcnt(0)" ::: "memory");
    if (lane == 0) __hip_atomic_fetch_add((LAS u32*)(LAS unsigned char*)(lds + L_CNT), 1u, __ATOMIC_RELAXED, __HIP_MEMORY_SCOPE_WORKGROUP);
  }
  SB();

  float acc[8];
#pragma unroll
  for (int k = 0; k < 8; ++k) acc[k] = 0.f;
  u32x4 WO[5], BF[4][5];
  u32 vWO[5];
#pragma unroll
  for (int k = 0; k < 5; ++k) { const int it = lane + 64 * k; vWO[k] = (it / 10) * (DM * 2) + (it % 10) * 16 + w * 160; }
  sfor<64, 128>([&](auto I) INL {
    constexpr int i = decltype(I)::value;
    constexpr int vi = i - 64, t = vi & 15;
    const u32x4 V = S[i];
    float v0 = bflo(V[0]);
    asm volatile("" : "+v"(v0));
    SB();
    sfor<target(i - 1), target(i)>([&](auto N) INL { S[decltype(N)::value] = ldS(decltype(N)::value); });
    if constexpr (vi == VSIG) { if (tid == 0) __hip_atomic_fetch_add((LAS u32*)(LAS unsigned char*)(lds + L_CNT + 8), 1u, __ATOMIC_RELAXED, __HIP_MEMORY_SCOPE_WORKGROUP); }
    if constexpr (vi >= 2 && vi < 50 && (vi - 2) % 12 < 10 && (vi - 2) % 2 == 0) { const int rho = (vi - 2) / 12, k = ((vi - 2) % 12) / 2;
 WO[k] = bld128<W_AUX>(rsWO, vWO[k], rho * 32 * DM * 2);
 }
    SB();
    {
      const float pb = bcast<t>(pr[vi >> 4]);
      acc[0] = fmaf(pb, v0, acc[0]); acc[1] = fmaf(pb, bfhi(V[0]), acc[1]);
#pragma unroll
      for (int u = 1; u < 4; ++u) { acc[2 * u] = fmaf(pb, bflo(V[u]), acc[2 * u]); acc[2 * u + 1] = fmaf(pb, bfhi(V[u]), acc[2 * u + 1]); }
      asm volatile("" : "+v"(acc[0]), "+v"(acc[1]), "+v"(acc[2]), "+v"(acc[3]), "+v"(acc[4]), "+v"(acc[5]), "+v"(acc[6]), "+v"(acc[7]));
    }
    if constexpr (vi >= 13 && vi <= 49 && (vi - 13) % 12 == 0) {
      unsigned char* st = lds + L_WO + w * 5120;
#pragma unroll
      for (int k = 0; k < 5; ++k) *(u32x4*)(st + (lane + 64 * k) * 16) = WO[k];
    }
    if constexpr (vi >= 14 && vi <= 50 && (vi - 14) % 12 == 0) {
      constexpr int rho = (vi - 14) / 12;
      LAS unsigned char* st = (LAS unsigned char*)(lds + L_WO + w * 5120);
#pragma unroll
      for (int n = 0; n < 5; ++n) {
        LAS unsigned char* a = st + (8 * g4 + (c >> 2)) * 160 + 32 * n + 8 * (c & 3);
        const u32x2 lo = __builtin_bit_cast(u32x2, __builtin_amdgcn_ds_read_tr16_b64_v4i16((LAS s16x4*)a));
        const u32x2 hi = __builtin_bit_cast(u32x2, __builtin_amdgcn_ds_read_tr16_b64_v4i16((LAS s16x4*)(a + 4 * 160)));
        BF[rho][n] = u32x4{lo.x, lo.y, hi.x, hi.y};
      }
    }
    if constexpr (vi >= 16 && vi <= 52 && (vi - 16) % 12 == 0) {
      constexpr int rho = (vi - 16) / 12;
#pragma unroll
      for (int n = 0; n < 5; ++n) asm volatile("" : "+v"(BF[rho][n]));
    }
    SB();
  });
  STAMP(4);

  {
    const float z0 = tr16(tr32(acc[0], acc[4]), tr32(acc[2], acc[6])), z1 = tr16(tr32(acc[1], acc[5]), tr32(acc[3], acc[7]));
    const u32 hi = pkbf(z0, z1), lo = pkbf(z0 - bflo(hi), z1 - bfhi(hi));
    *(u32*)(lds + L_OA + w * 272 + 16 * c + 4 * g4) = hi;
    *(u32*)(lds + L_OA + (8 + w) * 272 + 16 * c + 4 * g4) = lo;
  }
  __syncthreads();
  const u32x2 tg = *(const u32x2*)(lds + L_TAG);
  const u32 tA = tg.x, tB = tg.y;
  {
    f32x4 D[5];
#pragma unroll
    for (int n = 0; n < 5; ++n) D[n] = f32x4{0.f, 0.f, 0.f, 0.f};
    const f32x4 fr = *(const f32x4*)(lds + L_FW + 16 * g4);
#pragma unroll
    for (int rho = 0; rho < 4; ++rho) {
      const u32x4 A = *(const u32x4*)(lds + L_OA + c * 272 + 64 * rho + 16 * g4);
#pragma unroll
      for (int n = 0; n < 5; ++n) D[n] = __builtin_amdgcn_mfma_f32_16x16x32_bf16(__builtin_bit_cast(bf16x8, A), __builtin_bit_cast(bf16x8, BF[rho][n]), D[n], 0, 0, 0);
    }
    float e[5];
#pragma unroll
    for (int n = 0; n < 5; ++n) e[n] = fmaf(D[n][0], fr[0], D[n][1] * fr[1]) + fmaf(D[n][2], fr[2], D[n][3] * fr[3]);
    const float ya = tr16(tr32(e[0], e[2]), tr32(e[1], e[3]));
    const float yb = rows_sum(e[4]);
    const float yan = dpp<XOR1>(ya), ybn = dpp<XOR1>(yb);
    const bool odd = c & 1;
    const u32 cp = odd ? 40 * w + 32 + (c >> 1) : 40 * w + 8 * g4 + (c >> 1);
    const u32 off = (!odd || g4 == 0) ? WS_Y + (prod * 320 + cp) * 16 : OOB;
    const u32x4 gr = odd ? u32x4{f2u(ybn), tA, f2u(yb), tB} : u32x4{f2u(ya), tA, f2u(yan), tB};
    __builtin_amdgcn_raw_buffer_store_b128(gr, rsWS, (int)off, 0, AUX_SC1);
  }
  STAMP(5);
  if (!is_cons) return;

  const u32 offY = WS_Y + ((cb * 128 + (tid >> 2)) * 320 + 4 * coct + (tid & 3)) * 16;
  u32x4 YG = bld128<AUX_SC1>(rsWS, offY, 0);
  YG = wait_granule<Y_SLEEP>(YG, rsWS, offY, tA, tB, true);
  {
    float y0 = u2f(YG.x), y1 = u2f(YG.z);
    y0 += dpp<ROR4>(y0); y1 += dpp<ROR4>(y1); y0 += dpp<ROR8>(y0); y1 += dpp<ROR8>(y1);
    y0 = rows_sum(y0); y1 = rows_sum(y1);
    *(f32x2*)(lds + (lane < 4 ? L_YP + w * 32 : L_TRASH) + lane * 8) = f32x2{y0, y1};
  }
  __syncthreads();
  STAMP(8);
}

__global__ void __launch_bounds__(NTH, 3) fused_block(Params p) {
  __shared__ __attribute__((aligned(16))) unsigned char lds[L_END];
  body<K_AUX, V_AUX>(p, lds);
}

}

extern "C" void kernel_launch(void* const* d_in, const int* in_sizes, int n_in, void* d_out, int out_size, void* d_ws, size_t ws_size, hipStream_t stream) {
  (void)in_sizes; (void)n_in; (void)out_size; (void)ws_size;
  Params p;
  p.x = d_in[0]; p.k = d_in[1]; p.v = d_in[2]; p.wq = d_in[3]; p.wo = d_in[4];
  p.w1 = d_in[6]; p.b1 = d_in[7]; p.w2 = d_in[8]; p.b2 = d_in[9]; p.out = d_out;
  p.ws = d_ws;
  hipLaunchKernelGGL(fused_block, dim3(NWG), dim3(NTH), 0, stream, p);
}
```

```cpp
#define K_N0 24
#define Q_DELAY 64
#define POLL_SLEEP 16
#define W_AUX 0
#define WQ_AUX 0
#define TOUCH 32768
#include <hip/hip_runtime.h>
#include <hip/hip_bf16.h>
#include <stdint.h>

namespace {

typedef unsigned u32;
typedef unsigned long long u64;
typedef unsigned short u16;
typedef u32 u32x4 __attribute__((ext_vector_type(4)));
typedef u32 u32x2 __attribute__((ext_vector_type(2)));
typedef float f32x4 __attribute__((ext_vector_type(4)));
typedef float f32x2 __attribute__((ext_vector_type(2)));
typedef short s16x4 __attribute__((ext_vector_type(4)));
typedef __bf16 bf16x2 __attribute__((ext_vector_type(2)));
typedef __bf16 bf16x8 __attribute__((ext_vector_type(8)));
typedef __amdgpu_buffer_rsrc_t rsrc_t;
#define GAS __attribute__((address_space(1)))
#define LAS __attribute__((address_space(3)))

constexpr int DM = 640, NH = 32, SK = 8192, DH = 128, FF = 1728;
constexpr int NWG = 256, NTH = 768;
constexpr int NFFN = FF / 8;
constexpr int CONS0 = 96;
constexpr u32 WS_E = 0;
constexpr u32 WS_ML = 4096;
constexpr u32 WS_Q = 8192;
constexpr u32 WS_H = WS_Q + NH * 8 * 2 * 64 * 16;
constexpr u32 WS_F = WS_H + NFFN * 64 * 16;
constexpr u32 WS_Y = WS_F + NFFN * 80 * 16;
constexpr u32 WS_END = WS_Y + NWG * 320 * 16;
constexpr u32 OOB = 0x7ffffff0u;
constexpr int AUX_NT = 2, AUX_SC1 = 16;
constexpr u32 SPIN_CAP = 1u << 16;

#define X_AUX 0
#define K_AUX 2
#define V_AUX 2
#define Y_SLEEP 2
#define VSIG 40
#define K_PRE 3
#define K_A 5
#define PACE 3
#define RING 3
#define BUMP 3
#define RING_END RING
#define RAMP_AT 96
#define RAMP_STEP 2
constexpr int N0 = K_N0, R = RING;
constexpr int target(int i) {
  int t = i + 1 + R;
  const int top = N0 + (i + 1 < R ? i + 1 : R);
  if (t < top) t = top;
  if (i >= 64 - BUMP && i < 64) t = i + 1 + R + (i - (64 - BUMP) + 1);
  if (i >= 64 && t < 64 + R + BUMP) t = 64 + R + BUMP;
  if (i >= RAMP_AT) { int rr = R + (i - RAMP_AT) / RAMP_STEP + 1; if (rr > RING_END) rr = RING_END; if (t < i + 1 + rr) t = i + 1 + rr; }
  return t > 128 ? 128 : t;
}

struct Params {
  const void* x; const void* k; const void* v; const void* wq; const void* wo;
  const void* w1; const void* b1; const void* w2; const void* b2; void* out; void* ws;
};

__device__ __forceinline__ rsrc_t mk_rsrc(const void* p, u32 bytes) { return __builtin_amdgcn_make_buffer_rsrc((void*)p, 0, (int)bytes, 0x00020000); }
template <int AUX> __device__ __forceinline__ u32x4 bld128(rsrc_t r, u32 voff, u32 soff) { return __builtin_amdgcn_raw_buffer_load_b128(r, (int)voff, (int)soff, AUX); }
template <int AUX> __device__ __forceinline__ u32 bld32(rsrc_t r, u32 voff, u32 soff) { return __builtin_amdgcn_raw_buffer_load_b32(r, (int)voff, (int)soff, AUX); }
__device__ __forceinline__ u32 bld16(rsrc_t r, u32 voff) { return (u32)__builtin_amdgcn_raw_buffer_load_b16(r, (int)voff, 0, 2); }
__device__ __forceinline__ float bflo(u32 x) { return __builtin_bit_cast(float, x << 16); }
__device__ __forceinline__ float bfhi(u32 x) { return __builtin_bit_cast(float, x & 0xffff0000u); }
__device__ __forceinline__ u32 pkbf(float lo, float hi) { bf16x2 v = {(__bf16)lo, (__bf16)hi}; return __builtin_bit_cast(u32, v); }
__device__ __forceinline__ float rbf(float x) { return bflo(pkbf(x, 0.f) & 0xffffu); }
__device__ __forceinline__ float dot2(u32 a, u32 b, float c) { return __builtin_amdgcn_fdot2_f32_bf16(__builtin_bit_cast(bf16x2, a), __builtin_bit_cast(bf16x2, b), c, false); }
template <int CTRL> __device__ __forceinline__ float dpp(float x) { return __builtin_bit_cast(float, __builtin_amdgcn_mov_dpp(__builtin_bit_cast(int, x), CTRL, 0xf, 0xf, true)); }
constexpr int XOR1 = 0xB1, XOR2 = 0x4E, ROR4 = 0x124, ROR8 = 0x128, ROR2 = 0x122, ROR1 = 0x121, BCAST = 0x150;
__device__ __forceinline__ float u2f(u32 x) { return __builtin_bit_cast(float, x); }
__device__ __forceinline__ u32 f2u(float x) { return __builtin_bit_cast(u32, x); }
__device__ __forceinline__ float tr32(float a, float b) { auto s = __builtin_amdgcn_permlane32_swap(f2u(a), f2u(b), false, false); return u2f(s[0]) + u2f(s[1]); }
__device__ __forceinline__ float tr16(float a, float b) { auto s = __builtin_amdgcn_permlane16_swap(f2u(a), f2u(b), false, false); return u2f(s[0]) + u2f(s[1]); }
__device__ __forceinline__ float rows_sum(float x) { x = tr16(x, x); return tr32(x, x); }
__device__ __forceinline__ float rows_max(float x) {
  auto s = __builtin_amdgcn_permlane16_swap(f2u(x), f2u(x), false, false); x = fmaxf(u2f(s[0]), u2f(s[1]));
  auto t = __builtin_amdgcn_permlane32_swap(f2u(x), f2u(x), false, false); return fmaxf(u2f(t[0]), u2f(t[1]));
}
__device__ __forceinline__ float row16_sum(float x) { x += dpp<ROR8>(x); x += dpp<ROR4>(x); x += dpp<ROR2>(x); x += dpp<ROR1>(x); return x; }
__device__ __forceinline__ float row16_max(float x) { x = fmaxf(x, dpp<ROR8>(x)); x = fmaxf(x, dpp<ROR4>(x)); x = fmaxf(x, dpp<ROR2>(x)); x = fmaxf(x, dpp<ROR1>(x)); return x; }
__device__ __forceinline__ float wave_sum(float x) { return rows_sum(row16_sum(x)); }
__device__ __forceinline__ float wave_max(float x) { return rows_max(row16_max(x)); }
template <int K> __device__ __forceinline__ float merge(float lo, float hi, int c) {
  const bool bit = (c >> K) & 1;
  const float keep = bit ? hi : lo, send = bit ? lo : hi;
  constexpr int CTRL = K == 0 ? XOR1 : K == 1 ? XOR2 : K == 2 ? ROR4 : ROR8;
  return keep + dpp<CTRL>(send);
}
template <int T> __device__ __forceinline__ float bcast(float x) { return dpp<BCAST + T>(x); }
#define SB() __builtin_amdgcn_sched_barrier(0)
template <int I> struct IC { static constexpr int value = I; };
template <int B, int E, class F> __device__ __forceinline__ void sfor(F&& f) { if constexpr (B < E) { f(IC<B>{}); sfor<B + 1, E>(f); } }
#define INL __attribute__((always_inline))

__device__ __forceinline__ bool tags_ok(const u32x4& g, u32 tA, u32 tB) { return g.y == tA && g.w == tB; }
template <int SLEEP = 2> __device__ __forceinline__ u32x4 wait_granule(u32x4 g, rsrc_t rs, u32 off, u32 tA, u32 tB, bool need) {
  bool ok = !need || tags_ok(g, tA, tB);
  if (!__all(ok)) {
#pragma unroll 1
    for (u32 spin = 0; spin < SPIN_CAP; ++spin) {
      if (!ok) { g = bld128<AUX_SC1>(rs, off, 0); ok = tags_ok(g, tA, tB); }
      if (__all(ok)) break;
      __builtin_amdgcn_s_sleep(SLEEP);
    }
  }
  return g;
}

template <int N> __device__ __forceinline__ void wait_granules(u32x4 (&g)[N], rsrc_t rs, u32 off, u32 stride, u32 tA, u32 tB) {
  bool ok = true;
#pragma unroll
  for (int k = 0; k < N; ++k) ok = ok && tags_ok(g[k], tA, tB);
  if (!__all(ok)) {
#pragma unroll 1
    for (u32 spin = 0; spin < SPIN_CAP; ++spin) {
#pragma unroll
      for (int k = 0; k < N; ++k) if (!tags_ok(g[k], tA, tB)) g[k] = bld128<AUX_SC1>(rs, off, k * stride);
      ok = true;
#pragma unroll
      for (int k = 0; k < N; ++k) ok = ok && tags_ok(g[k], tA, tB);
      if (__all(ok)) break;
      __builtin_amdgcn_s_sleep(POLL_SLEEP);
    }
  }
}
__device__ __forceinline__ void lds_wait(LAS unsigned char* word, u32 want) {
#pragma unroll 1
  for (u32 spin = 0; spin < (SPIN_CAP << 4); ++spin) {
    if (*(volatile LAS u32*)word >= want) break;
    __builtin_amdgcn_s_sleep(2);
  }
  asm volatile("" ::: "memory");
}

constexpr int L_XN = 0;
constexpr int L_QP = 2560;
constexpr int L_Q = L_QP + 4096;
constexpr int L_ML = L_Q + 256;
constexpr int L_CNT = L_ML + 64;
constexpr int L_TAG = L_CNT + 16;
constexpr int L_FW = L_CNT + 32;
constexpr int L_FP = L_FW + 64;
constexpr int L_YP = L_FP + 32;
constexpr int L_H2 = L_YP + 256;
constexpr int L_FR = L_H2 + 512;
constexpr int L_OA = L_FR + 3840;
constexpr int L_WO = L_OA + 16 * 272;
constexpr int L_TRASH = L_WO + 8 * 5120;
constexpr int L_END = L_TRASH + 1024;
static_assert(L_OA % 16 == 0 && L_WO % 16 == 0 && L_FW % 16 == 0 && L_FR % 16 == 0 && L_QP % 16 == 0, "LDS alignment");

#define STAMP(n) do {} while (0)

__global__ void __launch_bounds__(NTH, 3) fused_block(Params p) {
  __shared__ __attribute__((aligned(16))) unsigned char lds[L_END];
  const int tid = threadIdx.x, lane = tid & 63, w = __builtin_amdgcn_readfirstlane(tid >> 6), g4 = lane >> 4, c = lane & 15;
  const int bid = blockIdx.x, xc = bid & 7, rr = bid >> 3;
#define HMAP 0
  const int h = HMAP == 0 ? (rr >> 3) * 8 + xc : HMAP == 1 ? (rr >> 3) * 8 + ((xc + 2 * (rr >> 3)) & 7) : (rr >> 3) * 8 + ((xc + 3 * (rr >> 3)) & 7), b = (rr >> 2) & 1, s = rr & 3;
  const int pair = b * NH + h, prod = pair * 4 + s;
  const int fu = xc + 8 * (rr >> 3), fv = rr & 7;
  const bool is_ffn = fu < 27;
  const bool is_cons = bid >= CONS0;
  const int cj = bid - CONS0, cb = is_cons ? cj / 80 : 0, coct = is_cons ? cj % 80 : 0;
  STAMP(0);

  const rsrc_t rsWS = mk_rsrc(p.ws, WS_END);
  const rsrc_t rsK = mk_rsrc((const char*)p.k + (size_t)pair * (SK * DH * 2) + (size_t)s * (2048 * DH * 2), 2048 * DH * 2);
  const rsrc_t rsV = mk_rsrc((const char*)p.v + (size_t)pair * (SK * DH * 2) + (size_t)s * (2048 * DH * 2), 2048 * DH * 2);
  const rsrc_t rsX = mk_rsrc(p.x, 2 * DM * 2);
  const rsrc_t rsWQ = mk_rsrc(p.wq, DM * NH * DH * 2);
  const rsrc_t rsWO = mk_rsrc((const char*)p.wo + (size_t)h * (DH * DM * 2), DH * DM * 2);
  const rsrc_t rsW1 = mk_rsrc(p.w1, is_ffn ? DM * FF * 2 : 0);
  const rsrc_t rsW2 = mk_rsrc(p.w2, is_ffn ? FF * DM * 2 : 0);
  const rsrc_t rsB1 = mk_rsrc(p.b1, is_ffn ? FF * 2 : 0);
  const rsrc_t rsB2 = mk_rsrc(p.b2, DM * 2);

  auto ln_stats = [&](const u32x4& ra, u32 rb, float& mb, float& inv) INL {
    float v[10];
#pragma unroll
    for (int k = 0; k < 4; ++k) { v[2 * k] = bflo(ra[k]); v[2 * k + 1] = bfhi(ra[k]); }
    v[8] = bflo(rb); v[9] = bfhi(rb);
    float sum = 0.f;
#pragma unroll
    for (int k = 0; k < 10; ++k) sum += v[k];
    const float mean = wave_sum(sum) * (1.f / DM);
    float sq = 0.f;
#pragma unroll
    for (int k = 0; k < 10; ++k) { const float d = v[k] - mean; sq = fmaf(d, d, sq); }
    const float var = wave_sum(sq) * (1.f / DM);
    mb = rbf(mean);
    inv = 1.f / rbf(sqrtf(rbf(rbf(var) + 1e-5f)));
  };
  auto ln_row = [&](const u32x4& ra, u32 rb, float mb, float inv, int dst) INL {
#pragma unroll
    for (int k = 0; k < 5; ++k) {
      const u32 r = k < 4 ? ra[k & 3] : rb;
      const u32 d = pkbf(bflo(r) - mb, bfhi(r) - mb);
      *(u32*)(lds + dst + lane * 20 + 4 * k) = pkbf(bflo(d) * inv, bfhi(d) * inv);
    }
  };

  if (w >= 8) {
    const int hw = w - 8;
    if (hw == 0) { *(volatile LAS u32*)(LAS unsigned char*)(lds + L_CNT) = 0u; *(volatile LAS u32*)(LAS unsigned char*)(lds + L_CNT + 4) = 0u; *(volatile LAS u32*)(LAS unsigned char*)(lds + L_CNT + 8) = 0u; }
    __syncthreads();
    const u64 E = __hip_atomic_load((GAS u64*)p.ws, __ATOMIC_RELAXED, __HIP_MEMORY_SCOPE_AGENT);
    const u32x4 x0a = bld128<X_AUX>(rsX, lane * 20, 0), x1a = bld128<X_AUX>(rsX, 1280 + lane * 20, 0);
    const u32 x0b = bld32<X_AUX>(rsX, lane * 20 + 16, 0), x1b = bld32<X_AUX>(rsX, 1280 + lane * 20 + 16, 0);
    const int qrow = 80 * (4 * b + s) + 20 * hw;
    u32x4 WQ[5]; u32 xr0[5], xr1[5];
#pragma unroll
    for (int k = 0; k < 5; ++k) WQ[k] = bld128<WQ_AUX>(rsWQ, g4 * 8192 + c * 16, (qrow + 4 * k) * 8192 + h * 256);
#pragma unroll
    for (int k = 0; k < 5; ++k) { xr0[k] = __builtin_amdgcn_raw_buffer_load_b16(rsX, (qrow + 4 * k + g4) * 2, 0, X_AUX); xr1[k] = __builtin_amdgcn_raw_buffer_load_b16(rsX, 1280 + (qrow + 4 * k + g4) * 2, 0, X_AUX); }
    float mb0, inv0, mb1, inv1;
    ln_stats(x0a, x0b, mb0, inv0);
    ln_stats(x1a, x1b, mb1, inv1);
    STAMP(9);
    {
      float a[16];
#pragma unroll
      for (int k = 0; k < 16; ++k) a[k] = 0.f;
#pragma unroll
      for (int k = 0; k < 5; ++k) {
        const float x0 = rbf(rbf(bflo(xr0[k]) - mb0) * inv0), x1 = rbf(rbf(bflo(xr1[k]) - mb1) * inv1);
#pragma unroll
        for (int u = 0; u < 4; ++u) {
          const float lo = bflo(WQ[k][u]), hi = bfhi(WQ[k][u]);
          a[2 * u] = fmaf(x0, lo, a[2 * u]); a[2 * u + 1] = fmaf(x0, hi, a[2 * u + 1]);
          a[8 + 2 * u] = fmaf(x1, lo, a[8 + 2 * u]); a[8 + 2 * u + 1] = fmaf(x1, hi, a[8 + 2 * u + 1]);
        }
      }
#pragma unroll
      for (int e = 0; e < 8; ++e) a[e] = tr32(a[e], a[8 + e]);
      const f32x4 z = {tr16(a[0], a[4]), tr16(a[1], a[5]), tr16(a[2], a[6]), tr16(a[3], a[7])};
      *(f32x4*)(lds + L_QP + (hw * 256 + (g4 >> 1) * 128 + 8 * c + 4 * (g4 & 1)) * 4) = z;
      asm volatile("s_waitcnt lgkmcnt(0)" ::: "memory");
      if (lane == 0) __hip_atomic_fetch_add((LAS u32*)(LAS unsigned char*)(lds + L_CNT + 4), 1u, __ATOMIC_RELAXED, __HIP_MEMORY_SCOPE_WORKGROUP);
    }
    if (hw != 0) {
      u32 t = 0;
      if (hw == 1) {
        t ^= bld32<AUX_NT>(rsV, lane * TOUCH < 2048 * DH * 2 ? lane * TOUCH : OOB, 0);
        t ^= bld32<AUX_NT>(rsK, lane * TOUCH < 2048 * DH * 2 ? lane * TOUCH : OOB, 0);
      } else if (hw == 2) {
        t ^= bld32<AUX_NT>(rsWO, lane * TOUCH < DH * DM * 2 ? lane * TOUCH : OOB, 0);
        t ^= bld32<AUX_NT>(rsW1, lane * TOUCH < 80 * FF * 2 + TOUCH ? (80 * fv) * (FF * 2) + fu * 128 + lane * TOUCH : OOB, 0);
        t ^= bld32<AUX_NT>(rsW2, lane * TOUCH < 64 * DM * 2 + TOUCH ? (64 * fu) * (DM * 2) + lane * TOUCH : OOB, 0);
      } else {
        t ^= bld32<AUX_SC1>(rsWS, lane * 65536 < WS_END ? lane * 65536 : OOB, 0);
        t ^= bld16(rsB2, lane * 2) ^ bld16(rsB1, lane * 2);
        t ^= ((const GAS u32*)p.out)[0];
      }
      asm volatile("" :: "v"(t));
      return;
    }
    if (hw != 0) return;
    const u32 tA = (u32)E + 0x9E3779B9u, tB = (u32)(E >> 32) + 0x85EBCA6Bu;
    *(u32x2*)(lds + (lane == 0 ? L_TAG : L_TRASH + lane * 8)) = u32x2{tA, tB};
    lds_wait((LAS unsigned char*)(lds + L_CNT + 4), 4u);
    STAMP(7);
    {
      f32x2 t0 = {0.f, 0.f}, t1 = {0.f, 0.f};
#pragma unroll
      for (int k = 0; k < 4; ++k) { t0 += *(const f32x2*)(lds + L_QP + (k * 256 + 2 * lane) * 4); t1 += *(const f32x2*)(lds + L_QP + (k * 256 + 128 + 2 * lane) * 4); }
      const u32 offP = WS_Q + (((h * 8 + b * 4 + s) * 2) * 64 + lane) * 16;
      __builtin_amdgcn_raw_buffer_store_b128(u32x4{f2u(t0.x), tA, f2u(t0.y), tB}, rsWS, (int)offP, 0, AUX_SC1);
      __builtin_amdgcn_raw_buffer_store_b128(u32x4{f2u(t1.x), tA, f2u(t1.y), tB}, rsWS, (int)offP, 64 * 16, AUX_SC1);
      const u32 offQ = WS_Q + ((h * 8 * 2 + b) * 64 + lane) * 16;
      u32x4 g[8];
      if (Q_DELAY) __builtin_amdgcn_s_sleep(Q_DELAY);
#pragma unroll
      for (int k = 0; k < 8; ++k) g[k] = bld128<AUX_SC1>(rsWS, offQ, k * 2 * 64 * 16);
      STAMP(6);
      wait_granules<8>(g, rsWS, offQ, 2 * 64 * 16, tA, tB);
      float q0 = 0.f, q1 = 0.f;
#pragma unroll
      for (int k = 0; k < 8; ++k) { q0 += u2f(g[k].x); q1 += u2f(g[k].z); }
      *(u32*)(lds + L_Q + lane * 4) = pkbf(q0, q1);
    }
    STAMP(1);
    __syncthreads();
    const u32 B2v = bld16(rsB2, (8 * coct + (lane & 7)) * 2), XV = bld16(rsX, cb * 1280 + (8 * coct + (lane & 7)) * 2);
    ln_row(x0a, x0b, mb0, inv0, L_XN);
    ln_row(x1a, x1b, mb1, inv1, L_XN + 1280);
    if (is_ffn) {
      u32x4 W1[10];
#pragma unroll
      for (int k = 0; k < 10; ++k) W1[k] = bld128<W_AUX>(rsW1, (lane >> 3) * (FF * 2) + (lane & 7) * 16, (80 * fv + 8 * k) * (FF * 2) + fu * 128);
      SB();
      float hv[16];
#pragma unroll
      for (int k = 0; k < 16; ++k) hv[k] = 0.f;
#pragma unroll
      for (int k = 0; k < 10; ++k) {
        const int row = 80 * fv + 8 * k + (lane >> 3);
        const float x0 = bflo(*(const u16*)(lds + L_XN + row * 2)), x1 = bflo(*(const u16*)(lds + L_XN + 1280 + row * 2));
#pragma unroll
        for (int u = 0; u < 4; ++u) {
          const float lo = bflo(W1[k][u]), hi = bfhi(W1[k][u]);
          hv[2 * u] = fmaf(x0, lo, hv[2 * u]); hv[2 * u + 1] = fmaf(x0, hi, hv[2 * u + 1]);
          hv[8 + 2 * u] = fmaf(x1, lo, hv[8 + 2 * u]); hv[8 + 2 * u + 1] = fmaf(x1, hi, hv[8 + 2 * u + 1]);
        }
      }
      SB();
#pragma unroll
      for (int e = 0; e < 8; ++e) hv[e] = merge<3>(hv[e], hv[8 + e], c);
#pragma unroll
      for (int e = 0; e < 4; ++e) hv[e] = tr16(hv[e], hv[e + 4]);
      const float h0 = tr32(hv[0], hv[2]), h1 = tr32(hv[1], hv[3]);
      const u32 gi = ((lane >> 3) & 1) * 32 + 4 * (lane & 7) + 2 * ((lane >> 4) & 1) + (lane >> 5);
      __builtin_amdgcn_raw_buffer_store_b128(u32x4{f2u(h0), tA, f2u(h1), tB}, rsWS, (int)(WS_H + ((fu * 8 + fv) * 64 + gi) * 16), 0, AUX_SC1);
    }
    STAMP(2);
      lds_wait((LAS unsigned char*)(lds + L_CNT), 8u);
      const f32x2 ml = *(const f32x2*)(lds + L_ML + (lane & 7) * 8);
      const float mg = wave_max(ml.x);
      const float lg = row16_sum(c < 8 ? ml.y * __builtin_amdgcn_exp2f(ml.x - mg) : 0.f);
      __builtin_amdgcn_raw_buffer_store_b128(u32x4{f2u(mg), tA, f2u(lg), tB}, rsWS, (int)(lane == 0 ? WS_ML + prod * 16 : OOB), 0, AUX_SC1);
      STAMP(3);
    if (is_ffn) {
      u32x4 W2[11];
      const u32 B1v = bld32<W_AUX>(rsB1, (lane & 31) * 4, fu * 128);
      const int rg = lane / 10, ch = lane - 10 * rg;
#pragma unroll
      for (int k = 0; k < 11; ++k) W2[k] = bld128<W_AUX>(rsW2, (lane < 60 && 6 * k + rg < 64) ? rg * (DM * 2) + ch * 16 : OOB, (64 * fu + 6 * k) * (DM * 2) + fv * 160);
      SB();
      const u32 offH = WS_H + (fu * 8 * 64 + lane) * 16;
      u32x4 g[8];
#pragma unroll
      for (int k = 0; k < 8; ++k) g[k] = bld128<AUX_SC1>(rsWS, offH, k * 64 * 16);
      wait_granules<8>(g, rsWS, offH, 64 * 16, tA, tB);
      float s0 = 0.f, s1 = 0.f;
#pragma unroll
      for (int k = 0; k < 8; ++k) { s0 += u2f(g[k].x); s1 += u2f(g[k].z); }
      s0 = rbf(rbf(s0) + bflo(B1v)); s1 = rbf(rbf(s1) + bfhi(B1v));
      s0 = s0 > 0.f ? s0 : 0.f; s1 = s1 > 0.f ? s1 : 0.f;
      *(f32x2*)(lds + L_H2 + lane * 8) = f32x2{rbf(s0 * s0), rbf(s1 * s1)};
      float ov[16];
#pragma unroll
      for (int k = 0; k < 16; ++k) ov[k] = 0.f;
#pragma unroll
      for (int k = 0; k < 11; ++k) {
        const int r = (6 * k + rg) & 63;
        const float a0 = *(const float*)(lds + L_H2 + r * 4), a1 = *(const float*)(lds + L_H2 + 256 + r * 4);
#pragma unroll
        for (int u = 0; u < 4; ++u) {
          const float lo = bflo(W2[k][u]), hi = bfhi(W2[k][u]);
          ov[2 * u] = fmaf(a0, lo, ov[2 * u]); ov[2 * u + 1] = fmaf(a0, hi, ov[2 * u + 1]);
          ov[8 + 2 * u] = fmaf(a1, lo, ov[8 + 2 * u]); ov[8 + 2 * u + 1] = fmaf(a1, hi, ov[8 + 2 * u + 1]);
        }
      }
      unsigned char* fr = lds + (lane < 60 ? L_FR + lane * 64 : L_TRASH + (lane & 3) * 64);
#pragma unroll
      for (int k = 0; k < 4; ++k) *(f32x4*)(fr + 16 * k) = f32x4{ov[4 * k], ov[4 * k + 1], ov[4 * k + 2], ov[4 * k + 3]};
#pragma unroll
      for (int j = 0; j < 2; ++j) {
        const int gq = lane + 64 * j, gb = gq >= 40 ? 1 : 0, cp = (gq - 40 * gb) % 40;
        f32x2 t = {0.f, 0.f};
#pragma unroll
        for (int k = 0; k < 6; ++k) t += *(const f32x2*)(lds + L_FR + (k * 10 + (cp >> 2)) * 64 + (gb * 8 + 2 * (cp & 3)) * 4);
        __builtin_amdgcn_raw_buffer_store_b128(u32x4{f2u(t.x), tA, f2u(t.y), tB}, rsWS, (int)(gq < 80 ? WS_F + ((fu * 8 + fv) * 80 + gq) * 16 : OOB), 0, AUX_SC1);
      }
    }
    {
      const u32 offML = WS_ML + (pair * 4 + (lane & 3)) * 16;
      u32x4 g = bld128<AUX_SC1>(rsWS, offML, 0);
      g = wait_granule(g, rsWS, offML, tA, tB, true);
      const float mv = u2f(g.x), M = row16_max(mv);
      const float L = row16_sum(c < 4 ? u2f(g.z) * __builtin_amdgcn_exp2f(mv - M) : 0.f);
      *(float*)(lds + (lane < 16 ? L_FW : L_TRASH) + lane * 4) = __builtin_amdgcn_exp2f(ml.x - M) / L;
    }
    STAMP(4);
    float gate = 0.f;
    if (is_cons) {
      lds_wait((LAS unsigned char*)(lds + L_CNT + 8), 1u);
      u32x4 FG[2]; u32 offF[2];
      const int cv = coct / 10, cp0 = (coct - 10 * cv) * 4;
#pragma unroll
      for (int k = 0; k < 2; ++k) {
        const int idx = lane + 64 * k;
        offF[k] = idx < 108 ? WS_F + (((idx >> 2) * 8 + cv) * 80 + cb * 40 + cp0 + (idx & 3)) * 16 : OOB;
        FG[k] = bld128<AUX_SC1>(rsWS, offF[k], 0);
      }
      FG[0] = wait_granule(FG[0], rsWS, offF[0], tA, tB, true);
      FG[1] = wait_granule(FG[1], rsWS, offF[1], tA, tB, lane < 44);
      float f0 = u2f(FG[0].x) + (lane < 44 ? u2f(FG[1].x) : 0.f), f1 = u2f(FG[0].z) + (lane < 44 ? u2f(FG[1].z) : 0.f);
      f0 += dpp<ROR4>(f0); f1 += dpp<ROR4>(f1); f0 += dpp<ROR8>(f0); f1 += dpp<ROR8>(f1);
      f0 = rows_sum(f0); f1 = rows_sum(f1);
      *(f32x2*)(lds + (lane < 4 ? L_FP : L_TRASH) + lane * 8) = f32x2{f0, f1};
      const float fs = *(const float*)(lds + L_FP + (lane & 7) * 4);
      const float o = rbf(rbf(fs) + bflo(B2v));
      gate = rbf(o / (1.f + __expf(-o)));
    }
    STAMP(5);
    __syncthreads();
    if (!is_cons) return;
    u32x4 og;
    const u32 offO = WS_Y + ((128 * (cb ^ 1) + lane) * 320) * 16;
    __syncthreads();
    {
      float ys = 0.f;
#pragma unroll
      for (int k = 0; k < 8; ++k) ys += *(const float*)(lds + L_YP + (k * 8 + (lane & 7)) * 4);
      const float res = bflo(XV) + rbf(rbf(ys) * gate);
      if (lane < 8) ((GAS u16*)p.out)[cb * DM + 8 * coct + lane] = (u16)(pkbf(res, 0.f) & 0xffffu);
    }
    STAMP(8);
    if (bid == CONS0) {
      og = bld128<AUX_SC1>(rsWS, offO, 0);
      og = wait_granule(og, rsWS, offO, tA, tB, true);
      og = bld128<AUX_SC1>(rsWS, offO + 64 * 320 * 16, 0);
      og = wait_granule(og, rsWS, offO + 64 * 320 * 16, tA, tB, true);
      if (lane == 0) __hip_atomic_store((GAS u64*)p.ws, (u64)tA | ((u64)tB << 32), __ATOMIC_RELAXED, __HIP_MEMORY_SCOPE_AGENT);
    }
    return;
  }

  const u32 vKV = w * 4096 + lane * 16;
  u32x4 S[128];
  auto ldS = [&](int i) INL -> u32x4 { const int t = i & 63; return i < 64 ? bld128<K_AUX>(rsK, vKV + (t & 3) * 1024, (t >> 2) * 32768) : bld128<V_AUX>(rsV, vKV + (t & 3) * 1024, (t >> 2) * 32768); };
  sfor<0, PACE>([&](auto I) INL { S[decltype(I)::value] = ldS(decltype(I)::value); });
  SB();
  __syncthreads();
  sfor<PACE, N0>([&](auto I) INL {
    constexpr int n = decltype(I)::value;
    asm volatile("" :: "v"(S[n - PACE][0]));
    SB();
    S[n] = ldS(n);
    SB();
    if constexpr (n == 6) STAMP(9);
    if constexpr (n == 10) STAMP(10);
    if constexpr (n == 14) STAMP(11);
  });
  STAMP(6);
  __syncthreads();
  u32 q[4];
  { const u32x4 qq = *(const u32x4*)(lds + L_Q + c * 16); q[0] = qq[0]; q[1] = qq[1]; q[2] = qq[2]; q[3] = qq[3]; }
  STAMP(2);

  float sc[4], d0 = 0.f, m1a = 0.f, m2a = 0.f, m3a = 0.f, m1 = 0.f, m2 = 0.f;
  sfor<0, 64>([&](auto I) INL {
    constexpr int i = decltype(I)::value;
    float d = dot2(S[i][0], q[0], 0.f);
    asm volatile("" : "+v"(d));
    SB();
    sfor<(i == 0 ? N0 : target(i - 1)), target(i)>([&](auto N) INL { S[decltype(N)::value] = ldS(decltype(N)::value); });
    SB();
    d = dot2(S[i][1], q[1], d); d = dot2(S[i][2], q[2], d); d = dot2(S[i][3], q[3], d);
    asm volatile("" : "+v"(d));
    constexpr int t = i & 15;
    if ((t & 1) == 0) d0 = d;
    else {
      const float a1 = merge<0>(d0, d, c);
      if ((t & 2) == 0) m1a = a1;
      else {
        m1 = merge<1>(m1a, a1, c);
        if ((t & 4) == 0) m2a = m1;
        else {
          m2 = merge<2>(m2a, m1, c);
          if ((t & 8) == 0) m3a = m2;
          else sc[i >> 4] = merge<3>(m3a, m2, c);
        }
      }
    }
    SB();
  });
  STAMP(3);
  constexpr float C2 = 0.08838834764831845f * 1.4426950408889634f;
  float pr[4];
  {
#pragma unroll
    for (int j = 0; j < 4; ++j) sc[j] *= C2;
    const float mw = wave_max(fmaxf(fmaxf(sc[0], sc[1]), fmaxf(sc[2], sc[3])));
#pragma unroll
    for (int j = 0; j < 4; ++j) pr[j] = __builtin_amdgcn_exp2f(sc[j] - mw);
    const float lw = wave_sum((pr[0] + pr[1]) + (pr[2] + pr[3]));
    *(f32x2*)(lds + (lane == 0 ? L_ML + w * 8 : L_TRASH + lane * 8)) = f32x2{mw, lw};
    asm volatile("s_waitcnt lgkmcnt(0)" ::: "memory");
    if (lane == 0) __hip_atomic_fetch_add((LAS u32*)(LAS unsigned char*)(lds + L_CNT), 1u, __ATOMIC_RELAXED, __HIP_MEMORY_SCOPE_WORKGROUP);
  }
  SB();

  float acc[8];
#pragma unroll
  for (int k = 0; k < 8; ++k) acc[k] = 0.f;
  u32x4 WO[5], BF[4][5];
  u32 vWO[5];
#pragma unroll
  for (int k = 0; k < 5; ++k) { const int it = lane + 64 * k; vWO[k] = (it / 10) * (DM * 2) + (it % 10) * 16 + w * 160; }
  sfor<64, 128>([&](auto I) INL {
    constexpr int i = decltype(I)::value;
    constexpr int vi = i - 64, t = vi & 15;
    const u32x4 V = S[i];
    float v0 = bflo(V[0]);
    asm volatile("" : "+v"(v0));
    SB();
    sfor<target(i - 1), target(i)>([&](auto N) INL { S[decltype(N)::value] = ldS(decltype(N)::value); });
    if constexpr (vi == VSIG) { if (tid == 0) __hip_atomic_fetch_add((LAS u32*)(LAS unsigned char*)(lds + L_CNT + 8), 1u, __ATOMIC_RELAXED, __HIP_MEMORY_SCOPE_WORKGROUP); }
    if constexpr (vi >= 2 && vi < 50 && (vi - 2) % 12 < 10 && (vi - 2) % 2 == 0) { const int rho = (vi - 2) / 12, k = ((vi - 2) % 12) / 2;
 WO[k] = bld128<W_AUX>(rsWO, vWO[k], rho * 32 * DM * 2);
 }
    SB();
    {
      const float pb = bcast<t>(pr[vi >> 4]);
      acc[0] = fmaf(pb, v0, acc[0]); acc[1] = fmaf(pb, bfhi(V[0]), acc[1]);
#pragma unroll
      for (int u = 1; u < 4; ++u) { acc[2 * u] = fmaf(pb, bflo(V[u]), acc[2 * u]); acc[2 * u + 1] = fmaf(pb, bfhi(V[u]), acc[2 * u + 1]); }
      asm volatile("" : "+v"(acc[0]), "+v"(acc[1]), "+v"(acc[2]), "+v"(acc[3]), "+v"(acc[4]), "+v"(acc[5]), "+v"(acc[6]), "+v"(acc[7]));
    }
    if constexpr (vi >= 13 && vi <= 49 && (vi - 13) % 12 == 0) {
      unsigned char* st = lds + L_WO + w * 5120;
#pragma unroll
      for (int k = 0; k < 5; ++k) *(u32x4*)(st + (lane + 64 * k) * 16) = WO[k];
    }
    if constexpr (vi >= 14 && vi <= 50 && (vi - 14) % 12 == 0) {
      constexpr int rho = (vi - 14) / 12;
      LAS unsigned char* st = (LAS unsigned char*)(lds + L_WO + w * 5120);
#pragma unroll
      for (int n = 0; n < 5; ++n) {
        LAS unsigned char* a = st + (8 * g4 + (c >> 2)) * 160 + 32 * n + 8 * (c & 3);
        const u32x2 lo = __builtin_bit_cast(u32x2, __builtin_amdgcn_ds_read_tr16_b64_v4i16((LAS s16x4*)a));
        const u32x2 hi = __builtin_bit_cast(u32x2, __builtin_amdgcn_ds_read_tr16_b64_v4i16((LAS s16x4*)(a + 4 * 160)));
        BF[rho][n] = u32x4{lo.x, lo.y, hi.x, hi.y};
      }
    }
    if constexpr (vi >= 16 && vi <= 52 && (vi - 16) % 12 == 0) {
      constexpr int rho = (vi - 16) / 12;
#pragma unroll
      for (int n = 0; n < 5; ++n) asm volatile("" : "+v"(BF[rho][n]));
    }
    SB();
  });
  STAMP(4);

  {
    const float z0 = tr16(tr32(acc[0], acc[4]), tr32(acc[2], acc[6])), z1 = tr16(tr32(acc[1], acc[5]), tr32(acc[3], acc[7]));
    const u32 hi = pkbf(z0, z1), lo = pkbf(z0 - bflo(hi), z1 - bfhi(hi));
    *(u32*)(lds + L_OA + w * 272 + 16 * c + 4 * g4) = hi;
    *(u32*)(lds + L_OA + (8 + w) * 272 + 16 * c + 4 * g4) = lo;
  }
  __syncthreads();
  const u32x2 tg = *(const u32x2*)(lds + L_TAG);
  const u32 tA = tg.x, tB = tg.y;
  {
    f32x4 D[5];
#pragma unroll
    for (int n = 0; n < 5; ++n) D[n] = f32x4{0.f, 0.f, 0.f, 0.f};
    const f32x4 fr = *(const f32x4*)(lds + L_FW + 16 * g4);
#pragma unroll
    for (int rho = 0; rho < 4; ++rho) {
      const u32x4 A = *(const u32x4*)(lds + L_OA + c * 272 + 64 * rho + 16 * g4);
#pragma unroll
      for (int n = 0; n < 5; ++n) D[n] = __builtin_amdgcn_mfma_f32_16x16x32_bf16(__builtin_bit_cast(bf16x8, A), __builtin_bit_cast(bf16x8, BF[rho][n]), D[n], 0, 0, 0);
    }
    float e[5];
#pragma unroll
    for (int n = 0; n < 5; ++n) e[n] = fmaf(D[n][0], fr[0], D[n][1] * fr[1]) + fmaf(D[n][2], fr[2], D[n][3] * fr[3]);
    const float ya = tr16(tr32(e[0], e[2]), tr32(e[1], e[3]));
    const float yb = rows_sum(e[4]);
    const float yan = dpp<XOR1>(ya), ybn = dpp<XOR1>(yb);
    const bool odd = c & 1;
    const u32 cp = odd ? 40 * w + 32 + (c >> 1) : 40 * w + 8 * g4 + (c >> 1);
    const u32 off = (!odd || g4 == 0) ? WS_Y + (prod * 320 + cp) * 16 : OOB;
    const u32x4 gr = odd ? u32x4{f2u(ybn), tA, f2u(yb), tB} : u32x4{f2u(ya), tA, f2u(yan), tB};
    __builtin_amdgcn_raw_buffer_store_b128(gr, rsWS, (int)off, 0, AUX_SC1);
  }
  STAMP(5);
  if (!is_cons) return;

  const u32 offY = WS_Y + ((cb * 128 + (tid >> 2)) * 320 + 4 * coct + (tid & 3)) * 16;
  u32x4 YG = bld128<AUX_SC1>(rsWS, offY, 0);
  YG = wait_granule<Y_SLEEP>(YG, rsWS, offY, tA, tB, true);
  {
    float y0 = u2f(YG.x), y1 = u2f(YG.z);
    y0 += dpp<ROR4>(y0); y1 += dpp<ROR4>(y1); y0 += dpp<ROR8>(y0); y1 += dpp<ROR8>(y1);
    y0 = rows_sum(y0); y1 = rows_sum(y1);
    *(f32x2*)(lds + (lane < 4 ? L_YP + w * 32 : L_TRASH) + lane * 8) = f32x2{y0, y1};
  }
  __syncthreads();
  STAMP(8);
}

}

extern "C" void kernel_launch(void* const* d_in, const int* in_sizes, int n_in, void* d_out, int out_size, void* d_ws, size_t ws_size, hipStream_t stream) {
  (void)in_sizes; (void)n_in; (void)out_size; (void)ws_size;
  Params p;
  p.x = d_in[0]; p.k = d_in[1]; p.v = d_in[2]; p.wq = d_in[3]; p.wo = d_in[4];
  p.w1 = d_in[6]; p.b1 = d_in[7]; p.w2 = d_in[8]; p.b2 = d_in[9]; p.out = d_out;
  p.ws = d_ws;
  hipLaunchKernelGGL(fused_block, dim3(NWG), dim3(NTH), 0, stream, p);
}
```

```cpp
#define K_N0 18 Q_DELAY
#include <hip/hip_runtime.h>
#include <hip/hip_bf16.h>
#include <stdint.h>

namespace {

typedef unsigned u32;
typedef unsigned long long u64;
typedef unsigned short u16;
typedef u32 u32x4 __attribute__((ext_vector_type(4)));
typedef u32 u32x2 __attribute__((ext_vector_type(2)));
typedef float f32x4 __attribute__((ext_vector_type(4)));
typedef float f32x2 __attribute__((ext_vector_type(2)));
typedef short s16x4 __attribute__((ext_vector_type(4)));
typedef __bf16 bf16x2 __attribute__((ext_vector_type(2)));
typedef __bf16 bf16x8 __attribute__((ext_vector_type(8)));
typedef __amdgpu_buffer_rsrc_t rsrc_t;
#define GAS __attribute__((address_space(1)))
#define LAS __attribute__((address_space(3)))

constexpr int DM = 640, NH = 32, SK = 8192, DH = 128, FF = 1728;
constexpr int NWG = 256, NTH = 768;
constexpr int NFFN = FF / 8;
constexpr int CONS0 = 96;
constexpr u32 WS_E = 0;
constexpr u32 WS_ML = 4096;
constexpr u32 WS_Q = 8192;
constexpr u32 WS_H = WS_Q + NH * 8 * 2 * 64 * 16;
constexpr u32 WS_F = WS_H + NFFN * 64 * 16;
constexpr u32 WS_Y = WS_F + NFFN * 80 * 16;
constexpr u32 WS_END = WS_Y + NWG * 320 * 16;
constexpr u32 OOB = 0x7ffffff0u;
constexpr int AUX_NT = 2, AUX_SC1 = 16;
constexpr u32 SPIN_CAP = 1u << 16;

#define WQ_AUX 2
#define W_AUX 2
#define X_AUX 0
#define K_AUX 2
#define V_AUX 2
#define Y_SLEEP 2
#define VSIG 40
#define POLL_SLEEP 1
#define Q_DELAY 0
#define K_PRE 3
#define K_A 5
#define PACE 3
#define RING 3
#define BUMP 3
#define RING_END RING
#define RAMP_AT 96
#define RAMP_STEP 2
constexpr int N0 = K_N0, R = RING;
constexpr int target(int i) {
  int t = i + 1 + R;
  const int top = N0 + (i + 1 < R ? i + 1 : R);
  if (t < top) t = top;
  if (i >= 64 - BUMP && i < 64) t = i + 1 + R + (i - (64 - BUMP) + 1);
  if (i >= 64 && t < 64 + R + BUMP) t = 64 + R + BUMP;
  if (i >= RAMP_AT) { int rr = R + (i - RAMP_AT) / RAMP_STEP + 1; if (rr > RING_END) rr = RING_END; if (t < i + 1 + rr) t = i + 1 + rr; }
  return t > 128 ? 128 : t;
}

struct Params {
  const void* x; const void* k; const void* v; const void* wq; const void* wo;
  const void* w1; const void* b1; const void* w2; const void* b2; void* out; void* ws;
};

__device__ __forceinline__ rsrc_t mk_rsrc(const void* p, u32 bytes) { return __builtin_amdgcn_make_buffer_rsrc((void*)p, 0, (int)bytes, 0x00020000); }
template <int AUX> __device__ __forceinline__ u32x4 bld128(rsrc_t r, u32 voff, u32 soff) { return __builtin_amdgcn_raw_buffer_load_b128(r, (int)voff, (int)soff, AUX); }
template <int AUX> __device__ __forceinline__ u32 bld32(rsrc_t r, u32 voff, u32 soff) { return __builtin_amdgcn_raw_buffer_load_b32(r, (int)voff, (int)soff, AUX); }
__device__ __forceinline__ u32 bld16(rsrc_t r, u32 voff) { return (u32)__builtin_amdgcn_raw_buffer_load_b16(r, (int)voff, 0, 2); }
__device__ __forceinline__ float bflo(u32 x) { return __builtin_bit_cast(float, x << 16); }
__device__ __forceinline__ float bfhi(u32 x) { return __builtin_bit_cast(float, x & 0xffff0000u); }
__device__ __forceinline__ u32 pkbf(float lo, float hi) { bf16x2 v = {(__bf16)lo, (__bf16)hi}; return __builtin_bit_cast(u32, v); }
__device__ __forceinline__ float rbf(float x) { return bflo(pkbf(x, 0.f) & 0xffffu); }
__device__ __forceinline__ float dot2(u32 a, u32 b, float c) { return __builtin_amdgcn_fdot2_f32_bf16(__builtin_bit_cast(bf16x2, a), __builtin_bit_cast(bf16x2, b), c, false); }
template <int CTRL> __device__ __forceinline__ float dpp(float x) { return __builtin_bit_cast(float, __builtin_amdgcn_mov_dpp(__builtin_bit_cast(int, x), CTRL, 0xf, 0xf, true)); }
constexpr int XOR1 = 0xB1, XOR2 = 0x4E, ROR4 = 0x124, ROR8 = 0x128, ROR2 = 0x122, ROR1 = 0x121, BCAST = 0x150;
__device__ __forceinline__ float u2f(u32 x) { return __builtin_bit_cast(float, x); }
__device__ __forceinline__ u32 f2u(float x) { return __builtin_bit_cast(u32, x); }
__device__ __forceinline__ float tr32(float a, float b) { auto s = __builtin_amdgcn_permlane32_swap(f2u(a), f2u(b), false, false); return u2f(s[0]) + u2f(s[1]); }
__device__ __forceinline__ float tr16(float a, float b) { auto s = __builtin_amdgcn_permlane16_swap(f2u(a), f2u(b), false, false); return u2f(s[0]) + u2f(s[1]); }
__device__ __forceinline__ float rows_sum(float x) { x = tr16(x, x); return tr32(x, x); }
__device__ __forceinline__ float rows_max(float x) {
  auto s = __builtin_amdgcn_permlane16_swap(f2u(x), f2u(x), false, false); x = fmaxf(u2f(s[0]), u2f(s[1]));
  auto t = __builtin_amdgcn_permlane32_swap(f2u(x), f2u(x), false, false); return fmaxf(u2f(t[0]), u2f(t[1]));
}
__device__ __forceinline__ float row16_sum(float x) { x += dpp<ROR8>(x); x += dpp<ROR4>(x); x += dpp<ROR2>(x); x += dpp<ROR1>(x); return x; }
__device__ __forceinline__ float row16_max(float x) { x = fmaxf(x, dpp<ROR8>(x)); x = fmaxf(x, dpp<ROR4>(x)); x = fmaxf(x, dpp<ROR2>(x)); x = fmaxf(x, dpp<ROR1>(x)); return x; }
__device__ __forceinline__ float wave_sum(float x) { return rows_sum(row16_sum(x)); }
__device__ __forceinline__ float wave_max(float x) { return rows_max(row16_max(x)); }
template <int K> __device__ __forceinline__ float merge(float lo, float hi, int c) {
  const bool bit = (c >> K) & 1;
  const float keep = bit ? hi : lo, send = bit ? lo : hi;
  constexpr int CTRL = K == 0 ? XOR1 : K == 1 ? XOR2 : K == 2 ? ROR4 : ROR8;
  return keep + dpp<CTRL>(send);
}
template <int T> __device__ __forceinline__ float bcast(float x) { return dpp<BCAST + T>(x); }
#define SB() __builtin_amdgcn_sched_barrier(0)
template <int I> struct IC { static constexpr int value = I; };
template <int B, int E, class F> __device__ __forceinline__ void sfor(F&& f) { if constexpr (B < E) { f(IC<B>{}); sfor<B + 1, E>(f); } }
#define INL __attribute__((always_inline))

__device__ __forceinline__ bool tags_ok(const u32x4& g, u32 tA, u32 tB) { return g.y == tA && g.w == tB; }
template <int SLEEP = 2> __device__ __forceinline__ u32x4 wait_granule(u32x4 g, rsrc_t rs, u32 off, u32 tA, u32 tB, bool need) {
  bool ok = !need || tags_ok(g, tA, tB);
  if (!__all(ok)) {
#pragma unroll 1
    for (u32 spin = 0; spin < SPIN_CAP; ++spin) {
      if (!ok) { g = bld128<AUX_SC1>(rs, off, 0); ok = tags_ok(g, tA, tB); }
      if (__all(ok)) break;
      __builtin_amdgcn_s_sleep(SLEEP);
    }
  }
  return g;
}

template <int N> __device__ __forceinline__ void wait_granules(u32x4 (&g)[N], rsrc_t rs, u32 off, u32 stride, u32 tA, u32 tB) {
  bool ok = true;
#pragma unroll
  for (int k = 0; k < N; ++k) ok = ok && tags_ok(g[k], tA, tB);
  if (!__all(ok)) {
#pragma unroll 1
    for (u32 spin = 0; spin < SPIN_CAP; ++spin) {
#pragma unroll
      for (int k = 0; k < N; ++k) if (!tags_ok(g[k], tA, tB)) g[k] = bld128<AUX_SC1>(rs, off, k * stride);
      ok = true;
#pragma unroll
      for (int k = 0; k < N; ++k) ok = ok && tags_ok(g[k], tA, tB);
      if (__all(ok)) break;
      __builtin_amdgcn_s_sleep(POLL_SLEEP);
    }
  }
}
__device__ __forceinline__ void lds_wait(LAS unsigned char* word, u32 want) {
#pragma unroll 1
  for (u32 spin = 0; spin < (SPIN_CAP << 4); ++spin) {
    if (*(volatile LAS u32*)word >= want) break;
    __builtin_amdgcn_s_sleep(2);
  }
  asm volatile("" ::: "memory");
}

constexpr int L_XN = 0;
constexpr int L_QP = 2560;
constexpr int L_Q = L_QP + 4096;
constexpr int L_ML = L_Q + 256;
constexpr int L_CNT = L_ML + 64;
constexpr int L_TAG = L_CNT + 16;
constexpr int L_FW = L_CNT + 32;
constexpr int L_FP = L_FW + 64;
constexpr int L_YP = L_FP + 32;
constexpr int L_H2 = L_YP + 256;
constexpr int L_FR = L_H2 + 512;
constexpr int L_OA = L_FR + 3840;
constexpr int L_WO = L_OA + 16 * 272;
constexpr int L_TRASH = L_WO + 8 * 5120;
constexpr int L_END = L_TRASH + 1024;
static_assert(L_OA % 16 == 0 && L_WO % 16 == 0 && L_FW % 16 == 0 && L_FR % 16 == 0 && L_QP % 16 == 0, "LDS alignment");

#define STAMP(n) do {} while (0)

__global__ void __launch_bounds__(NTH, 3) fused_block(Params p) {
  __shared__ __attribute__((aligned(16))) unsigned char lds[L_END];
  const int tid = threadIdx.x, lane = tid & 63, w = __builtin_amdgcn_readfirstlane(tid >> 6), g4 = lane >> 4, c = lane & 15;
  const int bid = blockIdx.x, xc = bid & 7, rr = bid >> 3;
#define HMAP 0
  const int h = HMAP == 0 ? (rr >> 3) * 8 + xc : HMAP == 1 ? (rr >> 3) * 8 + ((xc + 2 * (rr >> 3)) & 7) : (rr >> 3) * 8 + ((xc + 3 * (rr >> 3)) & 7), b = (rr >> 2) & 1, s = rr & 3;
  const int pair = b * NH + h, prod = pair * 4 + s;
  const int fu = xc + 8 * (rr >> 3), fv = rr & 7;
  const bool is_ffn = fu < 27;
  const bool is_cons = bid >= CONS0;
  const int cj = bid - CONS0, cb = is_cons ? cj / 80 : 0, coct = is_cons ? cj % 80 : 0;
  STAMP(0);

  const rsrc_t rsWS = mk_rsrc(p.ws, WS_END);
  const rsrc_t rsK = mk_rsrc((const char*)p.k + (size_t)pair * (SK * DH * 2) + (size_t)s * (2048 * DH * 2), 2048 * DH * 2);
  const rsrc_t rsV = mk_rsrc((const char*)p.v + (size_t)pair * (SK * DH * 2) + (size_t)s * (2048 * DH * 2), 2048 * DH * 2);
  const rsrc_t rsX = mk_rsrc(p.x, 2 * DM * 2);
  const rsrc_t rsWQ = mk_rsrc(p.wq, DM * NH * DH * 2);
  const rsrc_t rsWO = mk_rsrc((const char*)p.wo + (size_t)h * (DH * DM * 2), DH * DM * 2);
  const rsrc_t rsW1 = mk_rsrc(p.w1, is_ffn ? DM * FF * 2 : 0);
  const rsrc_t rsW2 = mk_rsrc(p.w2, is_ffn ? FF * DM * 2 : 0);
  const rsrc_t rsB1 = mk_rsrc(p.b1, is_ffn ? FF * 2 : 0);
  const rsrc_t rsB2 = mk_rsrc(p.b2, DM * 2);

  auto ln_stats = [&](const u32x4& ra, u32 rb, float& mb, float& inv) INL {
    float v[10];
#pragma unroll
    for (int k = 0; k < 4; ++k) { v[2 * k] = bflo(ra[k]); v[2 * k + 1] = bfhi(ra[k]); }
    v[8] = bflo(rb); v[9] = bfhi(rb);
    float sum = 0.f;
#pragma unroll
    for (int k = 0; k < 10; ++k) sum += v[k];
    const float mean = wave_sum(sum) * (1.f / DM);
    float sq = 0.f;
#pragma unroll
    for (int k = 0; k < 10; ++k) { const float d = v[k] - mean; sq = fmaf(d, d, sq); }
    const float var = wave_sum(sq) * (1.f / DM);
    mb = rbf(mean);
    inv = 1.f / rbf(sqrtf(rbf(rbf(var) + 1e-5f)));
  };
  auto ln_row = [&](const u32x4& ra, u32 rb, float mb, float inv, int dst) INL {
#pragma unroll
    for (int k = 0; k < 5; ++k) {
      const u32 r = k < 4 ? ra[k & 3] : rb;
      const u32 d = pkbf(bflo(r) - mb, bfhi(r) - mb);
      *(u32*)(lds + dst + lane * 20 + 4 * k) = pkbf(bflo(d) * inv, bfhi(d) * inv);
    }
  };

  if (w >= 8) {
    const int hw = w - 8;
    if (hw == 0) { *(volatile LAS u32*)(LAS unsigned char*)(lds + L_CNT) = 0u; *(volatile LAS u32*)(LAS unsigned char*)(lds + L_CNT + 4) = 0u; *(volatile LAS u32*)(LAS unsigned char*)(lds + L_CNT + 8) = 0u; }
    __syncthreads();
    const u64 E = __hip_atomic_load((GAS u64*)p.ws, __ATOMIC_RELAXED, __HIP_MEMORY_SCOPE_AGENT);
    const u32x4 x0a = bld128<X_AUX>(rsX, lane * 20, 0), x1a = bld128<X_AUX>(rsX, 1280 + lane * 20, 0);
    const u32 x0b = bld32<X_AUX>(rsX, lane * 20 + 16, 0), x1b = bld32<X_AUX>(rsX, 1280 + lane * 20 + 16, 0);
    const int qrow = 80 * (4 * b + s) + 20 * hw;
    u32x4 WQ[5]; u32 xr0[5], xr1[5];
#pragma unroll
    for (int k = 0; k < 5; ++k) WQ[k] = bld128<WQ_AUX>(rsWQ, g4 * 8192 + c * 16, (qrow + 4 * k) * 8192 + h * 256);
#pragma unroll
    for (int k = 0; k < 5; ++k) { xr0[k] = __builtin_amdgcn_raw_buffer_load_b16(rsX, (qrow + 4 * k + g4) * 2, 0, X_AUX); xr1[k] = __builtin_amdgcn_raw_buffer_load_b16(rsX, 1280 + (qrow + 4 * k + g4) * 2, 0, X_AUX); }
    float mb0, inv0, mb1, inv1;
    ln_stats(x0a, x0b, mb0, inv0);
    ln_stats(x1a, x1b, mb1, inv1);
    STAMP(9);
    {
      float a[16];
#pragma unroll
      for (int k = 0; k < 16; ++k) a[k] = 0.f;
#pragma unroll
      for (int k = 0; k < 5; ++k) {
        const float x0 = rbf(rbf(bflo(xr0[k]) - mb0) * inv0), x1 = rbf(rbf(bflo(xr1[k]) - mb1) * inv1);
#pragma unroll
        for (int u = 0; u < 4; ++u) {
          const float lo = bflo(WQ[k][u]), hi = bfhi(WQ[k][u]);
          a[2 * u] = fmaf(x0, lo, a[2 * u]); a[2 * u + 1] = fmaf(x0, hi, a[2 * u + 1]);
          a[8 + 2 * u] = fmaf(x1, lo, a[8 + 2 * u]); a[8 + 2 * u + 1] = fmaf(x1, hi, a[8 + 2 * u + 1]);
        }
      }
#pragma unroll
      for (int e = 0; e < 8; ++e) a[e] = tr32(a[e], a[8 + e]);
      const f32x4 z = {tr16(a[0], a[4]), tr16(a[1], a[5]), tr16(a[2], a[6]), tr16(a[3], a[7])};
      *(f32x4*)(lds + L_QP + (hw * 256 + (g4 >> 1) * 128 + 8 * c + 4 * (g4 & 1)) * 4) = z;
      asm volatile("s_waitcnt lgkmcnt(0)" ::: "memory");
      if (lane == 0) __hip_atomic_fetch_add((LAS u32*)(LAS unsigned char*)(lds + L_CNT + 4), 1u, __ATOMIC_RELAXED, __HIP_MEMORY_SCOPE_WORKGROUP);
    }
    if (hw != 0) return;
    const u32 tA = (u32)E + 0x9E3779B9u, tB = (u32)(E >> 32) + 0x85EBCA6Bu;
    *(u32x2*)(lds + (lane == 0 ? L_TAG : L_TRASH + lane * 8)) = u32x2{tA, tB};
    lds_wait((LAS unsigned char*)(lds + L_CNT + 4), 4u);
    STAMP(7);
    {
      f32x2 t0 = {0.f, 0.f}, t1 = {0.f, 0.f};
#pragma unroll
      for (int k = 0; k < 4; ++k) { t0 += *(const f32x2*)(lds + L_QP + (k * 256 + 2 * lane) * 4); t1 += *(const f32x2*)(lds + L_QP + (k * 256 + 128 + 2 * lane) * 4); }
      const u32 offP = WS_Q + (((h * 8 + b * 4 + s) * 2) * 64 + lane) * 16;
      __builtin_amdgcn_raw_buffer_store_b128(u32x4{f2u(t0.x), tA, f2u(t0.y), tB}, rsWS, (int)offP, 0, AUX_SC1);
      __builtin_amdgcn_raw_buffer_store_b128(u32x4{f2u(t1.x), tA, f2u(t1.y), tB}, rsWS, (int)offP, 64 * 16, AUX_SC1);
      const u32 offQ = WS_Q + ((h * 8 * 2 + b) * 64 + lane) * 16;
      u32x4 g[8];
      if (Q_DELAY) __builtin_amdgcn_s_sleep(Q_DELAY);
#pragma unroll
      for (int k = 0; k < 8; ++k) g[k] = bld128<AUX_SC1>(rsWS, offQ, k * 2 * 64 * 16);
      STAMP(6);
      wait_granules<8>(g, rsWS, offQ, 2 * 64 * 16, tA, tB);
      float q0 = 0.f, q1 = 0.f;
#pragma unroll
      for (int k = 0; k < 8; ++k) { q0 += u2f(g[k].x); q1 += u2f(g[k].z); }
      *(u32*)(lds + L_Q + lane * 4) = pkbf(q0, q1);
    }
    STAMP(1);
    __syncthreads();
    const u32 B2v = bld16(rsB2, (8 * coct + (lane & 7)) * 2), XV = bld16(rsX, cb * 1280 + (8 * coct + (lane & 7)) * 2);
    ln_row(x0a, x0b, mb0, inv0, L_XN);
    ln_row(x1a, x1b, mb1, inv1, L_XN + 1280);
    if (is_ffn) {
      u32x4 W1[10];
#pragma unroll
      for (int k = 0; k < 10; ++k) W1[k] = bld128<W_AUX>(rsW1, (lane >> 3) * (FF * 2) + (lane & 7) * 16, (80 * fv + 8 * k) * (FF * 2) + fu * 128);
      SB();
      float hv[16];
#pragma unroll
      for (int k = 0; k < 16; ++k) hv[k] = 0.f;
#pragma unroll
      for (int k = 0; k < 10; ++k) {
        const int row = 80 * fv + 8 * k + (lane >> 3);
        const float x0 = bflo(*(const u16*)(lds + L_XN + row * 2)), x1 = bflo(*(const u16*)(lds + L_XN + 1280 + row * 2));
#pragma unroll
        for (int u = 0; u < 4; ++u) {
          const float lo = bflo(W1[k][u]), hi = bfhi(W1[k][u]);
          hv[2 * u] = fmaf(x0, lo, hv[2 * u]); hv[2 * u + 1] = fmaf(x0, hi, hv[2 * u + 1]);
          hv[8 + 2 * u] = fmaf(x1, lo, hv[8 + 2 * u]); hv[8 + 2 * u + 1] = fmaf(x1, hi, hv[8 + 2 * u + 1]);
        }
      }
      SB();
#pragma unroll
      for (int e = 0; e < 8; ++e) hv[e] = merge<3>(hv[e], hv[8 + e], c);
#pragma unroll
      for (int e = 0; e < 4; ++e) hv[e] = tr16(hv[e], hv[e + 4]);
      const float h0 = tr32(hv[0], hv[2]), h1 = tr32(hv[1], hv[3]);
      const u32 gi = ((lane >> 3) & 1) * 32 + 4 * (lane & 7) + 2 * ((lane >> 4) & 1) + (lane >> 5);
      __builtin_amdgcn_raw_buffer_store_b128(u32x4{f2u(h0), tA, f2u(h1), tB}, rsWS, (int)(WS_H + ((fu * 8 + fv) * 64 + gi) * 16), 0, AUX_SC1);
    }
    STAMP(2);
      lds_wait((LAS unsigned char*)(lds + L_CNT), 8u);
      const f32x2 ml = *(const f32x2*)(lds + L_ML + (lane & 7) * 8);
      const float mg = wave_max(ml.x);
      const float lg = row16_sum(c < 8 ? ml.y * __builtin_amdgcn_exp2f(ml.x - mg) : 0.f);
      __builtin_amdgcn_raw_buffer_store_b128(u32x4{f2u(mg), tA, f2u(lg), tB}, rsWS, (int)(lane == 0 ? WS_ML + prod * 16 : OOB), 0, AUX_SC1);
      STAMP(3);
    if (is_ffn) {
      u32x4 W2[11];
      const u32 B1v = bld32<W_AUX>(rsB1, (lane & 31) * 4, fu * 128);
      const int rg = lane / 10, ch = lane - 10 * rg;
#pragma unroll
      for (int k = 0; k < 11; ++k) W2[k] = bld128<W_AUX>(rsW2, (lane < 60 && 6 * k + rg < 64) ? rg * (DM * 2) + ch * 16 : OOB, (64 * fu + 6 * k) * (DM * 2) + fv * 160);
      SB();
      const u32 offH = WS_H + (fu * 8 * 64 + lane) * 16;
      u32x4 g[8];
#pragma unroll
      for (int k = 0; k < 8; ++k) g[k] = bld128<AUX_SC1>(rsWS, offH, k * 64 * 16);
      wait_granules<8>(g, rsWS, offH, 64 * 16, tA, tB);
      float s0 = 0.f, s1 = 0.f;
#pragma unroll
      for (int k = 0; k < 8; ++k) { s0 += u2f(g[k].x); s1 += u2f(g[k].z); }
      s0 = rbf(rbf(s0) + bflo(B1v)); s1 = rbf(rbf(s1) + bfhi(B1v));
      s0 = s0 > 0.f ? s0 : 0.f; s1 = s1 > 0.f ? s1 : 0.f;
      *(f32x2*)(lds + L_H2 + lane * 8) = f32x2{rbf(s0 * s0), rbf(s1 * s1)};
      float ov[16];
#pragma unroll
      for (int k = 0; k < 16; ++k) ov[k] = 0.f;
#pragma unroll
      for (int k = 0; k < 11; ++k) {
        const int r = (6 * k + rg) & 63;
        const float a0 = *(const float*)(lds + L_H2 + r * 4), a1 = *(const float*)(lds + L_H2 + 256 + r * 4);
#pragma unroll
        for (int u = 0; u < 4; ++u) {
          const float lo = bflo(W2[k][u]), hi = bfhi(W2[k][u]);
          ov[2 * u] = fmaf(a0, lo, ov[2 * u]); ov[2 * u + 1] = fmaf(a0, hi, ov[2 * u + 1]);
          ov[8 + 2 * u] = fmaf(a1, lo, ov[8 + 2 * u]); ov[8 + 2 * u + 1] = fmaf(a1, hi, ov[8 + 2 * u + 1]);
        }
      }
      unsigned char* fr = lds + (lane < 60 ? L_FR + lane * 64 : L_TRASH + (lane & 3) * 64);
#pragma unroll
      for (int k = 0; k < 4; ++k) *(f32x4*)(fr + 16 * k) = f32x4{ov[4 * k], ov[4 * k + 1], ov[4 * k + 2], ov[4 * k + 3]};
#pragma unroll
      for (int j = 0; j < 2; ++j) {
        const int gq = lane + 64 * j, gb = gq >= 40 ? 1 : 0, cp = (gq - 40 * gb) % 40;
        f32x2 t = {0.f, 0.f};
#pragma unroll
        for (int k = 0; k < 6; ++k) t += *(const f32x2*)(lds + L_FR + (k * 10 + (cp >> 2)) * 64 + (gb * 8 + 2 * (cp & 3)) * 4);
        __builtin_amdgcn_raw_buffer_store_b128(u32x4{f2u(t.x), tA, f2u(t.y), tB}, rsWS, (int)(gq < 80 ? WS_F + ((fu * 8 + fv) * 80 + gq) * 16 : OOB), 0, AUX_SC1);
      }
    }
    {
      const u32 offML = WS_ML + (pair * 4 + (lane & 3)) * 16;
      u32x4 g = bld128<AUX_SC1>(rsWS, offML, 0);
      g = wait_granule(g, rsWS, offML, tA, tB, true);
      const float mv = u2f(g.x), M = row16_max(mv);
      const float L = row16_sum(c < 4 ? u2f(g.z) * __builtin_amdgcn_exp2f(mv - M) : 0.f);
      *(float*)(lds + (lane < 16 ? L_FW : L_TRASH) + lane * 4) = __builtin_amdgcn_exp2f(ml.x - M) / L;
    }
    STAMP(4);
    float gate = 0.f;
    if (is_cons) {
      lds_wait((LAS unsigned char*)(lds + L_CNT + 8), 1u);
      u32x4 FG[2]; u32 offF[2];
      const int cv = coct / 10, cp0 = (coct - 10 * cv) * 4;
#pragma unroll
      for (int k = 0; k < 2; ++k) {
        const int idx = lane + 64 * k;
        offF[k] = idx < 108 ? WS_F + (((idx >> 2) * 8 + cv) * 80 + cb * 40 + cp0 + (idx & 3)) * 16 : OOB;
        FG[k] = bld128<AUX_SC1>(rsWS, offF[k], 0);
      }
      FG[0] = wait_granule(FG[0], rsWS, offF[0], tA, tB, true);
      FG[1] = wait_granule(FG[1], rsWS, offF[1], tA, tB, lane < 44);
      float f0 = u2f(FG[0].x) + (lane < 44 ? u2f(FG[1].x) : 0.f), f1 = u2f(FG[0].z) + (lane < 44 ? u2f(FG[1].z) : 0.f);
      f0 += dpp<ROR4>(f0); f1 += dpp<ROR4>(f1); f0 += dpp<ROR8>(f0); f1 += dpp<ROR8>(f1);
      f0 = rows_sum(f0); f1 = rows_sum(f1);
      *(f32x2*)(lds + (lane < 4 ? L_FP : L_TRASH) + lane * 8) = f32x2{f0, f1};
      const float fs = *(const float*)(lds + L_FP + (lane & 7) * 4);
      const float o = rbf(rbf(fs) + bflo(B2v));
      gate = rbf(o / (1.f + __expf(-o)));
    }
    STAMP(5);
    __syncthreads();
    if (!is_cons) return;
    u32x4 og;
    const u32 offO = WS_Y + ((128 * (cb ^ 1) + lane) * 320) * 16;
    __syncthreads();
    {
      float ys = 0.f;
#pragma unroll
      for (int k = 0; k < 8; ++k) ys += *(const float*)(lds + L_YP + (k * 8 + (lane & 7)) * 4);
      const float res = bflo(XV) + rbf(rbf(ys) * gate);
      if (lane < 8) ((GAS u16*)p.out)[cb * DM + 8 * coct + lane] = (u16)(pkbf(res, 0.f) & 0xffffu);
    }
    STAMP(8);
    if (bid == CONS0) {
      og = bld128<AUX_SC1>(rsWS, offO, 0);
      og = wait_granule(og, rsWS, offO, tA, tB, true);
      og = bld128<AUX_SC1>(rsWS, offO + 64 * 320 * 16, 0);
      og = wait_granule(og, rsWS, offO + 64 * 320 * 16, tA, tB, true);
      if (lane == 0) __hip_atomic_store((GAS u64*)p.ws, (u64)tA | ((u64)tB << 32), __ATOMIC_RELAXED, __HIP_MEMORY_SCOPE_AGENT);
    }
    return;
  }

  const u32 vKV = w * 4096 + lane * 16;
  u32x4 S[128];
  auto ldS = [&](int i) INL -> u32x4 { const int t = i & 63; return i < 64 ? bld128<K_AUX>(rsK, vKV + (t & 3) * 1024, (t >> 2) * 32768) : bld128<V_AUX>(rsV, vKV + (t & 3) * 1024, (t >> 2) * 32768); };
  sfor<0, PACE>([&](auto I) INL { S[decltype(I)::value] = ldS(decltype(I)::value); });
  SB();
  __syncthreads();
  sfor<PACE, N0>([&](auto I) INL {
    constexpr int n = decltype(I)::value;
    asm volatile("" :: "v"(S[n - PACE][0]));
    SB();
    S[n] = ldS(n);
    SB();
    if constexpr (n == 6) STAMP(9);
    if constexpr (n == 10) STAMP(10);
    if constexpr (n == 14) STAMP(11);
  });
  STAMP(6);
  __syncthreads();
  u32 q[4];
  { const u32x4 qq = *(const u32x4*)(lds + L_Q + c * 16); q[0] = qq[0]; q[1] = qq[1]; q[2] = qq[2]; q[3] = qq[3]; }
  STAMP(2);

  float sc[4], d0 = 0.f, m1a = 0.f, m2a = 0.f, m3a = 0.f, m1 = 0.f, m2 = 0.f;
  sfor<0, 64>([&](auto I) INL {
    constexpr int i = decltype(I)::value;
    float d = dot2(S[i][0], q[0], 0.f);
    asm volatile("" : "+v"(d));
    SB();
    sfor<(i == 0 ? N0 : target(i - 1)), target(i)>([&](auto N) INL { S[decltype(N)::value] = ldS(decltype(N)::value); });
    SB();
    d = dot2(S[i][1], q[1], d); d = dot2(S[i][2], q[2], d); d = dot2(S[i][3], q[3], d);
    asm volatile("" : "+v"(d));
    constexpr int t = i & 15;
    if ((t & 1) == 0) d0 = d;
    else {
      const float a1 = merge<0>(d0, d, c);
      if ((t & 2) == 0) m1a = a1;
      else {
        m1 = merge<1>(m1a, a1, c);
        if ((t & 4) == 0) m2a = m1;
        else {
          m2 = merge<2>(m2a, m1, c);
          if ((t & 8) == 0) m3a = m2;
          else sc[i >> 4] = merge<3>(m3a, m2, c);
        }
      }
    }
    SB();
  });
  STAMP(3);
  constexpr float C2 = 0.08838834764831845f * 1.4426950408889634f;
  float pr[4];
  {
#pragma unroll
    for (int j = 0; j < 4; ++j) sc[j] *= C2;
    const float mw = wave_max(fmaxf(fmaxf(sc[0], sc[1]), fmaxf(sc[2], sc[3])));
#pragma unroll
    for (int j = 0; j < 4; ++j) pr[j] = __builtin_amdgcn_exp2f(sc[j] - mw);
    const float lw = wave_sum((pr[0] + pr[1]) + (pr[2] + pr[3]));
    *(f32x2*)(lds + (lane == 0 ? L_ML + w * 8 : L_TRASH + lane * 8)) = f32x2{mw, lw};
    asm volatile("s_waitcnt lgkmcnt(0)" ::: "memory");
    if (lane == 0) __hip_atomic_fetch_add((LAS u32*)(LAS unsigned char*)(lds + L_CNT), 1u, __ATOMIC_RELAXED, __HIP_MEMORY_SCOPE_WORKGROUP);
  }
  SB();

  float acc[8];
#pragma unroll
  for (int k = 0; k < 8; ++k) acc[k] = 0.f;
  u32x4 WO[5], BF[4][5];
  u32 vWO[5];
#pragma unroll
  for (int k = 0; k < 5; ++k) { const int it = lane + 64 * k; vWO[k] = (it / 10) * (DM * 2) + (it % 10) * 16 + w * 160; }
  sfor<64, 128>([&](auto I) INL {
    constexpr int i = decltype(I)::value;
    constexpr int vi = i - 64, t = vi & 15;
    const u32x4 V = S[i];
    float v0 = bflo(V[0]);
    asm volatile("" : "+v"(v0));
    SB();
    sfor<target(i - 1), target(i)>([&](auto N) INL { S[decltype(N)::value] = ldS(decltype(N)::value); });
    if constexpr (vi == VSIG) { if (tid == 0) __hip_atomic_fetch_add((LAS u32*)(LAS unsigned char*)(lds + L_CNT + 8), 1u, __ATOMIC_RELAXED, __HIP_MEMORY_SCOPE_WORKGROUP); }
    if constexpr (vi >= 2 && vi < 50 && (vi - 2) % 12 < 10 && (vi - 2) % 2 == 0) { const int rho = (vi - 2) / 12, k = ((vi - 2) % 12) / 2;
 WO[k] = bld128<W_AUX>(rsWO, vWO[k], rho * 32 * DM * 2);
 }
    SB();
    {
      const float pb = bcast<t>(pr[vi >> 4]);
      acc[0] = fmaf(pb, v0, acc[0]); acc[1] = fmaf(pb, bfhi(V[0]), acc[1]);
#pragma unroll
      for (int u = 1; u < 4; ++u) { acc[2 * u] = fmaf(pb, bflo(V[u]), acc[2 * u]); acc[2 * u + 1] = fmaf(pb, bfhi(V[u]), acc[2 * u + 1]); }
      asm volatile("" : "+v"(acc[0]), "+v"(acc[1]), "+v"(acc[2]), "+v"(acc[3]), "+v"(acc[4]), "+v"(acc[5]), "+v"(acc[6]), "+v"(acc[7]));
    }
    if constexpr (vi >= 13 && vi <= 49 && (vi - 13) % 12 == 0) {
      unsigned char* st = lds + L_WO + w * 5120;
#pragma unroll
      for (int k = 0; k < 5; ++k) *(u32x4*)(st + (lane + 64 * k) * 16) = WO[k];
    }
    if constexpr (vi >= 14 && vi <= 50 && (vi - 14) % 12 == 0) {
      constexpr int rho = (vi - 14) / 12;
      LAS unsigned char* st = (LAS unsigned char*)(lds + L_WO + w * 5120);
#pragma unroll
      for (int n = 0; n < 5; ++n) {
        LAS unsigned char* a = st + (8 * g4 + (c >> 2)) * 160 + 32 * n + 8 * (c & 3);
        const u32x2 lo = __builtin_bit_cast(u32x2, __builtin_amdgcn_ds_read_tr16_b64_v4i16((LAS s16x4*)a));
        const u32x2 hi = __builtin_bit_cast(u32x2, __builtin_amdgcn_ds_read_tr16_b64_v4i16((LAS s16x4*)(a + 4 * 160)));
        BF[rho][n] = u32x4{lo.x, lo.y, hi.x, hi.y};
      }
    }
    if constexpr (vi >= 16 && vi <= 52 && (vi - 16) % 12 == 0) {
      constexpr int rho = (vi - 16) / 12;
#pragma unroll
      for (int n = 0; n < 5; ++n) asm volatile("" : "+v"(BF[rho][n]));
    }
    SB();
  });
  STAMP(4);

  {
    const float z0 = tr16(tr32(acc[0], acc[4]), tr32(acc[2], acc[6])), z1 = tr16(tr32(acc[1], acc[5]), tr32(acc[3], acc[7]));
    const u32 hi = pkbf(z0, z1), lo = pkbf(z0 - bflo(hi), z1 - bfhi(hi));
    *(u32*)(lds + L_OA + w * 272 + 16 * c + 4 * g4) = hi;
    *(u32*)(lds + L_OA + (8 + w) * 272 + 16 * c + 4 * g4) = lo;
  }
  __syncthreads();
  const u32x2 tg = *(const u32x2*)(lds + L_TAG);
  const u32 tA = tg.x, tB = tg.y;
  {
    f32x4 D[5];
#pragma unroll
    for (int n = 0; n < 5; ++n) D[n] = f32x4{0.f, 0.f, 0.f, 0.f};
    const f32x4 fr = *(const f32x4*)(lds + L_FW + 16 * g4);
#pragma unroll
    for (int rho = 0; rho < 4; ++rho) {
      const u32x4 A = *(const u32x4*)(lds + L_OA + c * 272 + 64 * rho + 16 * g4);
#pragma unroll
      for (int n = 0; n < 5; ++n) D[n] = __builtin_amdgcn_mfma_f32_16x16x32_bf16(__builtin_bit_cast(bf16x8, A), __builtin_bit_cast(bf16x8, BF[rho][n]), D[n], 0, 0, 0);
    }
    float e[5];
#pragma unroll
    for (int n = 0; n < 5; ++n) e[n] = fmaf(D[n][0], fr[0], D[n][1] * fr[1]) + fmaf(D[n][2], fr[2], D[n][3] * fr[3]);
    const float ya = tr16(tr32(e[0], e[2]), tr32(e[1], e[3]));
    const float yb = rows_sum(e[4]);
    const float yan = dpp<XOR1>(ya), ybn = dpp<XOR1>(yb);
    const bool odd = c & 1;
    const u32 cp = odd ? 40 * w + 32 + (c >> 1) : 40 * w + 8 * g4 + (c >> 1);
    const u32 off = (!odd || g4 == 0) ? WS_Y + (prod * 320 + cp) * 16 : OOB;
    const u32x4 gr = odd ? u32x4{f2u(ybn), tA, f2u(yb), tB} : u32x4{f2u(ya), tA, f2u(yan), tB};
    __builtin_amdgcn_raw_buffer_store_b128(gr, rsWS, (int)off, 0, AUX_SC1);
  }
  STAMP(5);
  if (!is_cons) return;

  const u32 offY = WS_Y + ((cb * 128 + (tid >> 2)) * 320 + 4 * coct + (tid & 3)) * 16;
  u32x4 YG = bld128<AUX_SC1>(rsWS, offY, 0);
  YG = wait_granule<Y_SLEEP>(YG, rsWS, offY, tA, tB, true);
  {
    float y0 = u2f(YG.x), y1 = u2f(YG.z);
    y0 += dpp<ROR4>(y0); y1 += dpp<ROR4>(y1); y0 += dpp<ROR8>(y0); y1 += dpp<ROR8>(y1);
    y0 = rows_sum(y0); y1 = rows_sum(y1);
    *(f32x2*)(lds + (lane < 4 ? L_YP + w * 32 : L_TRASH) + lane * 8) = f32x2{y0, y1};
  }
  __syncthreads();
  STAMP(8);
}

}

extern "C" void kernel_launch(void* const* d_in, const int* in_sizes, int n_in, void* d_out, int out_size, void* d_ws, size_t ws_size, hipStream_t stream) {
  (void)in_sizes; (void)n_in; (void)out_size; (void)ws_size;
  Params p;
  p.x = d_in[0]; p.k = d_in[1]; p.v = d_in[2]; p.wq = d_in[3]; p.wo = d_in[4];
  p.w1 = d_in[6]; p.b1 = d_in[7]; p.w2 = d_in[8]; p.b2 = d_in[9]; p.out = d_out;
  p.ws = d_ws;
  hipLaunchKernelGGL(fused_block, dim3(NWG), dim3(NTH), 0, stream, p);
}
```

```cpp
#define K_N0 21 Q_DELAY
#include <hip/hip_runtime.h>
#include <hip/hip_bf16.h>
#include <stdint.h>

namespace {

typedef unsigned u32;
typedef unsigned long long u64;
typedef unsigned short u16;
typedef u32 u32x4 __attribute__((ext_vector_type(4)));
typedef u32 u32x2 __attribute__((ext_vector_type(2)));
typedef float f32x4 __attribute__((ext_vector_type(4)));
typedef float f32x2 __attribute__((ext_vector_type(2)));
typedef short s16x4 __attribute__((ext_vector_type(4)));
typedef __bf16 bf16x2 __attribute__((ext_vector_type(2)));
typedef __bf16 bf16x8 __attribute__((ext_vector_type(8)));
typedef __amdgpu_buffer_rsrc_t rsrc_t;
#define GAS __attribute__((address_space(1)))
#define LAS __attribute__((address_space(3)))

constexpr int DM = 640, NH = 32, SK = 8192, DH = 128, FF = 1728;
constexpr int NWG = 256, NTH = 768;
constexpr int NFFN = FF / 8;
constexpr int CONS0 = 96;
constexpr u32 WS_E = 0;
constexpr u32 WS_ML = 4096;
constexpr u32 WS_Q = 8192;
constexpr u32 WS_H = WS_Q + NH * 8 * 2 * 64 * 16;
constexpr u32 WS_F = WS_H + NFFN * 64 * 16;
constexpr u32 WS_Y = WS_F + NFFN * 80 * 16;
constexpr u32 WS_END = WS_Y + NWG * 320 * 16;
constexpr u32 OOB = 0x7ffffff0u;
constexpr int AUX_NT = 2, AUX_SC1 = 16;
constexpr u32 SPIN_CAP = 1u << 16;

#define WQ_AUX 2
#define W_AUX 2
#define X_AUX 0
#define K_AUX 2
#define V_AUX 2
#define E_AUX 0
#define Y_SLEEP 2
#define VSIG 40
#define POLL_SLEEP 1
#define Q_DELAY 0
#define K_PRE 3
#define K_A 5
#define PACE 3
#define RING 3
#define BUMP 3
#define RING_END RING
#define RAMP_AT 96
#define RAMP_STEP 2
constexpr int N0 = K_N0, R = RING;
constexpr int target(int i) {
  int t = i + 1 + R;
  const int top = N0 + (i + 1 < R ? i + 1 : R);
  if (t < top) t = top;
  if (i >= 64 - BUMP && i < 64) t = i + 1 + R + (i - (64 - BUMP) + 1);
  if (i >= 64 && t < 64 + R + BUMP) t = 64 + R + BUMP;
  if (i >= RAMP_AT) { int rr = R + (i - RAMP_AT) / RAMP_STEP + 1; if (rr > RING_END) rr = RING_END; if (t < i + 1 + rr) t = i + 1 + rr; }
  return t > 128 ? 128 : t;
}

struct Params {
  const void* x; const void* k; const void* v; const void* wq; const void* wo;
  const void* w1; const void* b1; const void* w2; const void* b2; void* out; void* ws;
};

__device__ __forceinline__ rsrc_t mk_rsrc(const void* p, u32 bytes) { return __builtin_amdgcn_make_buffer_rsrc((void*)p, 0, (int)bytes, 0x00020000); }
template <int AUX> __device__ __forceinline__ u32x4 bld128(rsrc_t r, u32 voff, u32 soff) { return __builtin_amdgcn_raw_buffer_load_b128(r, (int)voff, (int)soff, AUX); }
template <int AUX> __device__ __forceinline__ u32 bld32(rsrc_t r, u32 voff, u32 soff) { return __builtin_amdgcn_raw_buffer_load_b32(r, (int)voff, (int)soff, AUX); }
__device__ __forceinline__ u32 bld16(rsrc_t r, u32 voff) { return (u32)__builtin_amdgcn_raw_buffer_load_b16(r, (int)voff, 0, 2); }
__device__ __forceinline__ float bflo(u32 x) { return __builtin_bit_cast(float, x << 16); }
__device__ __forceinline__ float bfhi(u32 x) { return __builtin_bit_cast(float, x & 0xffff0000u); }
__device__ __forceinline__ u32 pkbf(float lo, float hi) { bf16x2 v = {(__bf16)lo, (__bf16)hi}; return __builtin_bit_cast(u32, v); }
__device__ __forceinline__ float rbf(float x) { return bflo(pkbf(x, 0.f) & 0xffffu); }
__device__ __forceinline__ float dot2(u32 a, u32 b, float c) { return __builtin_amdgcn_fdot2_f32_bf16(__builtin_bit_cast(bf16x2, a), __builtin_bit_cast(bf16x2, b), c, false); }
template <int CTRL> __device__ __forceinline__ float dpp(float x) { return __builtin_bit_cast(float, __builtin_amdgcn_mov_dpp(__builtin_bit_cast(int, x), CTRL, 0xf, 0xf, true)); }
constexpr int XOR1 = 0xB1, XOR2 = 0x4E, ROR4 = 0x124, ROR8 = 0x128, ROR2 = 0x122, ROR1 = 0x121, BCAST = 0x150;
__device__ __forceinline__ float u2f(u32 x) { return __builtin_bit_cast(float, x); }
__device__ __forceinline__ u32 f2u(float x) { return __builtin_bit_cast(u32, x); }
__device__ __forceinline__ float tr32(float a, float b) { auto s = __builtin_amdgcn_permlane32_swap(f2u(a), f2u(b), false, false); return u2f(s[0]) + u2f(s[1]); }
__device__ __forceinline__ float tr16(float a, float b) { auto s = __builtin_amdgcn_permlane16_swap(f2u(a), f2u(b), false, false); return u2f(s[0]) + u2f(s[1]); }
__device__ __forceinline__ float rows_sum(float x) { x = tr16(x, x); return tr32(x, x); }
__device__ __forceinline__ float rows_max(float x) {
  auto s = __builtin_amdgcn_permlane16_swap(f2u(x), f2u(x), false, false); x = fmaxf(u2f(s[0]), u2f(s[1]));
  auto t = __builtin_amdgcn_permlane32_swap(f2u(x), f2u(x), false, false); return fmaxf(u2f(t[0]), u2f(t[1]));
}
__device__ __forceinline__ float row16_sum(float x) { x += dpp<ROR8>(x); x += dpp<ROR4>(x); x += dpp<ROR2>(x); x += dpp<ROR1>(x); return x; }
__device__ __forceinline__ float row16_max(float x) { x = fmaxf(x, dpp<ROR8>(x)); x = fmaxf(x, dpp<ROR4>(x)); x = fmaxf(x, dpp<ROR2>(x)); x = fmaxf(x, dpp<ROR1>(x)); return x; }
__device__ __forceinline__ float wave_sum(float x) { return rows_sum(row16_sum(x)); }
__device__ __forceinline__ float wave_max(float x) { return rows_max(row16_max(x)); }
template <int K> __device__ __forceinline__ float merge(float lo, float hi, int c) {
  const bool bit = (c >> K) & 1;
  const float keep = bit ? hi : lo, send = bit ? lo : hi;
  constexpr int CTRL = K == 0 ? XOR1 : K == 1 ? XOR2 : K == 2 ? ROR4 : ROR8;
  return keep + dpp<CTRL>(send);
}
template <int T> __device__ __forceinline__ float bcast(float x) { return dpp<BCAST + T>(x); }
#define SB() __builtin_amdgcn_sched_barrier(0)
template <int I> struct IC { static constexpr int value = I; };
template <int B, int E, class F> __device__ __forceinline__ void sfor(F&& f) { if constexpr (B < E) { f(IC<B>{}); sfor<B + 1, E>(f); } }
#define INL __attribute__((always_inline))

__device__ __forceinline__ bool tags_ok(const u32x4& g, u32 tA, u32 tB) { return g.y == tA && g.w == tB; }
template <int SLEEP = 2> __device__ __forceinline__ u32x4 wait_granule(u32x4 g, rsrc_t rs, u32 off, u32 tA, u32 tB, bool need) {
  bool ok = !need || tags_ok(g, tA, tB);
  if (!__all(ok)) {
#pragma unroll 1
    for (u32 spin = 0; spin < SPIN_CAP; ++spin) {
      if (!ok) { g = bld128<AUX_SC1>(rs, off, 0); ok = tags_ok(g, tA, tB); }
      if (__all(ok)) break;
      __builtin_amdgcn_s_sleep(SLEEP);
    }
  }
  return g;
}

template <int N> __device__ __forceinline__ void wait_granules(u32x4 (&g)[N], rsrc_t rs, u32 off, u32 stride, u32 tA, u32 tB) {
  bool ok = true;
#pragma unroll
  for (int k = 0; k < N; ++k) ok = ok && tags_ok(g[k], tA, tB);
  if (!__all(ok)) {
#pragma unroll 1
    for (u32 spin = 0; spin < SPIN_CAP; ++spin) {
#pragma unroll
      for (int k = 0; k < N; ++k) if (!tags_ok(g[k], tA, tB)) g[k] = bld128<AUX_SC1>(rs, off, k * stride);
      ok = true;
#pragma unroll
      for (int k = 0; k < N; ++k) ok = ok && tags_ok(g[k], tA, tB);
      if (__all(ok)) break;
      __builtin_amdgcn_s_sleep(POLL_SLEEP);
    }
  }
}
__device__ __forceinline__ void lds_wait(LAS unsigned char* word, u32 want) {
#pragma unroll 1
  for (u32 spin = 0; spin < (SPIN_CAP << 4); ++spin) {
    if (*(volatile LAS u32*)word >= want) break;
    __builtin_amdgcn_s_sleep(2);
  }
  asm volatile("" ::: "memory");
}

constexpr int L_XN = 0;
constexpr int L_QP = 2560;
constexpr int L_Q = L_QP + 4096;
constexpr int L_ML = L_Q + 256;
constexpr int L_CNT = L_ML + 64;
constexpr int L_TAG = L_CNT + 16;
constexpr int L_FW = L_CNT + 32;
constexpr int L_FP = L_FW + 64;
constexpr int L_YP = L_FP + 32;
constexpr int L_H2 = L_YP + 256;
constexpr int L_FR = L_H2 + 512;
constexpr int L_OA = L_FR + 3840;
constexpr int L_WO = L_OA + 16 * 272;
constexpr int L_TRASH = L_WO + 8 * 5120;
#define LDS_PAD 0
constexpr int L_END = L_TRASH + 1024 + LDS_PAD;
static_assert(L_OA % 16 == 0 && L_WO % 16 == 0 && L_FW % 16 == 0 && L_FR % 16 == 0 && L_QP % 16 == 0, "LDS alignment");

#define STAMP(n) do {} while (0)
#define STAMP_FLUSH() do {} while (0)

__global__ void __launch_bounds__(NTH, 3) fused_block(Params p) {
  __shared__ __attribute__((aligned(16))) unsigned char lds[L_END];
  const int tid = threadIdx.x, lane = tid & 63, w = __builtin_amdgcn_readfirstlane(tid >> 6), g4 = lane >> 4, c = lane & 15;
  const int bid = blockIdx.x, xc = bid & 7, rr = bid >> 3;
#define HMAP 0
  const int h = HMAP == 0 ? (rr >> 3) * 8 + xc : HMAP == 1 ? (rr >> 3) * 8 + ((xc + 2 * (rr >> 3)) & 7) : (rr >> 3) * 8 + ((xc + 3 * (rr >> 3)) & 7), b = (rr >> 2) & 1, s = rr & 3;
  const int pair = b * NH + h, prod = pair * 4 + s;
  const int fu = xc + 8 * (rr >> 3), fv = rr & 7;
  const bool is_ffn = fu < 27;
  const bool is_cons = bid >= CONS0;
  const int cj = bid - CONS0, cb = is_cons ? cj / 80 : 0, coct = is_cons ? cj % 80 : 0;
  STAMP(0);

  const rsrc_t rsWS = mk_rsrc(p.ws, WS_END);
  const rsrc_t rsK = mk_rsrc((const char*)p.k + (size_t)pair * (SK * DH * 2) + (size_t)s * (2048 * DH * 2), 2048 * DH * 2);
  const rsrc_t rsV = mk_rsrc((const char*)p.v + (size_t)pair * (SK * DH * 2) + (size_t)s * (2048 * DH * 2), 2048 * DH * 2);
  const rsrc_t rsX = mk_rsrc(p.x, 2 * DM * 2);
  const rsrc_t rsWQ = mk_rsrc(p.wq, DM * NH * DH * 2);
  const rsrc_t rsWO = mk_rsrc((const char*)p.wo + (size_t)h * (DH * DM * 2), DH * DM * 2);
  const rsrc_t rsW1 = mk_rsrc(p.w1, is_ffn ? DM * FF * 2 : 0);
  const rsrc_t rsW2 = mk_rsrc(p.w2, is_ffn ? FF * DM * 2 : 0);
  const rsrc_t rsFX = rsWS;
#define FX_WAIT 1
  const rsrc_t rsB1 = mk_rsrc(p.b1, is_ffn ? FF * 2 : 0);
  const rsrc_t rsB2 = mk_rsrc(p.b2, DM * 2);

  auto ln_stats = [&](const u32x4& ra, u32 rb, float& mb, float& inv) INL {
    float v[10];
#pragma unroll
    for (int k = 0; k < 4; ++k) { v[2 * k] = bflo(ra[k]); v[2 * k + 1] = bfhi(ra[k]); }
    v[8] = bflo(rb); v[9] = bfhi(rb);
    float sum = 0.f;
#pragma unroll
    for (int k = 0; k < 10; ++k) sum += v[k];
    const float mean = wave_sum(sum) * (1.f / DM);
    float sq = 0.f;
#pragma unroll
    for (int k = 0; k < 10; ++k) { const float d = v[k] - mean; sq = fmaf(d, d, sq); }
    const float var = wave_sum(sq) * (1.f / DM);
    mb = rbf(mean);
    inv = 1.f / rbf(sqrtf(rbf(rbf(var) + 1e-5f)));
  };
  auto ln_row = [&](const u32x4& ra, u32 rb, float mb, float inv, int dst) INL {
#pragma unroll
    for (int k = 0; k < 5; ++k) {
      const u32 r = k < 4 ? ra[k & 3] : rb;
      const u32 d = pkbf(bflo(r) - mb, bfhi(r) - mb);
      *(u32*)(lds + dst + lane * 20 + 4 * k) = pkbf(bflo(d) * inv, bfhi(d) * inv);
    }
  };

  if (w >= 8) {
    const int hw = w - 8;
    if (hw == 0) { *(volatile LAS u32*)(LAS unsigned char*)(lds + L_CNT) = 0u; *(volatile LAS u32*)(LAS unsigned char*)(lds + L_CNT + 4) = 0u; *(volatile LAS u32*)(LAS unsigned char*)(lds + L_CNT + 8) = 0u; }
    __syncthreads();
    const u32x4 x0a = bld128<X_AUX>(rsX, lane * 20, 0), x1a = bld128<X_AUX>(rsX, 1280 + lane * 20, 0);
    const u32 x0b = bld32<X_AUX>(rsX, lane * 20 + 16, 0), x1b = bld32<X_AUX>(rsX, 1280 + lane * 20 + 16, 0);
    const int qrow = 80 * (4 * b + s) + 20 * hw + 5 * g4;
    u32x4 WQ[5];
#pragma unroll
    for (int k = 0; k < 5; ++k) WQ[k] = bld128<WQ_AUX>(rsWQ, qrow * 8192 + c * 16, k * 8192 + h * 256);
    typedef u32 u32x3 __attribute__((ext_vector_type(3)));
    const u32x3 xr0 = __builtin_amdgcn_raw_buffer_load_b96(rsX, (qrow * 2) & ~3, 0, X_AUX), xr1 = __builtin_amdgcn_raw_buffer_load_b96(rsX, 1280 + ((qrow * 2) & ~3), 0, X_AUX);
    const u32x2 E2 = __builtin_bit_cast(u32x2, __builtin_amdgcn_raw_buffer_load_b64(rsWS, hw == 0 ? (int)WS_E : (int)OOB, 0, E_AUX));
    const u64 E = (u64)E2.x | ((u64)E2.y << 32);
    SB();
    float mb0, inv0, mb1, inv1;
    ln_stats(x0a, x0b, mb0, inv0);
    ln_stats(x1a, x1b, mb1, inv1);
    STAMP(9);
    {
      float a[16];
#pragma unroll
      for (int k = 0; k < 16; ++k) a[k] = 0.f;
#pragma unroll
      for (int k = 0; k < 5; ++k) {
        constexpr int dummy = 0; (void)dummy;
        const int e0 = k, e1 = k + 1;
        const float r0 = (qrow & 1) ? ((e1 & 1) ? bfhi(xr0[e1 >> 1]) : bflo(xr0[e1 >> 1])) : ((e0 & 1) ? bfhi(xr0[e0 >> 1]) : bflo(xr0[e0 >> 1]));
        const float r1 = (qrow & 1) ? ((e1 & 1) ? bfhi(xr1[e1 >> 1]) : bflo(xr1[e1 >> 1])) : ((e0 & 1) ? bfhi(xr1[e0 >> 1]) : bflo(xr1[e0 >> 1]));
        const float x0 = rbf(rbf(r0 - mb0) * inv0), x1 = rbf(rbf(r1 - mb1) * inv1);
#pragma unroll
        for (int u = 0; u < 4; ++u) {
          const float lo = bflo(WQ[k][u]), hi = bfhi(WQ[k][u]);
          a[2 * u] = fmaf(x0, lo, a[2 * u]); a[2 * u + 1] = fmaf(x0, hi, a[2 * u + 1]);
          a[8 + 2 * u] = fmaf(x1, lo, a[8 + 2 * u]); a[8 + 2 * u + 1] = fmaf(x1, hi, a[8 + 2 * u + 1]);
        }
      }
#pragma unroll
      for (int e = 0; e < 8; ++e) a[e] = tr32(a[e], a[8 + e]);
      const f32x4 z = {tr16(a[0], a[4]), tr16(a[1], a[5]), tr16(a[2], a[6]), tr16(a[3], a[7])};
      *(f32x4*)(lds + L_QP + (hw * 256 + (g4 >> 1) * 128 + 8 * c + 4 * (g4 & 1)) * 4) = z;
      asm volatile("s_waitcnt lgkmcnt(0)" ::: "memory");
      if (lane == 0) __hip_atomic_fetch_add((LAS u32*)(LAS unsigned char*)(lds + L_CNT + 4), 1u, __ATOMIC_RELAXED, __HIP_MEMORY_SCOPE_WORKGROUP);
    }
    if (hw != 0) return;
    const u32 tA = (u32)E + 0x9E3779B9u, tB = (u32)(E >> 32) + 0x85EBCA6Bu;
    *(u32x2*)(lds + (lane == 0 ? L_TAG : L_TRASH + lane * 8)) = u32x2{tA, tB};
    lds_wait((LAS unsigned char*)(lds + L_CNT + 4), 4u);
    STAMP(7);
    {
      f32x2 t0 = {0.f, 0.f}, t1 = {0.f, 0.f};
#pragma unroll
      for (int k = 0; k < 4; ++k) { t0 += *(const f32x2*)(lds + L_QP + (k * 256 + 2 * lane) * 4); t1 += *(const f32x2*)(lds + L_QP + (k * 256 + 128 + 2 * lane) * 4); }
      const u32 offP = WS_Q + (((h * 8 + b * 4 + s) * 2) * 64 + lane) * 16;
      __builtin_amdgcn_raw_buffer_store_b128(u32x4{f2u(t0.x), tA, f2u(t0.y), tB}, rsWS, (int)offP, 0, AUX_SC1);
      __builtin_amdgcn_raw_buffer_store_b128(u32x4{f2u(t1.x), tA, f2u(t1.y), tB}, rsWS, (int)offP, 64 * 16, AUX_SC1);
      const u32 offQ = WS_Q + ((h * 8 * 2 + b) * 64 + lane) * 16;
      u32x4 g[8];
      if (Q_DELAY) __builtin_amdgcn_s_sleep(Q_DELAY);
#pragma unroll
      for (int k = 0; k < 8; ++k) g[k] = bld128<AUX_SC1>(rsWS, offQ, k * 2 * 64 * 16);
      STAMP(6);
      wait_granules<8>(g, rsWS, offQ, 2 * 64 * 16, tA, tB);
      float q0 = 0.f, q1 = 0.f;
#pragma unroll
      for (int k = 0; k < 8; ++k) { q0 += u2f(g[k].x); q1 += u2f(g[k].z); }
      *(u32*)(lds + L_Q + lane * 4) = pkbf(q0, q1);
    }
    STAMP(1);
    __syncthreads();
    const u32 B2v = bld16(rsB2, (8 * coct + (lane & 7)) * 2), XV = bld16(rsX, cb * 1280 + (8 * coct + (lane & 7)) * 2);
    ln_row(x0a, x0b, mb0, inv0, L_XN);
    ln_row(x1a, x1b, mb1, inv1, L_XN + 1280);
    if (is_ffn) {
      u32x4 W1[10];
#pragma unroll
      for (int k = 0; k < 10; ++k) W1[k] = bld128<W_AUX>(rsW1, (lane >> 3) * (FF * 2) + (lane & 7) * 16, (80 * fv + 8 * k) * (FF * 2) + fu * 128);
      SB();
      float hv[16];
#pragma unroll
      for (int k = 0; k < 16; ++k) hv[k] = 0.f;
#pragma unroll
      for (int k = 0; k < 10; ++k) {
        const int row = 80 * fv + 8 * k + (lane >> 3);
        const float x0 = bflo(*(const u16*)(lds + L_XN + row * 2)), x1 = bflo(*(const u16*)(lds + L_XN + 1280 + row * 2));
#pragma unroll
        for (int u = 0; u < 4; ++u) {
          const float lo = bflo(W1[k][u]), hi = bfhi(W1[k][u]);
          hv[2 * u] = fmaf(x0, lo, hv[2 * u]); hv[2 * u + 1] = fmaf(x0, hi, hv[2 * u + 1]);
          hv[8 + 2 * u] = fmaf(x1, lo, hv[8 + 2 * u]); hv[8 + 2 * u + 1] = fmaf(x1, hi, hv[8 + 2 * u + 1]);
        }
      }
      SB();
#pragma unroll
      for (int e = 0; e < 8; ++e) hv[e] = merge<3>(hv[e], hv[8 + e], c);
#pragma unroll
      for (int e = 0; e < 4; ++e) hv[e] = tr16(hv[e], hv[e + 4]);
      const float h0 = tr32(hv[0], hv[2]), h1 = tr32(hv[1], hv[3]);
      const u32 gi = ((lane >> 3) & 1) * 32 + 4 * (lane & 7) + 2 * ((lane >> 4) & 1) + (lane >> 5);
      __builtin_amdgcn_raw_buffer_store_b128(u32x4{f2u(h0), tA, f2u(h1), tB}, rsFX, (int)(WS_H + ((fu * 8 + fv) * 64 + gi) * 16), 0, AUX_SC1);
    }
    STAMP(2);
      lds_wait((LAS unsigned char*)(lds + L_CNT), 8u);
      const f32x2 ml = *(const f32x2*)(lds + L_ML + (lane & 7) * 8);
      const float mg = wave_max(ml.x);
      const float lg = row16_sum(c < 8 ? ml.y * __builtin_amdgcn_exp2f(ml.x - mg) : 0.f);
      __builtin_amdgcn_raw_buffer_store_b128(u32x4{f2u(mg), tA, f2u(lg), tB}, rsWS, (int)(lane == 0 ? WS_ML + prod * 16 : OOB), 0, AUX_SC1);
      STAMP(3);
    if (is_ffn) {
      u32x4 W2[11];
      const u32 B1v = bld32<W_AUX>(rsB1, (lane & 31) * 4, fu * 128);
      const int rg = lane / 10, ch = lane - 10 * rg;
#pragma unroll
      for (int k = 0; k < 11; ++k) W2[k] = bld128<W_AUX>(rsW2, (lane < 60 && 6 * k + rg < 64) ? rg * (DM * 2) + ch * 16 : OOB, (64 * fu + 6 * k) * (DM * 2) + fv * 160);
      SB();
      const u32 offH = WS_H + (fu * 8 * 64 + lane) * 16;
      u32x4 g[8];
#pragma unroll
      for (int k = 0; k < 8; ++k) g[k] = bld128<AUX_SC1>(rsFX, offH, k * 64 * 16);
      if (FX_WAIT) wait_granules<8>(g, rsWS, offH, 64 * 16, tA, tB);
      float s0 = 0.f, s1 = 0.f;
#pragma unroll
      for (int k = 0; k < 8; ++k) { s0 += u2f(g[k].x); s1 += u2f(g[k].z); }
      s0 = rbf(rbf(s0) + bflo(B1v)); s1 = rbf(rbf(s1) + bfhi(B1v));
      s0 = s0 > 0.f ? s0 : 0.f; s1 = s1 > 0.f ? s1 : 0.f;
      *(f32x2*)(lds + L_H2 + lane * 8) = f32x2{rbf(s0 * s0), rbf(s1 * s1)};
      float ov[16];
#pragma unroll
      for (int k = 0; k < 16; ++k) ov[k] = 0.f;
#pragma unroll
      for (int k = 0; k < 11; ++k) {
        const int r = (6 * k + rg) & 63;
        const float a0 = *(const float*)(lds + L_H2 + r * 4), a1 = *(const float*)(lds + L_H2 + 256 + r * 4);
#pragma unroll
        for (int u = 0; u < 4; ++u) {
          const float lo = bflo(W2[k][u]), hi = bfhi(W2[k][u]);
          ov[2 * u] = fmaf(a0, lo, ov[2 * u]); ov[2 * u + 1] = fmaf(a0, hi, ov[2 * u + 1]);
          ov[8 + 2 * u] = fmaf(a1, lo, ov[8 + 2 * u]); ov[8 + 2 * u + 1] = fmaf(a1, hi, ov[8 + 2 * u + 1]);
        }
      }
      unsigned char* fr = lds + (lane < 60 ? L_FR + lane * 64 : L_TRASH + (lane & 3) * 64);
#pragma unroll
      for (int k = 0; k < 4; ++k) *(f32x4*)(fr + 16 * k) = f32x4{ov[4 * k], ov[4 * k + 1], ov[4 * k + 2], ov[4 * k + 3]};
#pragma unroll
      for (int j = 0; j < 2; ++j) {
        const int gq = lane + 64 * j, gb = gq >= 40 ? 1 : 0, cp = (gq - 40 * gb) % 40;
        f32x2 t = {0.f, 0.f};
#pragma unroll
        for (int k = 0; k < 6; ++k) t += *(const f32x2*)(lds + L_FR + (k * 10 + (cp >> 2)) * 64 + (gb * 8 + 2 * (cp & 3)) * 4);
        __builtin_amdgcn_raw_buffer_store_b128(u32x4{f2u(t.x), tA, f2u(t.y), tB}, rsFX, (int)(gq < 80 ? WS_F + ((fu * 8 + fv) * 80 + gq) * 16 : OOB), 0, AUX_SC1);
      }
    }
    {
      const u32 offML = WS_ML + (pair * 4 + (lane & 3)) * 16;
      u32x4 g = bld128<AUX_SC1>(rsWS, offML, 0);
      g = wait_granule(g, rsWS, offML, tA, tB, true);
      const float mv = u2f(g.x), M = row16_max(mv);
      const float L = row16_sum(c < 4 ? u2f(g.z) * __builtin_amdgcn_exp2f(mv - M) : 0.f);
      *(float*)(lds + (lane < 16 ? L_FW : L_TRASH) + lane * 4) = __builtin_amdgcn_exp2f(ml.x - M) / L;
    }
    STAMP(4);
    float gate = 0.f;
    if (is_cons) {
      lds_wait((LAS unsigned char*)(lds + L_CNT + 8), 1u);
      u32x4 FG[2]; u32 offF[2];
      const int cv = coct / 10, cp0 = (coct - 10 * cv) * 4;
#pragma unroll
      for (int k = 0; k < 2; ++k) {
        const int idx = lane + 64 * k;
        offF[k] = idx < 108 ? WS_F + (((idx >> 2) * 8 + cv) * 80 + cb * 40 + cp0 + (idx & 3)) * 16 : OOB;
        FG[k] = bld128<AUX_SC1>(rsFX, offF[k], 0);
      }
      FG[0] = wait_granule(FG[0], rsWS, offF[0], tA, tB, FX_WAIT);
      FG[1] = wait_granule(FG[1], rsWS, offF[1], tA, tB, FX_WAIT && lane < 44);
      float f0 = u2f(FG[0].x) + (lane < 44 ? u2f(FG[1].x) : 0.f), f1 = u2f(FG[0].z) + (lane < 44 ? u2f(FG[1].z) : 0.f);
      f0 += dpp<ROR4>(f0); f1 += dpp<ROR4>(f1); f0 += dpp<ROR8>(f0); f1 += dpp<ROR8>(f1);
      f0 = rows_sum(f0); f1 = rows_sum(f1);
      *(f32x2*)(lds + (lane < 4 ? L_FP : L_TRASH) + lane * 8) = f32x2{f0, f1};
      const float fs = *(const float*)(lds + L_FP + (lane & 7) * 4);
      const float o = rbf(rbf(fs) + bflo(B2v));
      gate = rbf(o / (1.f + __expf(-o)));
    }
    STAMP(5);
    __syncthreads();
    if (!is_cons) { STAMP_FLUSH(); return; }
    u32x4 og;
    const u32 offO = WS_Y + ((128 * (cb ^ 1) + lane) * 320) * 16;
    __syncthreads();
    {
      float ys = 0.f;
#pragma unroll
      for (int k = 0; k < 8; ++k) ys += *(const float*)(lds + L_YP + (k * 8 + (lane & 7)) * 4);
      const float res = bflo(XV) + rbf(rbf(ys) * gate);
      if (lane < 8) ((GAS u16*)p.out)[cb * DM + 8 * coct + lane] = (u16)(pkbf(res, 0.f) & 0xffffu);
    }
    STAMP(8);
    if (bid == CONS0) {
      og = bld128<AUX_SC1>(rsWS, offO, 0);
      og = wait_granule(og, rsWS, offO, tA, tB, true);
      og = bld128<AUX_SC1>(rsWS, offO + 64 * 320 * 16, 0);
      og = wait_granule(og, rsWS, offO + 64 * 320 * 16, tA, tB, true);
      if (lane == 0) __hip_atomic_store((GAS u64*)p.ws, (u64)tA | ((u64)tB << 32), __ATOMIC_RELAXED, __HIP_MEMORY_SCOPE_AGENT);
    }
    STAMP_FLUSH();
    return;
  }

  const u32 vKV = w * 4096 + lane * 16;
  u32x4 S[128];
  auto ldS = [&](int i) INL -> u32x4 { const int t = i & 63; return i < 64 ? bld128<K_AUX>(rsK, vKV + (t & 3) * 1024, (t >> 2) * 32768) : bld128<V_AUX>(rsV, vKV + (t & 3) * 1024, (t >> 2) * 32768); };
  sfor<0, PACE>([&](auto I) INL { S[decltype(I)::value] = ldS(decltype(I)::value); });
  SB();
  __syncthreads();
  sfor<PACE, N0>([&](auto I) INL {
    constexpr int n = decltype(I)::value;
    asm volatile("" :: "v"(S[n - PACE][0]));
    SB();
    S[n] = ldS(n);
    SB();
    if constexpr (n == 6) STAMP(9);
    if constexpr (n == 10) STAMP(10);
    if constexpr (n == 14) STAMP(11);
  });
  STAMP(6);
  __syncthreads();
  u32 q[4];
  { const u32x4 qq = *(const u32x4*)(lds + L_Q + c * 16); q[0] = qq[0]; q[1] = qq[1]; q[2] = qq[2]; q[3] = qq[3]; }
  STAMP(2);

  float sc[4], d0 = 0.f, m1a = 0.f, m2a = 0.f, m3a = 0.f, m1 = 0.f, m2 = 0.f;
  sfor<0, 64>([&](auto I) INL {
    constexpr int i = decltype(I)::value;
    float d = dot2(S[i][0], q[0], 0.f);
    asm volatile("" : "+v"(d));
    SB();
    sfor<(i == 0 ? N0 : target(i - 1)), target(i)>([&](auto N) INL { S[decltype(N)::value] = ldS(decltype(N)::value); });
    SB();
    d = dot2(S[i][1], q[1], d); d = dot2(S[i][2], q[2], d); d = dot2(S[i][3], q[3], d);
    asm volatile("" : "+v"(d));
    constexpr int t = i & 15;
    if ((t & 1) == 0) d0 = d;
    else {
      const float a1 = merge<0>(d0, d, c);
      if ((t & 2) == 0) m1a = a1;
      else {
        m1 = merge<1>(m1a, a1, c);
        if ((t & 4) == 0) m2a = m1;
        else {
          m2 = merge<2>(m2a, m1, c);
          if ((t & 8) == 0) m3a = m2;
          else sc[i >> 4] = merge<3>(m3a, m2, c);
        }
      }
    }
    SB();
  });
  STAMP(3);
  constexpr float C2 = 0.08838834764831845f * 1.4426950408889634f;
  float pr[4];
  {
#pragma unroll
    for (int j = 0; j < 4; ++j) sc[j] *= C2;
    const float mw = wave_max(fmaxf(fmaxf(sc[0], sc[1]), fmaxf(sc[2], sc[3])));
#pragma unroll
    for (int j = 0; j < 4; ++j) pr[j] = __builtin_amdgcn_exp2f(sc[j] - mw);
    const float lw = wave_sum((pr[0] + pr[1]) + (pr[2] + pr[3]));
    *(f32x2*)(lds + (lane == 0 ? L_ML + w * 8 : L_TRASH + lane * 8)) = f32x2{mw, lw};
    asm volatile("s_waitcnt lgkmcnt(0)" ::: "memory");
    if (lane == 0) __hip_atomic_fetch_add((LAS u32*)(LAS unsigned char*)(lds + L_CNT), 1u, __ATOMIC_RELAXED, __HIP_MEMORY_SCOPE_WORKGROUP);
  }
  SB();

  float acc[8];
#pragma unroll
  for (int k = 0; k < 8; ++k) acc[k] = 0.f;
  u32x4 WO[5], BF[4][5];
  u32 vWO[5];
#pragma unroll
  for (int k = 0; k < 5; ++k) { const int it = lane + 64 * k; vWO[k] = (it / 10) * (DM * 2) + (it % 10) * 16 + w * 160; }
  sfor<64, 128>([&](auto I) INL {
    constexpr int i = decltype(I)::value;
    constexpr int vi = i - 64, t = vi & 15;
    const u32x4 V = S[i];
    float v0 = bflo(V[0]);
    asm volatile("" : "+v"(v0));
    SB();
    sfor<target(i - 1), target(i)>([&](auto N) INL { S[decltype(N)::value] = ldS(decltype(N)::value); });
    if constexpr (vi == VSIG) { if (tid == 0) __hip_atomic_fetch_add((LAS u32*)(LAS unsigned char*)(lds + L_CNT + 8), 1u, __ATOMIC_RELAXED, __HIP_MEMORY_SCOPE_WORKGROUP); }
    if constexpr (vi >= 2 && vi < 50 && (vi - 2) % 12 < 10 && (vi - 2) % 2 == 0) { const int rho = (vi - 2) / 12, k = ((vi - 2) % 12) / 2;
 WO[k] = bld128<W_AUX>(rsWO, vWO[k], rho * 32 * DM * 2);
 }
    SB();
    {
      const float pb = bcast<t>(pr[vi >> 4]);
      acc[0] = fmaf(pb, v0, acc[0]); acc[1] = fmaf(pb, bfhi(V[0]), acc[1]);
#pragma unroll
      for (int u = 1; u < 4; ++u) { acc[2 * u] = fmaf(pb, bflo(V[u]), acc[2 * u]); acc[2 * u + 1] = fmaf(pb, bfhi(V[u]), acc[2 * u + 1]); }
      asm volatile("" : "+v"(acc[0]), "+v"(acc[1]), "+v"(acc[2]), "+v"(acc[3]), "+v"(acc[4]), "+v"(acc[5]), "+v"(acc[6]), "+v"(acc[7]));
    }
    if constexpr (vi >= 13 && vi <= 49 && (vi - 13) % 12 == 0) {
      unsigned char* st = lds + L_WO + w * 5120;
#pragma unroll
      for (int k = 0; k < 5; ++k) *(u32x4*)(st + (lane + 64 * k) * 16) = WO[k];
    }
    if constexpr (vi >= 14 && vi <= 50 && (vi - 14) % 12 == 0) {
      constexpr int rho = (vi - 14) / 12;
      LAS unsigned char* st = (LAS unsigned char*)(lds + L_WO + w * 5120);
#pragma unroll
      for (int n = 0; n < 5; ++n) {
        LAS unsigned char* a = st + (8 * g4 + (c >> 2)) * 160 + 32 * n + 8 * (c & 3);
        const u32x2 lo = __builtin_bit_cast(u32x2, __builtin_amdgcn_ds_read_tr16_b64_v4i16((LAS s16x4*)a));
        const u32x2 hi = __builtin_bit_cast(u32x2, __builtin_amdgcn_ds_read_tr16_b64_v4i16((LAS s16x4*)(a + 4 * 160)));
        BF[rho][n] = u32x4{lo.x, lo.y, hi.x, hi.y};
      }
    }
    if constexpr (vi >= 16 && vi <= 52 && (vi - 16) % 12 == 0) {
      constexpr int rho = (vi - 16) / 12;
#pragma unroll
      for (int n = 0; n < 5; ++n) asm volatile("" : "+v"(BF[rho][n]));
    }
    SB();
  });
  STAMP(4);

  {
    const float z0 = tr16(tr32(acc[0], acc[4]), tr32(acc[2], acc[6])), z1 = tr16(tr32(acc[1], acc[5]), tr32(acc[3], acc[7]));
    const u32 hi = pkbf(z0, z1), lo = pkbf(z0 - bflo(hi), z1 - bfhi(hi));
    *(u32*)(lds + L_OA + w * 272 + 16 * c + 4 * g4) = hi;
    *(u32*)(lds + L_OA + (8 + w) * 272 + 16 * c + 4 * g4) = lo;
  }
  __syncthreads();
  const u32x2 tg = *(const u32x2*)(lds + L_TAG);
  const u32 tA = tg.x, tB = tg.y;
  {
    f32x4 D[5];
#pragma unroll
    for (int n = 0; n < 5; ++n) D[n] = f32x4{0.f, 0.f, 0.f, 0.f};
    const f32x4 fr = *(const f32x4*)(lds + L_FW + 16 * g4);
#pragma unroll
    for (int rho = 0; rho < 4; ++rho) {
      const u32x4 A = *(const u32x4*)(lds + L_OA + c * 272 + 64 * rho + 16 * g4);
#pragma unroll
      for (int n = 0; n < 5; ++n) D[n] = __builtin_amdgcn_mfma_f32_16x16x32_bf16(__builtin_bit_cast(bf16x8, A), __builtin_bit_cast(bf16x8, BF[rho][n]), D[n], 0, 0, 0);
    }
    float e[5];
#pragma unroll
    for (int n = 0; n < 5; ++n) e[n] = fmaf(D[n][0], fr[0], D[n][1] * fr[1]) + fmaf(D[n][2], fr[2], D[n][3] * fr[3]);
    const float ya = tr16(tr32(e[0], e[2]), tr32(e[1], e[3]));
    const float yb = rows_sum(e[4]);
    const float yan = dpp<XOR1>(ya), ybn = dpp<XOR1>(yb);
    const bool odd = c & 1;
    const u32 cp = odd ? 40 * w + 32 + (c >> 1) : 40 * w + 8 * g4 + (c >> 1);
    const u32 off = (!odd || g4 == 0) ? WS_Y + (prod * 320 + cp) * 16 : OOB;
    const u32x4 gr = odd ? u32x4{f2u(ybn), tA, f2u(yb), tB} : u32x4{f2u(ya), tA, f2u(yan), tB};
    __builtin_amdgcn_raw_buffer_store_b128(gr, rsWS, (int)off, 0, AUX_SC1);
  }
  STAMP(5);
  if (!is_cons) { STAMP_FLUSH(); return; }

  const u32 offY = WS_Y + ((cb * 128 + (tid >> 2)) * 320 + 4 * coct + (tid & 3)) * 16;
  u32x4 YG = bld128<AUX_SC1>(rsWS, offY, 0);
  YG = wait_granule<Y_SLEEP>(YG, rsWS, offY, tA, tB, true);
  {
    float y0 = u2f(YG.x), y1 = u2f(YG.z);
    y0 += dpp<ROR4>(y0); y1 += dpp<ROR4>(y1); y0 += dpp<ROR8>(y0); y1 += dpp<ROR8>(y1);
    y0 = rows_sum(y0); y1 = rows_sum(y1);
    *(f32x2*)(lds + (lane < 4 ? L_YP + w * 32 : L_TRASH) + lane * 8) = f32x2{y0, y1};
  }
  __syncthreads();
  STAMP(8);
  STAMP_FLUSH();
}

}

extern "C" void kernel_launch(void* const* d_in, const int* in_sizes, int n_in, void* d_out, int out_size, void* d_ws, size_t ws_size, hipStream_t stream) {
  (void)in_sizes; (void)n_in; (void)out_size; (void)ws_size;
  Params p;
  p.x = d_in[0]; p.k = d_in[1]; p.v = d_in[2]; p.wq = d_in[3]; p.wo = d_in[4];
  p.w1 = d_in[6]; p.b1 = d_in[7]; p.w2 = d_in[8]; p.b2 = d_in[9]; p.out = d_out;
  p.ws = d_ws;
  hipLaunchKernelGGL(fused_block, dim3(NWG), dim3(NTH), 0, stream, p);
}
```

```cpp
#define K_N0 21 Q_DELAY
#define FIXT 6000
#define FIXP 0
#include <hip/hip_runtime.h>
#include <hip/hip_bf16.h>
#include <stdint.h>

namespace {

typedef unsigned u32;
typedef unsigned long long u64;
typedef unsigned short u16;
typedef u32 u32x4 __attribute__((ext_vector_type(4)));
typedef u32 u32x2 __attribute__((ext_vector_type(2)));
typedef float f32x4 __attribute__((ext_vector_type(4)));
typedef float f32x2 __attribute__((ext_vector_type(2)));
typedef short s16x4 __attribute__((ext_vector_type(4)));
typedef __bf16 bf16x2 __attribute__((ext_vector_type(2)));
typedef __bf16 bf16x8 __attribute__((ext_vector_type(8)));
typedef __amdgpu_buffer_rsrc_t rsrc_t;
#define GAS __attribute__((address_space(1)))
#define LAS __attribute__((address_space(3)))

constexpr int DM = 640, NH = 32, SK = 8192, DH = 128, FF = 1728;
constexpr int NWG = 256, NTH = 768;
constexpr int NFFN = FF / 8;
constexpr int CONS0 = 96;
constexpr u32 WS_E = 0;
constexpr u32 WS_ML = 4096;
constexpr u32 WS_Q = 8192;
constexpr u32 WS_H = WS_Q + NH * 8 * 2 * 64 * 16;
constexpr u32 WS_F = WS_H + NFFN * 64 * 16;
constexpr u32 WS_Y = WS_F + NFFN * 80 * 16;
constexpr u32 WS_END = WS_Y + NWG * 320 * 16;
constexpr u32 OOB = 0x7ffffff0u;
constexpr int AUX_NT = 2, AUX_SC1 = 16;
constexpr u32 SPIN_CAP = 1u << 16;

#define WQ_AUX 2
#define W_AUX 2
#define X_AUX 0
#define K_AUX 2
#define V_AUX 2
#define E_AUX 0
#define Y_SLEEP 2
#define VSIG 40
#define POLL_SLEEP 1
#define Q_DELAY 0
#define K_PRE 3
#define K_A 5
#define PACE 3
#define RING 3
#define BUMP 3
#define RING_END RING
#define RAMP_AT 96
#define RAMP_STEP 2
constexpr int N0 = K_N0, R = RING;
constexpr int target(int i) {
  int t = i + 1 + R;
  const int top = N0 + (i + 1 < R ? i + 1 : R);
  if (t < top) t = top;
  if (i >= 64 - BUMP && i < 64) t = i + 1 + R + (i - (64 - BUMP) + 1);
  if (i >= 64 && t < 64 + R + BUMP) t = 64 + R + BUMP;
  if (i >= RAMP_AT) { int rr = R + (i - RAMP_AT) / RAMP_STEP + 1; if (rr > RING_END) rr = RING_END; if (t < i + 1 + rr) t = i + 1 + rr; }
  return t > 128 ? 128 : t;
}

struct Params {
  const void* x; const void* k; const void* v; const void* wq; const void* wo;
  const void* w1; const void* b1; const void* w2; const void* b2; void* out; void* ws;
};

__device__ __forceinline__ rsrc_t mk_rsrc(const void* p, u32 bytes) { return __builtin_amdgcn_make_buffer_rsrc((void*)p, 0, (int)bytes, 0x00020000); }
template <int AUX> __device__ __forceinline__ u32x4 bld128(rsrc_t r, u32 voff, u32 soff) { return __builtin_amdgcn_raw_buffer_load_b128(r, (int)voff, (int)soff, AUX); }
template <int AUX> __device__ __forceinline__ u32 bld32(rsrc_t r, u32 voff, u32 soff) { return __builtin_amdgcn_raw_buffer_load_b32(r, (int)voff, (int)soff, AUX); }
__device__ __forceinline__ u32 bld16(rsrc_t r, u32 voff) { return (u32)__builtin_amdgcn_raw_buffer_load_b16(r, (int)voff, 0, 2); }
__device__ __forceinline__ float bflo(u32 x) { return __builtin_bit_cast(float, x << 16); }
__device__ __forceinline__ float bfhi(u32 x) { return __builtin_bit_cast(float, x & 0xffff0000u); }
__device__ __forceinline__ u32 pkbf(float lo, float hi) { bf16x2 v = {(__bf16)lo, (__bf16)hi}; return __builtin_bit_cast(u32, v); }
__device__ __forceinline__ float rbf(float x) { return bflo(pkbf(x, 0.f) & 0xffffu); }
__device__ __forceinline__ float dot2(u32 a, u32 b, float c) { return __builtin_amdgcn_fdot2_f32_bf16(__builtin_bit_cast(bf16x2, a), __builtin_bit_cast(bf16x2, b), c, false); }
template <int CTRL> __device__ __forceinline__ float dpp(float x) { return __builtin_bit_cast(float, __builtin_amdgcn_mov_dpp(__builtin_bit_cast(int, x), CTRL, 0xf, 0xf, true)); }
constexpr int XOR1 = 0xB1, XOR2 = 0x4E, ROR4 = 0x124, ROR8 = 0x128, ROR2 = 0x122, ROR1 = 0x121, BCAST = 0x150;
__device__ __forceinline__ float u2f(u32 x) { return __builtin_bit_cast(float, x); }
__device__ __forceinline__ u32 f2u(float x) { return __builtin_bit_cast(u32, x); }
__device__ __forceinline__ float tr32(float a, float b) { auto s = __builtin_amdgcn_permlane32_swap(f2u(a), f2u(b), false, false); return u2f(s[0]) + u2f(s[1]); }
__device__ __forceinline__ float tr16(float a, float b) { auto s = __builtin_amdgcn_permlane16_swap(f2u(a), f2u(b), false, false); return u2f(s[0]) + u2f(s[1]); }
__device__ __forceinline__ float rows_sum(float x) { x = tr16(x, x); return tr32(x, x); }
__device__ __forceinline__ float rows_max(float x) {
  auto s = __builtin_amdgcn_permlane16_swap(f2u(x), f2u(x), false, false); x = fmaxf(u2f(s[0]), u2f(s[1]));
  auto t = __builtin_amdgcn_permlane32_swap(f2u(x), f2u(x), false, false); return fmaxf(u2f(t[0]), u2f(t[1]));
}
__device__ __forceinline__ float row16_sum(float x) { x += dpp<ROR8>(x); x += dpp<ROR4>(x); x += dpp<ROR2>(x); x += dpp<ROR1>(x); return x; }
__device__ __forceinline__ float row16_max(float x) { x = fmaxf(x, dpp<ROR8>(x)); x = fmaxf(x, dpp<ROR4>(x)); x = fmaxf(x, dpp<ROR2>(x)); x = fmaxf(x, dpp<ROR1>(x)); return x; }
__device__ __forceinline__ float wave_sum(float x) { return rows_sum(row16_sum(x)); }
__device__ __forceinline__ float wave_max(float x) { return rows_max(row16_max(x)); }
template <int K> __device__ __forceinline__ float merge(float lo, float hi, int c) {
  const bool bit = (c >> K) & 1;
  const float keep = bit ? hi : lo, send = bit ? lo : hi;
  constexpr int CTRL = K == 0 ? XOR1 : K == 1 ? XOR2 : K == 2 ? ROR4 : ROR8;
  return keep + dpp<CTRL>(send);
}
template <int T> __device__ __forceinline__ float bcast(float x) { return dpp<BCAST + T>(x); }
#define SB() __builtin_amdgcn_sched_barrier(0)
template <int I> struct IC { static constexpr int value = I; };
template <int B, int E, class F> __device__ __forceinline__ void sfor(F&& f) { if constexpr (B < E) { f(IC<B>{}); sfor<B + 1, E>(f); } }
#define INL __attribute__((always_inline))

__device__ __forceinline__ bool tags_ok(const u32x4& g, u32 tA, u32 tB) { return g.y == tA && g.w == tB; }
template <int SLEEP = 2> __device__ __forceinline__ u32x4 wait_granule(u32x4 g, rsrc_t rs, u32 off, u32 tA, u32 tB, bool need) {
  bool ok = !need || tags_ok(g, tA, tB);
  if (!__all(ok)) {
#pragma unroll 1
    for (u32 spin = 0; spin < SPIN_CAP; ++spin) {
      if (!ok) { g = bld128<AUX_SC1>(rs, off, 0); ok = tags_ok(g, tA, tB); }
      if (__all(ok)) break;
      __builtin_amdgcn_s_sleep(SLEEP);
    }
  }
  return g;
}

template <int N> __device__ __forceinline__ void wait_granules(u32x4 (&g)[N], rsrc_t rs, u32 off, u32 stride, u32 tA, u32 tB) {
  bool ok = true;
#pragma unroll
  for (int k = 0; k < N; ++k) ok = ok && tags_ok(g[k], tA, tB);
  if (!__all(ok)) {
#pragma unroll 1
    for (u32 spin = 0; spin < SPIN_CAP; ++spin) {
#pragma unroll
      for (int k = 0; k < N; ++k) if (!tags_ok(g[k], tA, tB)) g[k] = bld128<AUX_SC1>(rs, off, k * stride);
      ok = true;
#pragma unroll
      for (int k = 0; k < N; ++k) ok = ok && tags_ok(g[k], tA, tB);
      if (__all(ok)) break;
      __builtin_amdgcn_s_sleep(POLL_SLEEP);
    }
  }
}
__device__ __forceinline__ void lds_wait(LAS unsigned char* word, u32 want) {
#pragma unroll 1
  for (u32 spin = 0; spin < (SPIN_CAP << 4); ++spin) {
    if (*(volatile LAS u32*)word >= want) break;
    __builtin_amdgcn_s_sleep(2);
  }
  asm volatile("" ::: "memory");
}

constexpr int L_XN = 0;
constexpr int L_QP = 2560;
constexpr int L_Q = L_QP + 4096;
constexpr int L_ML = L_Q + 256;
constexpr int L_CNT = L_ML + 64;
constexpr int L_TAG = L_CNT + 16;
constexpr int L_FW = L_CNT + 32;
constexpr int L_FP = L_FW + 64;
constexpr int L_YP = L_FP + 32;
constexpr int L_H2 = L_YP + 256;
constexpr int L_FR = L_H2 + 512;
constexpr int L_OA = L_FR + 3840;
constexpr int L_WO = L_OA + 16 * 272;
constexpr int L_TRASH = L_WO + 8 * 5120;
#define LDS_PAD 0
constexpr int L_END = L_TRASH + 1024 + LDS_PAD;
static_assert(L_OA % 16 == 0 && L_WO % 16 == 0 && L_FW % 16 == 0 && L_FR % 16 == 0 && L_QP % 16 == 0, "LDS alignment");

#define STAMP(n) do {} while (0)
#define STAMP_FLUSH() do {} while (0)

__global__ void __launch_bounds__(NTH, 3) fused_block(Params p) {
  __shared__ __attribute__((aligned(16))) unsigned char lds[L_END];
  const int tid = threadIdx.x, lane = tid & 63, w = __builtin_amdgcn_readfirstlane(tid >> 6), g4 = lane >> 4, c = lane & 15;
  const int bid = blockIdx.x, xc = bid & 7, rr = bid >> 3;
#define HMAP 0
  const int h = HMAP == 0 ? (rr >> 3) * 8 + xc : HMAP == 1 ? (rr >> 3) * 8 + ((xc + 2 * (rr >> 3)) & 7) : (rr >> 3) * 8 + ((xc + 3 * (rr >> 3)) & 7), b = (rr >> 2) & 1, s = rr & 3;
  const int pair = b * NH + h, prod = pair * 4 + s;
  const int fu = xc + 8 * (rr >> 3), fv = rr & 7;
  const bool is_ffn = fu < 27;
  const bool is_cons = bid >= CONS0;
  const int cj = bid - CONS0, cb = is_cons ? cj / 80 : 0, coct = is_cons ? cj % 80 : 0;
  u64 tstart = __builtin_amdgcn_s_memrealtime();
  __builtin_amdgcn_sched_barrier(0);
  STAMP(0);

  const rsrc_t rsWS = mk_rsrc(p.ws, WS_END);
  const rsrc_t rsK = mk_rsrc((const char*)p.k + (size_t)pair * (SK * DH * 2) + (size_t)s * (2048 * DH * 2), 2048 * DH * 2);
  const rsrc_t rsV = mk_rsrc((const char*)p.v + (size_t)pair * (SK * DH * 2) + (size_t)s * (2048 * DH * 2), 2048 * DH * 2);
  const rsrc_t rsX = mk_rsrc(p.x, 2 * DM * 2);
  const rsrc_t rsWQ = mk_rsrc(p.wq, DM * NH * DH * 2);
  const rsrc_t rsWO = mk_rsrc((const char*)p.wo + (size_t)h * (DH * DM * 2), DH * DM * 2);
  const rsrc_t rsW1 = mk_rsrc(p.w1, is_ffn ? DM * FF * 2 : 0);
  const rsrc_t rsW2 = mk_rsrc(p.w2, is_ffn ? FF * DM * 2 : 0);
  const rsrc_t rsFX = rsWS;
#define FX_WAIT 1
  const rsrc_t rsB1 = mk_rsrc(p.b1, is_ffn ? FF * 2 : 0);
  const rsrc_t rsB2 = mk_rsrc(p.b2, DM * 2);

  auto ln_stats = [&](const u32x4& ra, u32 rb, float& mb, float& inv) INL {
    float v[10];
#pragma unroll
    for (int k = 0; k < 4; ++k) { v[2 * k] = bflo(ra[k]); v[2 * k + 1] = bfhi(ra[k]); }
    v[8] = bflo(rb); v[9] = bfhi(rb);
    float sum = 0.f;
#pragma unroll
    for (int k = 0; k < 10; ++k) sum += v[k];
    const float mean = wave_sum(sum) * (1.f / DM);
    float sq = 0.f;
#pragma unroll
    for (int k = 0; k < 10; ++k) { const float d = v[k] - mean; sq = fmaf(d, d, sq); }
    const float var = wave_sum(sq) * (1.f / DM);
    mb = rbf(mean);
    inv = 1.f / rbf(sqrtf(rbf(rbf(var) + 1e-5f)));
  };
  auto ln_row = [&](const u32x4& ra, u32 rb, float mb, float inv, int dst) INL {
#pragma unroll
    for (int k = 0; k < 5; ++k) {
      const u32 r = k < 4 ? ra[k & 3] : rb;
      const u32 d = pkbf(bflo(r) - mb, bfhi(r) - mb);
      *(u32*)(lds + dst + lane * 20 + 4 * k) = pkbf(bflo(d) * inv, bfhi(d) * inv);
    }
  };

  if (w >= 8) {
    const int hw = w - 8;
    if (hw == 0) { *(volatile LAS u32*)(LAS unsigned char*)(lds + L_CNT) = 0u; *(volatile LAS u32*)(LAS unsigned char*)(lds + L_CNT + 4) = 0u; *(volatile LAS u32*)(LAS unsigned char*)(lds + L_CNT + 8) = 0u; }
    __syncthreads();
    const u32x4 x0a = bld128<X_AUX>(rsX, lane * 20, 0), x1a = bld128<X_AUX>(rsX, 1280 + lane * 20, 0);
    const u32 x0b = bld32<X_AUX>(rsX, lane * 20 + 16, 0), x1b = bld32<X_AUX>(rsX, 1280 + lane * 20 + 16, 0);
    const int qrow = 80 * (4 * b + s) + 20 * hw + 5 * g4;
    u32x4 WQ[5];
#pragma unroll
    for (int k = 0; k < 5; ++k) WQ[k] = bld128<WQ_AUX>(rsWQ, qrow * 8192 + c * 16, k * 8192 + h * 256);
    typedef u32 u32x3 __attribute__((ext_vector_type(3)));
    const u32x3 xr0 = __builtin_amdgcn_raw_buffer_load_b96(rsX, (qrow * 2) & ~3, 0, X_AUX), xr1 = __builtin_amdgcn_raw_buffer_load_b96(rsX, 1280 + ((qrow * 2) & ~3), 0, X_AUX);
    const u32x2 E2 = __builtin_bit_cast(u32x2, __builtin_amdgcn_raw_buffer_load_b64(rsWS, hw == 0 ? (int)WS_E : (int)OOB, 0, E_AUX));
    const u64 E = (u64)E2.x | ((u64)E2.y << 32);
    SB();
    float mb0, inv0, mb1, inv1;
    ln_stats(x0a, x0b, mb0, inv0);
    ln_stats(x1a, x1b, mb1, inv1);
    STAMP(9);
    {
      float a[16];
#pragma unroll
      for (int k = 0; k < 16; ++k) a[k] = 0.f;
#pragma unroll
      for (int k = 0; k < 5; ++k) {
        constexpr int dummy = 0; (void)dummy;
        const int e0 = k, e1 = k + 1;
        const float r0 = (qrow & 1) ? ((e1 & 1) ? bfhi(xr0[e1 >> 1]) : bflo(xr0[e1 >> 1])) : ((e0 & 1) ? bfhi(xr0[e0 >> 1]) : bflo(xr0[e0 >> 1]));
        const float r1 = (qrow & 1) ? ((e1 & 1) ? bfhi(xr1[e1 >> 1]) : bflo(xr1[e1 >> 1])) : ((e0 & 1) ? bfhi(xr1[e0 >> 1]) : bflo(xr1[e0 >> 1]));
        const float x0 = rbf(rbf(r0 - mb0) * inv0), x1 = rbf(rbf(r1 - mb1) * inv1);
#pragma unroll
        for (int u = 0; u < 4; ++u) {
          const float lo = bflo(WQ[k][u]), hi = bfhi(WQ[k][u]);
          a[2 * u] = fmaf(x0, lo, a[2 * u]); a[2 * u + 1] = fmaf(x0, hi, a[2 * u + 1]);
          a[8 + 2 * u] = fmaf(x1, lo, a[8 + 2 * u]); a[8 + 2 * u + 1] = fmaf(x1, hi, a[8 + 2 * u + 1]);
        }
      }
#pragma unroll
      for (int e = 0; e < 8; ++e) a[e] = tr32(a[e], a[8 + e]);
      const f32x4 z = {tr16(a[0], a[4]), tr16(a[1], a[5]), tr16(a[2], a[6]), tr16(a[3], a[7])};
      *(f32x4*)(lds + L_QP + (hw * 256 + (g4 >> 1) * 128 + 8 * c + 4 * (g4 & 1)) * 4) = z;
      asm volatile("s_waitcnt lgkmcnt(0)" ::: "memory");
      if (lane == 0) __hip_atomic_fetch_add((LAS u32*)(LAS unsigned char*)(lds + L_CNT + 4), 1u, __ATOMIC_RELAXED, __HIP_MEMORY_SCOPE_WORKGROUP);
    }
    if (hw != 0) return;
    const u32 tA = (u32)E + 0x9E3779B9u, tB = (u32)(E >> 32) + 0x85EBCA6Bu;
    *(u32x2*)(lds + (lane == 0 ? L_TAG : L_TRASH + lane * 8)) = u32x2{tA, tB};
    lds_wait((LAS unsigned char*)(lds + L_CNT + 4), 4u);
    STAMP(7);
    {
      f32x2 t0 = {0.f, 0.f}, t1 = {0.f, 0.f};
#pragma unroll
      for (int k = 0; k < 4; ++k) { t0 += *(const f32x2*)(lds + L_QP + (k * 256 + 2 * lane) * 4); t1 += *(const f32x2*)(lds + L_QP + (k * 256 + 128 + 2 * lane) * 4); }
      const u32 offP = WS_Q + (((h * 8 + b * 4 + s) * 2) * 64 + lane) * 16;
      __builtin_amdgcn_raw_buffer_store_b128(u32x4{f2u(t0.x), tA, f2u(t0.y), tB}, rsWS, (int)offP, 0, AUX_SC1);
      __builtin_amdgcn_raw_buffer_store_b128(u32x4{f2u(t1.x), tA, f2u(t1.y), tB}, rsWS, (int)offP, 64 * 16, AUX_SC1);
      const u32 offQ = WS_Q + ((h * 8 * 2 + b) * 64 + lane) * 16;
      u32x4 g[8];
      if (Q_DELAY) __builtin_amdgcn_s_sleep(Q_DELAY);
#pragma unroll
      for (int k = 0; k < 8; ++k) g[k] = bld128<AUX_SC1>(rsWS, offQ, k * 2 * 64 * 16);
      STAMP(6);
      wait_granules<8>(g, rsWS, offQ, 2 * 64 * 16, tA, tB);
      float q0 = 0.f, q1 = 0.f;
#pragma unroll
      for (int k = 0; k < 8; ++k) { q0 += u2f(g[k].x); q1 += u2f(g[k].z); }
      *(u32*)(lds + L_Q + lane * 4) = pkbf(q0, q1);
    }
    STAMP(1);
    __syncthreads();
    const u32 B2v = bld16(rsB2, (8 * coct + (lane & 7)) * 2), XV = bld16(rsX, cb * 1280 + (8 * coct + (lane & 7)) * 2);
    ln_row(x0a, x0b, mb0, inv0, L_XN);
    ln_row(x1a, x1b, mb1, inv1, L_XN + 1280);
    if (is_ffn) {
      u32x4 W1[10];
#pragma unroll
      for (int k = 0; k < 10; ++k) W1[k] = bld128<W_AUX>(rsW1, (lane >> 3) * (FF * 2) + (lane & 7) * 16, (80 * fv + 8 * k) * (FF * 2) + fu * 128);
      SB();
      float hv[16];
#pragma unroll
      for (int k = 0; k < 16; ++k) hv[k] = 0.f;
#pragma unroll
      for (int k = 0; k < 10; ++k) {
        const int row = 80 * fv + 8 * k + (lane >> 3);
        const float x0 = bflo(*(const u16*)(lds + L_XN + row * 2)), x1 = bflo(*(const u16*)(lds + L_XN + 1280 + row * 2));
#pragma unroll
        for (int u = 0; u < 4; ++u) {
          const float lo = bflo(W1[k][u]), hi = bfhi(W1[k][u]);
          hv[2 * u] = fmaf(x0, lo, hv[2 * u]); hv[2 * u + 1] = fmaf(x0, hi, hv[2 * u + 1]);
          hv[8 + 2 * u] = fmaf(x1, lo, hv[8 + 2 * u]); hv[8 + 2 * u + 1] = fmaf(x1, hi, hv[8 + 2 * u + 1]);
        }
      }
      SB();
#pragma unroll
      for (int e = 0; e < 8; ++e) hv[e] = merge<3>(hv[e], hv[8 + e], c);
#pragma unroll
      for (int e = 0; e < 4; ++e) hv[e] = tr16(hv[e], hv[e + 4]);
      const float h0 = tr32(hv[0], hv[2]), h1 = tr32(hv[1], hv[3]);
      const u32 gi = ((lane >> 3) & 1) * 32 + 4 * (lane & 7) + 2 * ((lane >> 4) & 1) + (lane >> 5);
      __builtin_amdgcn_raw_buffer_store_b128(u32x4{f2u(h0), tA, f2u(h1), tB}, rsFX, (int)(WS_H + ((fu * 8 + fv) * 64 + gi) * 16), 0, AUX_SC1);
    }
    STAMP(2);
      lds_wait((LAS unsigned char*)(lds + L_CNT), 8u);
      const f32x2 ml = *(const f32x2*)(lds + L_ML + (lane & 7) * 8);
      const float mg = wave_max(ml.x);
      const float lg = row16_sum(c < 8 ? ml.y * __builtin_amdgcn_exp2f(ml.x - mg) : 0.f);
      __builtin_amdgcn_raw_buffer_store_b128(u32x4{f2u(mg), tA, f2u(lg), tB}, rsWS, (int)(lane == 0 ? WS_ML + prod * 16 : OOB), 0, AUX_SC1);
      STAMP(3);
    if (is_ffn) {
      u32x4 W2[11];
      const u32 B1v = bld32<W_AUX>(rsB1, (lane & 31) * 4, fu * 128);
      const int rg = lane / 10, ch = lane - 10 * rg;
#pragma unroll
      for (int k = 0; k < 11; ++k) W2[k] = bld128<W_AUX>(rsW2, (lane < 60 && 6 * k + rg < 64) ? rg * (DM * 2) + ch * 16 : OOB, (64 * fu + 6 * k) * (DM * 2) + fv * 160);
      SB();
      const u32 offH = WS_H + (fu * 8 * 64 + lane) * 16;
      u32x4 g[8];
#pragma unroll
      for (int k = 0; k < 8; ++k) g[k] = bld128<AUX_SC1>(rsFX, offH, k * 64 * 16);
      if (FX_WAIT) wait_granules<8>(g, rsWS, offH, 64 * 16, tA, tB);
      float s0 = 0.f, s1 = 0.f;
#pragma unroll
      for (int k = 0; k < 8; ++k) { s0 += u2f(g[k].x); s1 += u2f(g[k].z); }
      s0 = rbf(rbf(s0) + bflo(B1v)); s1 = rbf(rbf(s1) + bfhi(B1v));
      s0 = s0 > 0.f ? s0 : 0.f; s1 = s1 > 0.f ? s1 : 0.f;
      *(f32x2*)(lds + L_H2 + lane * 8) = f32x2{rbf(s0 * s0), rbf(s1 * s1)};
      float ov[16];
#pragma unroll
      for (int k = 0; k < 16; ++k) ov[k] = 0.f;
#pragma unroll
      for (int k = 0; k < 11; ++k) {
        const int r = (6 * k + rg) & 63;
        const float a0 = *(const float*)(lds + L_H2 + r * 4), a1 = *(const float*)(lds + L_H2 + 256 + r * 4);
#pragma unroll
        for (int u = 0; u < 4; ++u) {
          const float lo = bflo(W2[k][u]), hi = bfhi(W2[k][u]);
          ov[2 * u] = fmaf(a0, lo, ov[2 * u]); ov[2 * u + 1] = fmaf(a0, hi, ov[2 * u + 1]);
          ov[8 + 2 * u] = fmaf(a1, lo, ov[8 + 2 * u]); ov[8 + 2 * u + 1] = fmaf(a1, hi, ov[8 + 2 * u + 1]);
        }
      }
      unsigned char* fr = lds + (lane < 60 ? L_FR + lane * 64 : L_TRASH + (lane & 3) * 64);
#pragma unroll
      for (int k = 0; k < 4; ++k) *(f32x4*)(fr + 16 * k) = f32x4{ov[4 * k], ov[4 * k + 1], ov[4 * k + 2], ov[4 * k + 3]};
#pragma unroll
      for (int j = 0; j < 2; ++j) {
        const int gq = lane + 64 * j, gb = gq >= 40 ? 1 : 0, cp = (gq - 40 * gb) % 40;
        f32x2 t = {0.f, 0.f};
#pragma unroll
        for (int k = 0; k < 6; ++k) t += *(const f32x2*)(lds + L_FR + (k * 10 + (cp >> 2)) * 64 + (gb * 8 + 2 * (cp & 3)) * 4);
        __builtin_amdgcn_raw_buffer_store_b128(u32x4{f2u(t.x), tA, f2u(t.y), tB}, rsFX, (int)(gq < 80 ? WS_F + ((fu * 8 + fv) * 80 + gq) * 16 : OOB), 0, AUX_SC1);
      }
    }
    {
      const u32 offML = WS_ML + (pair * 4 + (lane & 3)) * 16;
      u32x4 g = bld128<AUX_SC1>(rsWS, offML, 0);
      g = wait_granule(g, rsWS, offML, tA, tB, true);
      const float mv = u2f(g.x), M = row16_max(mv);
      const float L = row16_sum(c < 4 ? u2f(g.z) * __builtin_amdgcn_exp2f(mv - M) : 0.f);
      *(float*)(lds + (lane < 16 ? L_FW : L_TRASH) + lane * 4) = __builtin_amdgcn_exp2f(ml.x - M) / L;
    }
    STAMP(4);
    float gate = 0.f;
    if (is_cons) {
      lds_wait((LAS unsigned char*)(lds + L_CNT + 8), 1u);
      u32x4 FG[2]; u32 offF[2];
      const int cv = coct / 10, cp0 = (coct - 10 * cv) * 4;
#pragma unroll
      for (int k = 0; k < 2; ++k) {
        const int idx = lane + 64 * k;
        offF[k] = idx < 108 ? WS_F + (((idx >> 2) * 8 + cv) * 80 + cb * 40 + cp0 + (idx & 3)) * 16 : OOB;
        FG[k] = bld128<AUX_SC1>(rsFX, offF[k], 0);
      }
      FG[0] = wait_granule(FG[0], rsWS, offF[0], tA, tB, FX_WAIT);
      FG[1] = wait_granule(FG[1], rsWS, offF[1], tA, tB, FX_WAIT && lane < 44);
      float f0 = u2f(FG[0].x) + (lane < 44 ? u2f(FG[1].x) : 0.f), f1 = u2f(FG[0].z) + (lane < 44 ? u2f(FG[1].z) : 0.f);
      f0 += dpp<ROR4>(f0); f1 += dpp<ROR4>(f1); f0 += dpp<ROR8>(f0); f1 += dpp<ROR8>(f1);
      f0 = rows_sum(f0); f1 = rows_sum(f1);
      *(f32x2*)(lds + (lane < 4 ? L_FP : L_TRASH) + lane * 8) = f32x2{f0, f1};
      const float fs = *(const float*)(lds + L_FP + (lane & 7) * 4);
      const float o = rbf(rbf(fs) + bflo(B2v));
      gate = rbf(o / (1.f + __expf(-o)));
    }
    STAMP(5);
    __syncthreads();
    if (!is_cons) { STAMP_FLUSH(); return; }
    u32x4 og;
    const u32 offO = WS_Y + ((128 * (cb ^ 1) + lane) * 320) * 16;
    __syncthreads();
    while (__builtin_amdgcn_s_memrealtime() - tstart < FIXT) __builtin_amdgcn_s_sleep(1);
    {
      float ys = 0.f;
#pragma unroll
      for (int k = 0; k < 8; ++k) ys += *(const float*)(lds + L_YP + (k * 8 + (lane & 7)) * 4);
      const float res = bflo(XV) + rbf(rbf(ys) * gate);
      if (lane < 8) ((GAS u16*)p.out)[cb * DM + 8 * coct + lane] = (u16)(pkbf(res, 0.f) & 0xffffu);
    }
    STAMP(8);
    if (bid == CONS0) {
      og = bld128<AUX_SC1>(rsWS, offO, 0);
      og = wait_granule(og, rsWS, offO, tA, tB, true);
      og = bld128<AUX_SC1>(rsWS, offO + 64 * 320 * 16, 0);
      og = wait_granule(og, rsWS, offO + 64 * 320 * 16, tA, tB, true);
      if (lane == 0) __hip_atomic_store((GAS u64*)p.ws, (u64)tA | ((u64)tB << 32), __ATOMIC_RELAXED, __HIP_MEMORY_SCOPE_AGENT);
    }
    STAMP_FLUSH();
    return;
  }

  const u32 vKV = w * 4096 + lane * 16;
  u32x4 S[128];
  auto ldS = [&](int i) INL -> u32x4 { const int t = i & 63; return i < 64 ? bld128<K_AUX>(rsK, vKV + (t & 3) * 1024, (t >> 2) * 32768) : bld128<V_AUX>(rsV, vKV + (t & 3) * 1024, (t >> 2) * 32768); };
  sfor<0, PACE>([&](auto I) INL { S[decltype(I)::value] = ldS(decltype(I)::value); });
  SB();
  __syncthreads();
  sfor<PACE, N0>([&](auto I) INL {
    constexpr int n = decltype(I)::value;
    asm volatile("" :: "v"(S[n - PACE][0]));
    SB();
    S[n] = ldS(n);
    SB();
    if constexpr (n == 6) STAMP(9);
    if constexpr (n == 10) STAMP(10);
    if constexpr (n == 14) STAMP(11);
  });
  STAMP(6);
  __syncthreads();
  u32 q[4];
  { const u32x4 qq = *(const u32x4*)(lds + L_Q + c * 16); q[0] = qq[0]; q[1] = qq[1]; q[2] = qq[2]; q[3] = qq[3]; }
  STAMP(2);

  float sc[4], d0 = 0.f, m1a = 0.f, m2a = 0.f, m3a = 0.f, m1 = 0.f, m2 = 0.f;
  sfor<0, 64>([&](auto I) INL {
    constexpr int i = decltype(I)::value;
    float d = dot2(S[i][0], q[0], 0.f);
    asm volatile("" : "+v"(d));
    SB();
    sfor<(i == 0 ? N0 : target(i - 1)), target(i)>([&](auto N) INL { S[decltype(N)::value] = ldS(decltype(N)::value); });
    SB();
    d = dot2(S[i][1], q[1], d); d = dot2(S[i][2], q[2], d); d = dot2(S[i][3], q[3], d);
    asm volatile("" : "+v"(d));
    constexpr int t = i & 15;
    if ((t & 1) == 0) d0 = d;
    else {
      const float a1 = merge<0>(d0, d, c);
      if ((t & 2) == 0) m1a = a1;
      else {
        m1 = merge<1>(m1a, a1, c);
        if ((t & 4) == 0) m2a = m1;
        else {
          m2 = merge<2>(m2a, m1, c);
          if ((t & 8) == 0) m3a = m2;
          else sc[i >> 4] = merge<3>(m3a, m2, c);
        }
      }
    }
    SB();
  });
  STAMP(3);
  constexpr float C2 = 0.08838834764831845f * 1.4426950408889634f;
  float pr[4];
  {
#pragma unroll
    for (int j = 0; j < 4; ++j) sc[j] *= C2;
    const float mw = wave_max(fmaxf(fmaxf(sc[0], sc[1]), fmaxf(sc[2], sc[3])));
#pragma unroll
    for (int j = 0; j < 4; ++j) pr[j] = __builtin_amdgcn_exp2f(sc[j] - mw);
    const float lw = wave_sum((pr[0] + pr[1]) + (pr[2] + pr[3]));
    *(f32x2*)(lds + (lane == 0 ? L_ML + w * 8 : L_TRASH + lane * 8)) = f32x2{mw, lw};
    asm volatile("s_waitcnt lgkmcnt(0)" ::: "memory");
    if (lane == 0) __hip_atomic_fetch_add((LAS u32*)(LAS unsigned char*)(lds + L_CNT), 1u, __ATOMIC_RELAXED, __HIP_MEMORY_SCOPE_WORKGROUP);
  }
  SB();

  float acc[8];
#pragma unroll
  for (int k = 0; k < 8; ++k) acc[k] = 0.f;
  u32x4 WO[5], BF[4][5];
  u32 vWO[5];
#pragma unroll
  for (int k = 0; k < 5; ++k) { const int it = lane + 64 * k; vWO[k] = (it / 10) * (DM * 2) + (it % 10) * 16 + w * 160; }
  sfor<64, 128>([&](auto I) INL {
    constexpr int i = decltype(I)::value;
    constexpr int vi = i - 64, t = vi & 15;
    const u32x4 V = S[i];
    float v0 = bflo(V[0]);
    asm volatile("" : "+v"(v0));
    SB();
    sfor<target(i - 1), target(i)>([&](auto N) INL { S[decltype(N)::value] = ldS(decltype(N)::value); });
    if constexpr (vi == VSIG) { if (tid == 0) __hip_atomic_fetch_add((LAS u32*)(LAS unsigned char*)(lds + L_CNT + 8), 1u, __ATOMIC_RELAXED, __HIP_MEMORY_SCOPE_WORKGROUP); }
    if constexpr (vi >= 2 && vi < 50 && (vi - 2) % 12 < 10 && (vi - 2) % 2 == 0) { const int rho = (vi - 2) / 12, k = ((vi - 2) % 12) / 2;
 WO[k] = bld128<W_AUX>(rsWO, vWO[k], rho * 32 * DM * 2);
 }
    SB();
    {
      const float pb = bcast<t>(pr[vi >> 4]);
      acc[0] = fmaf(pb, v0, acc[0]); acc[1] = fmaf(pb, bfhi(V[0]), acc[1]);
#pragma unroll
      for (int u = 1; u < 4; ++u) { acc[2 * u] = fmaf(pb, bflo(V[u]), acc[2 * u]); acc[2 * u + 1] = fmaf(pb, bfhi(V[u]), acc[2 * u + 1]); }
      asm volatile("" : "+v"(acc[0]), "+v"(acc[1]), "+v"(acc[2]), "+v"(acc[3]), "+v"(acc[4]), "+v"(acc[5]), "+v"(acc[6]), "+v"(acc[7]));
    }
    if constexpr (vi >= 13 && vi <= 49 && (vi - 13) % 12 == 0) {
      unsigned char* st = lds + L_WO + w * 5120;
#pragma unroll
      for (int k = 0; k < 5; ++k) *(u32x4*)(st + (lane + 64 * k) * 16) = WO[k];
    }
    if constexpr (vi >= 14 && vi <= 50 && (vi - 14) % 12 == 0) {
      constexpr int rho = (vi - 14) / 12;
      LAS unsigned char* st = (LAS unsigned char*)(lds + L_WO + w * 5120);
#pragma unroll
      for (int n = 0; n < 5; ++n) {
        LAS unsigned char* a = st + (8 * g4 + (c >> 2)) * 160 + 32 * n + 8 * (c & 3);
        const u32x2 lo = __builtin_bit_cast(u32x2, __builtin_amdgcn_ds_read_tr16_b64_v4i16((LAS s16x4*)a));
        const u32x2 hi = __builtin_bit_cast(u32x2, __builtin_amdgcn_ds_read_tr16_b64_v4i16((LAS s16x4*)(a + 4 * 160)));
        BF[rho][n] = u32x4{lo.x, lo.y, hi.x, hi.y};
      }
    }
    if constexpr (vi >= 16 && vi <= 52 && (vi - 16) % 12 == 0) {
      constexpr int rho = (vi - 16) / 12;
#pragma unroll
      for (int n = 0; n < 5; ++n) asm volatile("" : "+v"(BF[rho][n]));
    }
    SB();
  });
  STAMP(4);

  {
    const float z0 = tr16(tr32(acc[0], acc[4]), tr32(acc[2], acc[6])), z1 = tr16(tr32(acc[1], acc[5]), tr32(acc[3], acc[7]));
    const u32 hi = pkbf(z0, z1), lo = pkbf(z0 - bflo(hi), z1 - bfhi(hi));
    *(u32*)(lds + L_OA + w * 272 + 16 * c + 4 * g4) = hi;
    *(u32*)(lds + L_OA + (8 + w) * 272 + 16 * c + 4 * g4) = lo;
  }
  __syncthreads();
  const u32x2 tg = *(const u32x2*)(lds + L_TAG);
  const u32 tA = tg.x, tB = tg.y;
  {
    f32x4 D[5];
#pragma unroll
    for (int n = 0; n < 5; ++n) D[n] = f32x4{0.f, 0.f, 0.f, 0.f};
    const f32x4 fr = *(const f32x4*)(lds + L_FW + 16 * g4);
#pragma unroll
    for (int rho = 0; rho < 4; ++rho) {
      const u32x4 A = *(const u32x4*)(lds + L_OA + c * 272 + 64 * rho + 16 * g4);
#pragma unroll
      for (int n = 0; n < 5; ++n) D[n] = __builtin_amdgcn_mfma_f32_16x16x32_bf16(__builtin_bit_cast(bf16x8, A), __builtin_bit_cast(bf16x8, BF[rho][n]), D[n], 0, 0, 0);
    }
    float e[5];
#pragma unroll
    for (int n = 0; n < 5; ++n) e[n] = fmaf(D[n][0], fr[0], D[n][1] * fr[1]) + fmaf(D[n][2], fr[2], D[n][3] * fr[3]);
    const float ya = tr16(tr32(e[0], e[2]), tr32(e[1], e[3]));
    const float yb = rows_sum(e[4]);
    const float yan = dpp<XOR1>(ya), ybn = dpp<XOR1>(yb);
    const bool odd = c & 1;
    const u32 cp = odd ? 40 * w + 32 + (c >> 1) : 40 * w + 8 * g4 + (c >> 1);
    const u32 off = (!odd || g4 == 0) ? WS_Y + (prod * 320 + cp) * 16 : OOB;
    const u32x4 gr = odd ? u32x4{f2u(ybn), tA, f2u(yb), tB} : u32x4{f2u(ya), tA, f2u(yan), tB};
    __builtin_amdgcn_raw_buffer_store_b128(gr, rsWS, (int)off, 0, AUX_SC1);
  }
  STAMP(5);
  if (!is_cons) { STAMP_FLUSH(); return; }

  const u32 offY = WS_Y + ((cb * 128 + (tid >> 2)) * 320 + 4 * coct + (tid & 3)) * 16;
  u32x4 YG = bld128<AUX_SC1>(rsWS, offY, 0);
  YG = wait_granule<Y_SLEEP>(YG, rsWS, offY, tA, tB, true);
  {
    float y0 = u2f(YG.x), y1 = u2f(YG.z);
    y0 += dpp<ROR4>(y0); y1 += dpp<ROR4>(y1); y0 += dpp<ROR8>(y0); y1 += dpp<ROR8>(y1);
    y0 = rows_sum(y0); y1 = rows_sum(y1);
    *(f32x2*)(lds + (lane < 4 ? L_YP + w * 32 : L_TRASH) + lane * 8) = f32x2{y0, y1};
  }
  __syncthreads();
  STAMP(8);
  STAMP_FLUSH();
}

}

extern "C" void kernel_launch(void* const* d_in, const int* in_sizes, int n_in, void* d_out, int out_size, void* d_ws, size_t ws_size, hipStream_t stream) {
  (void)in_sizes; (void)n_in; (void)out_size; (void)ws_size;
  Params p;
  p.x = d_in[0]; p.k = d_in[1]; p.v = d_in[2]; p.wq = d_in[3]; p.wo = d_in[4];
  p.w1 = d_in[6]; p.b1 = d_in[7]; p.w2 = d_in[8]; p.b2 = d_in[9]; p.out = d_out;
  p.ws = d_ws;
  hipLaunchKernelGGL(fused_block, dim3(NWG), dim3(NTH), 0, stream, p);
}
```

```cpp
#define K_N0 21 Q_DELAY
#define Y_SLEEP 0
#include <hip/hip_runtime.h>
#include <hip/hip_bf16.h>
#include <stdint.h>

namespace {

typedef unsigned u32;
typedef unsigned long long u64;
typedef unsigned short u16;
typedef u32 u32x4 __attribute__((ext_vector_type(4)));
typedef u32 u32x2 __attribute__((ext_vector_type(2)));
typedef float f32x4 __attribute__((ext_vector_type(4)));
typedef float f32x2 __attribute__((ext_vector_type(2)));
typedef short s16x4 __attribute__((ext_vector_type(4)));
typedef __bf16 bf16x2 __attribute__((ext_vector_type(2)));
typedef __bf16 bf16x8 __attribute__((ext_vector_type(8)));
typedef __amdgpu_buffer_rsrc_t rsrc_t;
#define GAS __attribute__((address_space(1)))
#define LAS __attribute__((address_space(3)))

constexpr int DM = 640, NH = 32, SK = 8192, DH = 128, FF = 1728;
constexpr int NWG = 256, NTH = 768;
constexpr int NFFN = FF / 8;
constexpr int CONS0 = 96;
constexpr u32 WS_E = 0;
constexpr u32 WS_ML = 4096;
constexpr u32 WS_Q = 8192;
constexpr u32 WS_H = WS_Q + NH * 8 * 2 * 64 * 16;
constexpr u32 WS_F = WS_H + NFFN * 64 * 16;
constexpr u32 WS_Y = WS_F + NFFN * 80 * 16;
constexpr u32 WS_END = WS_Y + NWG * 320 * 16;
constexpr u32 OOB = 0x7ffffff0u;
constexpr int AUX_NT = 2, AUX_SC1 = 16;
constexpr u32 SPIN_CAP = 1u << 16;

#define WQ_AUX 2
#define W_AUX 2
#define X_AUX 0
#define K_AUX 2
#define V_AUX 2
#define FIXP 0
#define Y_DELAY 0
#define E_AUX 0
#define VSIG 40
#define POLL_SLEEP 1
#define Q_DELAY 0
#define K_PRE 3
#define K_A 5
#define PACE 3
#define RING 3
#define BUMP 3
#define RING_END RING
#define RAMP_AT 96
#define RAMP_STEP 2
constexpr int N0 = K_N0, R = RING;
constexpr int target(int i) {
  int t = i + 1 + R;
  const int top = N0 + (i + 1 < R ? i + 1 : R);
  if (t < top) t = top;
  if (i >= 64 - BUMP && i < 64) t = i + 1 + R + (i - (64 - BUMP) + 1);
  if (i >= 64 && t < 64 + R + BUMP) t = 64 + R + BUMP;
  if (i >= RAMP_AT) { int rr = R + (i - RAMP_AT) / RAMP_STEP + 1; if (rr > RING_END) rr = RING_END; if (t < i + 1 + rr) t = i + 1 + rr; }
  return t > 128 ? 128 : t;
}

struct Params {
  const void* x; const void* k; const void* v; const void* wq; const void* wo;
  const void* w1; const void* b1; const void* w2; const void* b2; void* out; void* ws;
};

__device__ __forceinline__ rsrc_t mk_rsrc(const void* p, u32 bytes) { return __builtin_amdgcn_make_buffer_rsrc((void*)p, 0, (int)bytes, 0x00020000); }
template <int AUX> __device__ __forceinline__ u32x4 bld128(rsrc_t r, u32 voff, u32 soff) { return __builtin_amdgcn_raw_buffer_load_b128(r, (int)voff, (int)soff, AUX); }
template <int AUX> __device__ __forceinline__ u32 bld32(rsrc_t r, u32 voff, u32 soff) { return __builtin_amdgcn_raw_buffer_load_b32(r, (int)voff, (int)soff, AUX); }
__device__ __forceinline__ u32 bld16(rsrc_t r, u32 voff) { return (u32)__builtin_amdgcn_raw_buffer_load_b16(r, (int)voff, 0, 2); }
__device__ __forceinline__ float bflo(u32 x) { return __builtin_bit_cast(float, x << 16); }
__device__ __forceinline__ float bfhi(u32 x) { return __builtin_bit_cast(float, x & 0xffff0000u); }
__device__ __forceinline__ u32 pkbf(float lo, float hi) { bf16x2 v = {(__bf16)lo, (__bf16)hi}; return __builtin_bit_cast(u32, v); }
__device__ __forceinline__ float rbf(float x) { return bflo(pkbf(x, 0.f) & 0xffffu); }
__device__ __forceinline__ float dot2(u32 a, u32 b, float c) { return __builtin_amdgcn_fdot2_f32_bf16(__builtin_bit_cast(bf16x2, a), __builtin_bit_cast(bf16x2, b), c, false); }
template <int CTRL> __device__ __forceinline__ float dpp(float x) { return __builtin_bit_cast(float, __builtin_amdgcn_mov_dpp(__builtin_bit_cast(int, x), CTRL, 0xf, 0xf, true)); }
constexpr int XOR1 = 0xB1, XOR2 = 0x4E, ROR4 = 0x124, ROR8 = 0x128, ROR2 = 0x122, ROR1 = 0x121, BCAST = 0x150;
__device__ __forceinline__ float u2f(u32 x) { return __builtin_bit_cast(float, x); }
__device__ __forceinline__ u32 f2u(float x) { return __builtin_bit_cast(u32, x); }
__device__ __forceinline__ float tr32(float a, float b) { auto s = __builtin_amdgcn_permlane32_swap(f2u(a), f2u(b), false, false); return u2f(s[0]) + u2f(s[1]); }
__device__ __forceinline__ float tr16(float a, float b) { auto s = __builtin_amdgcn_permlane16_swap(f2u(a), f2u(b), false, false); return u2f(s[0]) + u2f(s[1]); }
__device__ __forceinline__ float rows_sum(float x) { x = tr16(x, x); return tr32(x, x); }
__device__ __forceinline__ float rows_max(float x) {
  auto s = __builtin_amdgcn_permlane16_swap(f2u(x), f2u(x), false, false); x = fmaxf(u2f(s[0]), u2f(s[1]));
  auto t = __builtin_amdgcn_permlane32_swap(f2u(x), f2u(x), false, false); return fmaxf(u2f(t[0]), u2f(t[1]));
}
__device__ __forceinline__ float row16_sum(float x) { x += dpp<ROR8>(x); x += dpp<ROR4>(x); x += dpp<ROR2>(x); x += dpp<ROR1>(x); return x; }
__device__ __forceinline__ float row16_max(float x) { x = fmaxf(x, dpp<ROR8>(x)); x = fmaxf(x, dpp<ROR4>(x)); x = fmaxf(x, dpp<ROR2>(x)); x = fmaxf(x, dpp<ROR1>(x)); return x; }
__device__ __forceinline__ float wave_sum(float x) { return rows_sum(row16_sum(x)); }
__device__ __forceinline__ float wave_max(float x) { return rows_max(row16_max(x)); }
template <int K> __device__ __forceinline__ float merge(float lo, float hi, int c) {
  const bool bit = (c >> K) & 1;
  const float keep = bit ? hi : lo, send = bit ? lo : hi;
  constexpr int CTRL = K == 0 ? XOR1 : K == 1 ? XOR2 : K == 2 ? ROR4 : ROR8;
  return keep + dpp<CTRL>(send);
}
template <int T> __device__ __forceinline__ float bcast(float x) { return dpp<BCAST + T>(x); }
#define SB() __builtin_amdgcn_sched_barrier(0)
template <int I> struct IC { static constexpr int value = I; };
template <int B, int E, class F> __device__ __forceinline__ void sfor(F&& f) { if constexpr (B < E) { f(IC<B>{}); sfor<B + 1, E>(f); } }
#define INL __attribute__((always_inline))

__device__ __forceinline__ bool tags_ok(const u32x4& g, u32 tA, u32 tB) { return g.y == tA && g.w == tB; }
template <int SLEEP = 2> __device__ __forceinline__ u32x4 wait_granule(u32x4 g, rsrc_t rs, u32 off, u32 tA, u32 tB, bool need) {
  bool ok = !need || tags_ok(g, tA, tB);
  if (!__all(ok)) {
#pragma unroll 1
    for (u32 spin = 0; spin < SPIN_CAP; ++spin) {
      if (!ok) { g = bld128<AUX_SC1>(rs, off, 0); ok = tags_ok(g, tA, tB); }
      if (__all(ok)) break;
      __builtin_amdgcn_s_sleep(SLEEP);
    }
  }
  return g;
}

template <int N, int SLEEP = POLL_SLEEP> __device__ __forceinline__ void wait_granules(u32x4 (&g)[N], rsrc_t rs, u32 off, u32 stride, u32 tA, u32 tB) {
  bool ok = true;
#pragma unroll
  for (int k = 0; k < N; ++k) ok = ok && tags_ok(g[k], tA, tB);
  if (!__all(ok)) {
#pragma unroll 1
    for (u32 spin = 0; spin < SPIN_CAP; ++spin) {
      u32x4 t[N];
#pragma unroll
      for (int k = 0; k < N; ++k) t[k] = bld128<AUX_SC1>(rs, tags_ok(g[k], tA, tB) ? OOB : off, k * stride);
      ok = true;
#pragma unroll
      for (int k = 0; k < N; ++k) { if (!tags_ok(g[k], tA, tB)) g[k] = t[k]; ok = ok && tags_ok(g[k], tA, tB); }
      if (__all(ok)) break;
      if (SLEEP) __builtin_amdgcn_s_sleep(SLEEP);
    }
  }
}
__device__ __forceinline__ void lds_wait(LAS unsigned char* word, u32 want) {
#pragma unroll 1
  for (u32 spin = 0; spin < (SPIN_CAP << 4); ++spin) {
    if (*(volatile LAS u32*)word >= want) break;
    __builtin_amdgcn_s_sleep(2);
  }
  asm volatile("" ::: "memory");
}

constexpr int L_XN = 0;
constexpr int L_QP = 2560;
constexpr int L_Q = L_QP + 4096;
constexpr int L_ML = L_Q + 256;
constexpr int L_CNT = L_ML + 64;
constexpr int L_TAG = L_CNT + 16;
constexpr int L_FW = L_CNT + 32;
constexpr int L_H2 = L_FW + 64;
constexpr int L_FR = L_H2 + 512;
constexpr int L_OA = L_FR + 3840;
constexpr int L_WO = L_OA + 16 * 272;
constexpr int L_TRASH = L_WO + 8 * 5120;
#define LDS_PAD 0
constexpr int L_END = L_TRASH + 1024 + LDS_PAD;
static_assert(L_OA % 16 == 0 && L_WO % 16 == 0 && L_FW % 16 == 0 && L_FR % 16 == 0 && L_QP % 16 == 0, "LDS alignment");

#define STAMP(n) do {} while (0)
#define STAMP_FLUSH() do {} while (0)

__global__ void __launch_bounds__(NTH, 3) fused_block(Params p) {
  __shared__ __attribute__((aligned(16))) unsigned char lds[L_END];
  const int tid = threadIdx.x, lane = tid & 63, w = __builtin_amdgcn_readfirstlane(tid >> 6), g4 = lane >> 4, c = lane & 15;
  const int bid = blockIdx.x, xc = bid & 7, rr = bid >> 3;
#define HMAP 0
  const int h = HMAP == 0 ? (rr >> 3) * 8 + xc : HMAP == 1 ? (rr >> 3) * 8 + ((xc + 2 * (rr >> 3)) & 7) : (rr >> 3) * 8 + ((xc + 3 * (rr >> 3)) & 7), b = (rr >> 2) & 1, s = rr & 3;
  const int pair = b * NH + h, prod = pair * 4 + s;
  const int fu = xc + 8 * (rr >> 3), fv = rr & 7;
  const bool is_ffn = fu < 27;
  const bool is_cons = bid >= CONS0;
  const int cj = bid - CONS0, cb = is_cons ? cj / 80 : 0, coct = is_cons ? cj % 80 : 0;
  STAMP(0);

  const rsrc_t rsWS = mk_rsrc(p.ws, WS_END);
  const rsrc_t rsK = mk_rsrc((const char*)p.k + (size_t)pair * (SK * DH * 2) + (size_t)s * (2048 * DH * 2), 2048 * DH * 2);
  const rsrc_t rsV = mk_rsrc((const char*)p.v + (size_t)pair * (SK * DH * 2) + (size_t)s * (2048 * DH * 2), 2048 * DH * 2);
  const rsrc_t rsX = mk_rsrc(p.x, 2 * DM * 2);
  const rsrc_t rsWQ = mk_rsrc(p.wq, DM * NH * DH * 2);
  const rsrc_t rsWO = mk_rsrc((const char*)p.wo + (size_t)h * (DH * DM * 2), DH * DM * 2);
  const rsrc_t rsW1 = mk_rsrc(p.w1, is_ffn ? DM * FF * 2 : 0);
  const rsrc_t rsW2 = mk_rsrc(p.w2, is_ffn ? FF * DM * 2 : 0);
  const rsrc_t rsFX = rsWS;
#define FX_WAIT 1
  const rsrc_t rsB1 = mk_rsrc(p.b1, is_ffn ? FF * 2 : 0);
  const rsrc_t rsB2 = mk_rsrc(p.b2, DM * 2);

  auto ln_stats = [&](const u32x4& ra, u32 rb, float& mb, float& inv) INL {
    float v[10];
#pragma unroll
    for (int k = 0; k < 4; ++k) { v[2 * k] = bflo(ra[k]); v[2 * k + 1] = bfhi(ra[k]); }
    v[8] = bflo(rb); v[9] = bfhi(rb);
    float sum = 0.f;
#pragma unroll
    for (int k = 0; k < 10; ++k) sum += v[k];
    const float mean = wave_sum(sum) * (1.f / DM);
    float sq = 0.f;
#pragma unroll
    for (int k = 0; k < 10; ++k) { const float d = v[k] - mean; sq = fmaf(d, d, sq); }
    const float var = wave_sum(sq) * (1.f / DM);
    mb = rbf(mean);
    inv = 1.f / rbf(sqrtf(rbf(rbf(var) + 1e-5f)));
  };
  auto ln_row = [&](const u32x4& ra, u32 rb, float mb, float inv, int dst) INL {
#pragma unroll
    for (int k = 0; k < 5; ++k) {
      const u32 r = k < 4 ? ra[k & 3] : rb;
      const u32 d = pkbf(bflo(r) - mb, bfhi(r) - mb);
      *(u32*)(lds + dst + lane * 20 + 4 * k) = pkbf(bflo(d) * inv, bfhi(d) * inv);
    }
  };

  if (w >= 8) {
    const int hw = w - 8;
    if (hw == 0) { *(volatile LAS u32*)(LAS unsigned char*)(lds + L_CNT) = 0u; *(volatile LAS u32*)(LAS unsigned char*)(lds + L_CNT + 4) = 0u; *(volatile LAS u32*)(LAS unsigned char*)(lds + L_CNT + 8) = 0u; }
    __syncthreads();
    const u32x4 x0a = bld128<X_AUX>(rsX, lane * 20, 0), x1a = bld128<X_AUX>(rsX, 1280 + lane * 20, 0);
    const u32 x0b = bld32<X_AUX>(rsX, lane * 20 + 16, 0), x1b = bld32<X_AUX>(rsX, 1280 + lane * 20 + 16, 0);
    const int qrow = 80 * (4 * b + s) + 20 * hw + 5 * g4;
    u32x4 WQ[5];
#pragma unroll
    for (int k = 0; k < 5; ++k) WQ[k] = bld128<WQ_AUX>(rsWQ, qrow * 8192 + c * 16, k * 8192 + h * 256);
    typedef u32 u32x3 __attribute__((ext_vector_type(3)));
    const u32x3 xr0 = __builtin_amdgcn_raw_buffer_load_b96(rsX, (qrow * 2) & ~3, 0, X_AUX), xr1 = __builtin_amdgcn_raw_buffer_load_b96(rsX, 1280 + ((qrow * 2) & ~3), 0, X_AUX);
    const u32x2 E2 = __builtin_bit_cast(u32x2, __builtin_amdgcn_raw_buffer_load_b64(rsWS, hw == 0 ? (int)WS_E : (int)OOB, 0, E_AUX));
    const u64 E = (u64)E2.x | ((u64)E2.y << 32);
    SB();
    float mb0, inv0, mb1, inv1;
    ln_stats(x0a, x0b, mb0, inv0);
    ln_stats(x1a, x1b, mb1, inv1);
    STAMP(9);
    {
      float a[16];
#pragma unroll
      for (int k = 0; k < 16; ++k) a[k] = 0.f;
#pragma unroll
      for (int k = 0; k < 5; ++k) {
        constexpr int dummy = 0; (void)dummy;
        const int e0 = k, e1 = k + 1;
        const float r0 = (qrow & 1) ? ((e1 & 1) ? bfhi(xr0[e1 >> 1]) : bflo(xr0[e1 >> 1])) : ((e0 & 1) ? bfhi(xr0[e0 >> 1]) : bflo(xr0[e0 >> 1]));
        const float r1 = (qrow & 1) ? ((e1 & 1) ? bfhi(xr1[e1 >> 1]) : bflo(xr1[e1 >> 1])) : ((e0 & 1) ? bfhi(xr1[e0 >> 1]) : bflo(xr1[e0 >> 1]));
        const float x0 = rbf(rbf(r0 - mb0) * inv0), x1 = rbf(rbf(r1 - mb1) * inv1);
#pragma unroll
        for (int u = 0; u < 4; ++u) {
          const float lo = bflo(WQ[k][u]), hi = bfhi(WQ[k][u]);
          a[2 * u] = fmaf(x0, lo, a[2 * u]); a[2 * u + 1] = fmaf(x0, hi, a[2 * u + 1]);
          a[8 + 2 * u] = fmaf(x1, lo, a[8 + 2 * u]); a[8 + 2 * u + 1] = fmaf(x1, hi, a[8 + 2 * u + 1]);
        }
      }
#pragma unroll
      for (int e = 0; e < 8; ++e) a[e] = tr32(a[e], a[8 + e]);
      const f32x4 z = {tr16(a[0], a[4]), tr16(a[1], a[5]), tr16(a[2], a[6]), tr16(a[3], a[7])};
      *(f32x4*)(lds + L_QP + (hw * 256 + (g4 >> 1) * 128 + 8 * c + 4 * (g4 & 1)) * 4) = z;
      asm volatile("s_waitcnt lgkmcnt(0)" ::: "memory");
      if (lane == 0) __hip_atomic_fetch_add((LAS u32*)(LAS unsigned char*)(lds + L_CNT + 4), 1u, __ATOMIC_RELAXED, __HIP_MEMORY_SCOPE_WORKGROUP);
    }
    if (hw != 0) return;
    const u32 tA = (u32)E + 0x9E3779B9u, tB = (u32)(E >> 32) + 0x85EBCA6Bu;
    *(u32x2*)(lds + (lane == 0 ? L_TAG : L_TRASH + lane * 8)) = u32x2{tA, tB};
    lds_wait((LAS unsigned char*)(lds + L_CNT + 4), 4u);
    STAMP(7);
    {
      f32x2 t0 = {0.f, 0.f}, t1 = {0.f, 0.f};
#pragma unroll
      for (int k = 0; k < 4; ++k) { t0 += *(const f32x2*)(lds + L_QP + (k * 256 + 2 * lane) * 4); t1 += *(const f32x2*)(lds + L_QP + (k * 256 + 128 + 2 * lane) * 4); }
      const u32 offP = WS_Q + (((h * 8 + b * 4 + s) * 2) * 64 + lane) * 16;
      __builtin_amdgcn_raw_buffer_store_b128(u32x4{f2u(t0.x), tA, f2u(t0.y), tB}, rsWS, (int)offP, 0, AUX_SC1);
      __builtin_amdgcn_raw_buffer_store_b128(u32x4{f2u(t1.x), tA, f2u(t1.y), tB}, rsWS, (int)offP, 64 * 16, AUX_SC1);
      const u32 offQ = WS_Q + ((h * 8 * 2 + b) * 64 + lane) * 16;
      u32x4 g[8];
      if (Q_DELAY) __builtin_amdgcn_s_sleep(Q_DELAY);
#pragma unroll
      for (int k = 0; k < 8; ++k) g[k] = bld128<AUX_SC1>(rsWS, offQ, k * 2 * 64 * 16);
      STAMP(6);
      wait_granules<8>(g, rsWS, offQ, 2 * 64 * 16, tA, tB);
      float q0 = 0.f, q1 = 0.f;
#pragma unroll
      for (int k = 0; k < 8; ++k) { q0 += u2f(g[k].x); q1 += u2f(g[k].z); }
      *(u32*)(lds + L_Q + lane * 4) = pkbf(q0, q1);
    }
    STAMP(1);
    __syncthreads();
    const u32 B2v = bld32<X_AUX>(rsB2, (8 * coct + 2 * (lane & 3)) * 2, 0), XV = bld32<X_AUX>(rsX, cb * 1280 + (8 * coct + 2 * (lane & 3)) * 2, 0);
    ln_row(x0a, x0b, mb0, inv0, L_XN);
    ln_row(x1a, x1b, mb1, inv1, L_XN + 1280);
    if (is_ffn) {
      u32x4 W1[10];
#pragma unroll
      for (int k = 0; k < 10; ++k) W1[k] = bld128<W_AUX>(rsW1, (lane >> 3) * (FF * 2) + (lane & 7) * 16, (80 * fv + 8 * k) * (FF * 2) + fu * 128);
      SB();
      float hv[16];
#pragma unroll
      for (int k = 0; k < 16; ++k) hv[k] = 0.f;
#pragma unroll
      for (int k = 0; k < 10; ++k) {
        const int row = 80 * fv + 8 * k + (lane >> 3);
        const float x0 = bflo(*(const u16*)(lds + L_XN + row * 2)), x1 = bflo(*(const u16*)(lds + L_XN + 1280 + row * 2));
#pragma unroll
        for (int u = 0; u < 4; ++u) {
          const float lo = bflo(W1[k][u]), hi = bfhi(W1[k][u]);
          hv[2 * u] = fmaf(x0, lo, hv[2 * u]); hv[2 * u + 1] = fmaf(x0, hi, hv[2 * u + 1]);
          hv[8 + 2 * u] = fmaf(x1, lo, hv[8 + 2 * u]); hv[8 + 2 * u + 1] = fmaf(x1, hi, hv[8 + 2 * u + 1]);
        }
      }
      SB();
#pragma unroll
      for (int e = 0; e < 8; ++e) hv[e] = merge<3>(hv[e], hv[8 + e], c);
#pragma unroll
      for (int e = 0; e < 4; ++e) hv[e] = tr16(hv[e], hv[e + 4]);
      const float h0 = tr32(hv[0], hv[2]), h1 = tr32(hv[1], hv[3]);
      const u32 gi = ((lane >> 3) & 1) * 32 + 4 * (lane & 7) + 2 * ((lane >> 4) & 1) + (lane >> 5);
      __builtin_amdgcn_raw_buffer_store_b128(u32x4{f2u(h0), tA, f2u(h1), tB}, rsFX, (int)(WS_H + ((fu * 8 + fv) * 64 + gi) * 16), 0, AUX_SC1);
    }
    STAMP(2);
      lds_wait((LAS unsigned char*)(lds + L_CNT), 8u);
      const f32x2 ml = *(const f32x2*)(lds + L_ML + (lane & 7) * 8);
      const float mg = wave_max(ml.x);
      const float lg = row16_sum(c < 8 ? ml.y * __builtin_amdgcn_exp2f(ml.x - mg) : 0.f);
      __builtin_amdgcn_raw_buffer_store_b128(u32x4{f2u(mg), tA, f2u(lg), tB}, rsWS, (int)(lane == 0 ? WS_ML + prod * 16 : OOB), 0, AUX_SC1);
      STAMP(3);
    if (is_ffn) {
      u32x4 W2[11];
      const u32 B1v = bld32<W_AUX>(rsB1, (lane & 31) * 4, fu * 128);
      const int rg = lane / 10, ch = lane - 10 * rg;
#pragma unroll
      for (int k = 0; k < 11; ++k) W2[k] = bld128<W_AUX>(rsW2, (lane < 60 && 6 * k + rg < 64) ? rg * (DM * 2) + ch * 16 : OOB, (64 * fu + 6 * k) * (DM * 2) + fv * 160);
      SB();
      const u32 offH = WS_H + (fu * 8 * 64 + lane) * 16;
      u32x4 g[8];
#pragma unroll
      for (int k = 0; k < 8; ++k) g[k] = bld128<AUX_SC1>(rsFX, offH, k * 64 * 16);
      if (FX_WAIT) wait_granules<8>(g, rsWS, offH, 64 * 16, tA, tB);
      float s0 = 0.f, s1 = 0.f;
#pragma unroll
      for (int k = 0; k < 8; ++k) { s0 += u2f(g[k].x); s1 += u2f(g[k].z); }
      s0 = rbf(rbf(s0) + bflo(B1v)); s1 = rbf(rbf(s1) + bfhi(B1v));
      s0 = s0 > 0.f ? s0 : 0.f; s1 = s1 > 0.f ? s1 : 0.f;
      *(f32x2*)(lds + L_H2 + lane * 8) = f32x2{rbf(s0 * s0), rbf(s1 * s1)};
      float ov[16];
#pragma unroll
      for (int k = 0; k < 16; ++k) ov[k] = 0.f;
#pragma unroll
      for (int k = 0; k < 11; ++k) {
        const int r = (6 * k + rg) & 63;
        const float a0 = *(const float*)(lds + L_H2 + r * 4), a1 = *(const float*)(lds + L_H2 + 256 + r * 4);
#pragma unroll
        for (int u = 0; u < 4; ++u) {
          const float lo = bflo(W2[k][u]), hi = bfhi(W2[k][u]);
          ov[2 * u] = fmaf(a0, lo, ov[2 * u]); ov[2 * u + 1] = fmaf(a0, hi, ov[2 * u + 1]);
          ov[8 + 2 * u] = fmaf(a1, lo, ov[8 + 2 * u]); ov[8 + 2 * u + 1] = fmaf(a1, hi, ov[8 + 2 * u + 1]);
        }
      }
      unsigned char* fr = lds + (lane < 60 ? L_FR + lane * 64 : L_TRASH + (lane & 3) * 64);
#pragma unroll
      for (int k = 0; k < 4; ++k) *(f32x4*)(fr + 16 * k) = f32x4{ov[4 * k], ov[4 * k + 1], ov[4 * k + 2], ov[4 * k + 3]};
#pragma unroll
      for (int j = 0; j < 2; ++j) {
        const int gq = lane + 64 * j, gb = gq >= 40 ? 1 : 0, cp = (gq - 40 * gb) % 40;
        f32x2 t = {0.f, 0.f};
#pragma unroll
        for (int k = 0; k < 6; ++k) t += *(const f32x2*)(lds + L_FR + (k * 10 + (cp >> 2)) * 64 + (gb * 8 + 2 * (cp & 3)) * 4);
        __builtin_amdgcn_raw_buffer_store_b128(u32x4{f2u(t.x), tA, f2u(t.y), tB}, rsFX, (int)(gq < 80 ? WS_F + ((fu * 8 + fv) * 80 + gq) * 16 : OOB), 0, AUX_SC1);
      }
    }
    {
      const u32 offML = WS_ML + (pair * 4 + (lane & 3)) * 16;
      u32x4 g = bld128<AUX_SC1>(rsWS, offML, 0);
      g = wait_granule(g, rsWS, offML, tA, tB, true);
      const float mv = u2f(g.x), M = row16_max(mv);
      const float L = row16_sum(c < 4 ? u2f(g.z) * __builtin_amdgcn_exp2f(mv - M) : 0.f);
      *(float*)(lds + (lane < 16 ? L_FW : L_TRASH) + lane * 4) = __builtin_amdgcn_exp2f(ml.x - M) / L;
    }
    STAMP(4);
    float gate0 = 0.f, gate1 = 0.f;
    if (is_cons) lds_wait((LAS unsigned char*)(lds + L_CNT + 8), 1u);
    if (is_cons) {
      u32x4 FG[2]; u32 offF[2];
      const int cv = coct / 10, cp0 = (coct - 10 * cv) * 4;
#pragma unroll
      for (int k = 0; k < 2; ++k) {
        const int idx = lane + 64 * k;
        offF[k] = idx < 108 ? WS_F + (((idx >> 2) * 8 + cv) * 80 + cb * 40 + cp0 + (idx & 3)) * 16 : OOB;
        FG[k] = bld128<AUX_SC1>(rsFX, offF[k], 0);
      }
      FG[0] = wait_granule(FG[0], rsWS, offF[0], tA, tB, FX_WAIT);
      FG[1] = wait_granule(FG[1], rsWS, offF[1], tA, tB, FX_WAIT && lane < 44);
      float f0 = u2f(FG[0].x) + (lane < 44 ? u2f(FG[1].x) : 0.f), f1 = u2f(FG[0].z) + (lane < 44 ? u2f(FG[1].z) : 0.f);
      f0 += dpp<ROR4>(f0); f1 += dpp<ROR4>(f1); f0 += dpp<ROR8>(f0); f1 += dpp<ROR8>(f1);
      f0 = rows_sum(f0); f1 = rows_sum(f1);
      const float o0 = rbf(rbf(f0) + bflo(B2v)), o1 = rbf(rbf(f1) + bfhi(B2v));
      gate0 = rbf(o0 / (1.f + __expf(-o0))); gate1 = rbf(o1 / (1.f + __expf(-o1)));
    }
    if (bid == CONS0) {
      u32x4 g[4];
#pragma unroll
      for (int k = 0; k < 4; ++k) g[k] = bld128<AUX_SC1>(rsWS, WS_ML + lane * 16, k * 64 * 16);
      wait_granules<4>(g, rsWS, WS_ML + lane * 16, 64 * 16, tA, tB);
      if (lane == 0) __hip_atomic_store((GAS u64*)p.ws, (u64)tA | ((u64)tB << 32), __ATOMIC_RELAXED, __HIP_MEMORY_SCOPE_AGENT);
    }
    STAMP(5);
    __syncthreads();
    if (!is_cons) { STAMP_FLUSH(); return; }
    {
      const u32 offY = WS_Y + ((cb * 128 + (lane >> 2)) * 320 + 4 * coct + (lane & 3)) * 16;
      u32x4 g[8];
      if (Y_DELAY) __builtin_amdgcn_s_sleep(Y_DELAY);
#pragma unroll
      for (int k = 0; k < 8; ++k) g[k] = bld128<AUX_SC1>(rsWS, offY, k * 16 * 320 * 16);
      wait_granules<8, Y_SLEEP>(g, rsWS, offY, 16 * 320 * 16, tA, tB);
      float y0 = 0.f, y1 = 0.f;
#pragma unroll
      for (int k = 0; k < 8; ++k) { y0 += u2f(g[k].x); y1 += u2f(g[k].z); }
      y0 += dpp<ROR4>(y0); y1 += dpp<ROR4>(y1); y0 += dpp<ROR8>(y0); y1 += dpp<ROR8>(y1);
      y0 = rows_sum(y0); y1 = rows_sum(y1);
      const float r0 = bflo(XV) + rbf(rbf(y0) * gate0), r1 = bfhi(XV) + rbf(rbf(y1) * gate1);
      if (lane < 4) ((GAS u32*)p.out)[(cb * DM + 8 * coct) / 2 + lane] = pkbf(r0, r1);
    }
    STAMP(8);
    STAMP_FLUSH();
    return;
  }

  const u32 vKV = w * 4096 + lane * 16;
  u32x4 S[128];
  auto ldS = [&](int i) INL -> u32x4 { const int t = i & 63; return i < 64 ? bld128<K_AUX>(rsK, vKV + (t & 3) * 1024, (t >> 2) * 32768) : bld128<V_AUX>(rsV, vKV + (t & 3) * 1024, (t >> 2) * 32768); };
  sfor<0, PACE>([&](auto I) INL { S[decltype(I)::value] = ldS(decltype(I)::value); });
  SB();
  __syncthreads();
  sfor<PACE, N0>([&](auto I) INL {
    constexpr int n = decltype(I)::value;
    asm volatile("" :: "v"(S[n - PACE][0]));
    SB();
    S[n] = ldS(n);
    SB();
    if constexpr (n == 6) STAMP(9);
    if constexpr (n == 10) STAMP(10);
    if constexpr (n == 14) STAMP(11);
  });
  STAMP(6);
  __syncthreads();
  u32 q[4];
  { const u32x4 qq = *(const u32x4*)(lds + L_Q + c * 16); q[0] = qq[0]; q[1] = qq[1]; q[2] = qq[2]; q[3] = qq[3]; }
  STAMP(2);

  float sc[4], d0 = 0.f, m1a = 0.f, m2a = 0.f, m3a = 0.f, m1 = 0.f, m2 = 0.f;
  sfor<0, 64>([&](auto I) INL {
    constexpr int i = decltype(I)::value;
    float d = dot2(S[i][0], q[0], 0.f);
    asm volatile("" : "+v"(d));
    SB();
    sfor<(i == 0 ? N0 : target(i - 1)), target(i)>([&](auto N) INL { S[decltype(N)::value] = ldS(decltype(N)::value); });
    SB();
    d = dot2(S[i][1], q[1], d); d = dot2(S[i][2], q[2], d); d = dot2(S[i][3], q[3], d);
    asm volatile("" : "+v"(d));
    constexpr int t = i & 15;
    if ((t & 1) == 0) d0 = d;
    else {
      const float a1 = merge<0>(d0, d, c);
      if ((t & 2) == 0) m1a = a1;
      else {
        m1 = merge<1>(m1a, a1, c);
        if ((t & 4) == 0) m2a = m1;
        else {
          m2 = merge<2>(m2a, m1, c);
          if ((t & 8) == 0) m3a = m2;
          else sc[i >> 4] = merge<3>(m3a, m2, c);
        }
      }
    }
    SB();
  });
  STAMP(3);
  constexpr float C2 = 0.08838834764831845f * 1.4426950408889634f;
  float pr[4];
  {
#pragma unroll
    for (int j = 0; j < 4; ++j) sc[j] *= C2;
    const float mw = wave_max(fmaxf(fmaxf(sc[0], sc[1]), fmaxf(sc[2], sc[3])));
#pragma unroll
    for (int j = 0; j < 4; ++j) pr[j] = __builtin_amdgcn_exp2f(sc[j] - mw);
    const float lw = wave_sum((pr[0] + pr[1]) + (pr[2] + pr[3]));
    *(f32x2*)(lds + (lane == 0 ? L_ML + w * 8 : L_TRASH + lane * 8)) = f32x2{mw, lw};
    asm volatile("s_waitcnt lgkmcnt(0)" ::: "memory");
    if (lane == 0) __hip_atomic_fetch_add((LAS u32*)(LAS unsigned char*)(lds + L_CNT), 1u, __ATOMIC_RELAXED, __HIP_MEMORY_SCOPE_WORKGROUP);
  }
  SB();

  float acc[8];
#pragma unroll
  for (int k = 0; k < 8; ++k) acc[k] = 0.f;
  u32x4 WO[5], BF[4][5];
  u32 vWO[5];
#pragma unroll
  for (int k = 0; k < 5; ++k) { const int it = lane + 64 * k; vWO[k] = (it / 10) * (DM * 2) + (it % 10) * 16 + w * 160; }
  sfor<64, 128>([&](auto I) INL {
    constexpr int i = decltype(I)::value;
    constexpr int vi = i - 64, t = vi & 15;
    const u32x4 V = S[i];
    float v0 = bflo(V[0]);
    asm volatile("" : "+v"(v0));
    SB();
    sfor<target(i - 1), target(i)>([&](auto N) INL { S[decltype(N)::value] = ldS(decltype(N)::value); });
    if constexpr (vi == VSIG) { if (tid == 0) __hip_atomic_fetch_add((LAS u32*)(LAS unsigned char*)(lds + L_CNT + 8), 1u, __ATOMIC_RELAXED, __HIP_MEMORY_SCOPE_WORKGROUP); }
    if constexpr (vi >= 2 && vi < 50 && (vi - 2) % 12 < 10 && (vi - 2) % 2 == 0) { const int rho = (vi - 2) / 12, k = ((vi - 2) % 12) / 2;
 WO[k] = bld128<W_AUX>(rsWO, vWO[k], rho * 32 * DM * 2);
 }
    SB();
    {
      const float pb = bcast<t>(pr[vi >> 4]);
      acc[0] = fmaf(pb, v0, acc[0]); acc[1] = fmaf(pb, bfhi(V[0]), acc[1]);
#pragma unroll
      for (int u = 1; u < 4; ++u) { acc[2 * u] = fmaf(pb, bflo(V[u]), acc[2 * u]); acc[2 * u + 1] = fmaf(pb, bfhi(V[u]), acc[2 * u + 1]); }
      asm volatile("" : "+v"(acc[0]), "+v"(acc[1]), "+v"(acc[2]), "+v"(acc[3]), "+v"(acc[4]), "+v"(acc[5]), "+v"(acc[6]), "+v"(acc[7]));
    }
    if constexpr (vi >= 13 && vi <= 49 && (vi - 13) % 12 == 0) {
      unsigned char* st = lds + L_WO + w * 5120;
#pragma unroll
      for (int k = 0; k < 5; ++k) *(u32x4*)(st + (lane + 64 * k) * 16) = WO[k];
    }
    if constexpr (vi >= 14 && vi <= 50 && (vi - 14) % 12 == 0) {
      constexpr int rho = (vi - 14) / 12;
      LAS unsigned char* st = (LAS unsigned char*)(lds + L_WO + w * 5120);
#pragma unroll
      for (int n = 0; n < 5; ++n) {
        LAS unsigned char* a = st + (8 * g4 + (c >> 2)) * 160 + 32 * n + 8 * (c & 3);
        const u32x2 lo = __builtin_bit_cast(u32x2, __builtin_amdgcn_ds_read_tr16_b64_v4i16((LAS s16x4*)a));
        const u32x2 hi = __builtin_bit_cast(u32x2, __builtin_amdgcn_ds_read_tr16_b64_v4i16((LAS s16x4*)(a + 4 * 160)));
        BF[rho][n] = u32x4{lo.x, lo.y, hi.x, hi.y};
      }
    }
    if constexpr (vi >= 16 && vi <= 52 && (vi - 16) % 12 == 0) {
      constexpr int rho = (vi - 16) / 12;
#pragma unroll
      for (int n = 0; n < 5; ++n) asm volatile("" : "+v"(BF[rho][n]));
    }
    SB();
  });
  STAMP(4);

  {
    const float z0 = tr16(tr32(acc[0], acc[4]), tr32(acc[2], acc[6])), z1 = tr16(tr32(acc[1], acc[5]), tr32(acc[3], acc[7]));
    const u32 hi = pkbf(z0, z1), lo = pkbf(z0 - bflo(hi), z1 - bfhi(hi));
    *(u32*)(lds + L_OA + w * 272 + 16 * c + 4 * g4) = hi;
    *(u32*)(lds + L_OA + (8 + w) * 272 + 16 * c + 4 * g4) = lo;
  }
  __syncthreads();
  const u32x2 tg = *(const u32x2*)(lds + L_TAG);
  const u32 tA = tg.x, tB = tg.y;
  {
    f32x4 D[5];
#pragma unroll
    for (int n = 0; n < 5; ++n) D[n] = f32x4{0.f, 0.f, 0.f, 0.f};
    const f32x4 fr = *(const f32x4*)(lds + L_FW + 16 * g4);
#pragma unroll
    for (int rho = 0; rho < 4; ++rho) {
      const u32x4 A = *(const u32x4*)(lds + L_OA + c * 272 + 64 * rho + 16 * g4);
#pragma unroll
      for (int n = 0; n < 5; ++n) D[n] = __builtin_amdgcn_mfma_f32_16x16x32_bf16(__builtin_bit_cast(bf16x8, A), __builtin_bit_cast(bf16x8, BF[rho][n]), D[n], 0, 0, 0);
    }
    float e[5];
#pragma unroll
    for (int n = 0; n < 5; ++n) e[n] = fmaf(D[n][0], fr[0], D[n][1] * fr[1]) + fmaf(D[n][2], fr[2], D[n][3] * fr[3]);
    const float ya = tr16(tr32(e[0], e[2]), tr32(e[1], e[3]));
    const float yb = rows_sum(e[4]);
    const float yan = dpp<XOR1>(ya), ybn = dpp<XOR1>(yb);
    const bool odd = c & 1;
    const u32 cp = odd ? 40 * w + 32 + (c >> 1) : 40 * w + 8 * g4 + (c >> 1);
    const u32 off = (!odd || g4 == 0) ? WS_Y + (prod * 320 + cp) * 16 : OOB;
    const u32x4 gr = odd ? u32x4{f2u(ybn), tA, f2u(yb), tB} : u32x4{f2u(ya), tA, f2u(yan), tB};
    __builtin_amdgcn_raw_buffer_store_b128(gr, rsWS, (int)off, 0, AUX_SC1);
  }
  STAMP(5);
  STAMP_FLUSH();
}

}

extern "C" void kernel_launch(void* const* d_in, const int* in_sizes, int n_in, void* d_out, int out_size, void* d_ws, size_t ws_size, hipStream_t stream) {
  (void)in_sizes; (void)n_in; (void)out_size; (void)ws_size;
  Params p;
  p.x = d_in[0]; p.k = d_in[1]; p.v = d_in[2]; p.wq = d_in[3]; p.wo = d_in[4];
  p.w1 = d_in[6]; p.b1 = d_in[7]; p.w2 = d_in[8]; p.b2 = d_in[9]; p.out = d_out;
  p.ws = d_ws;
  hipLaunchKernelGGL(fused_block, dim3(NWG), dim3(NTH), 0, stream, p);
}
```

```cpp
#define H_DELAY 200
#include <hip/hip_runtime.h>
#include <hip/hip_bf16.h>
#include <stdint.h>

namespace {

typedef unsigned u32;
typedef unsigned long long u64;
typedef unsigned short u16;
typedef u32 u32x4 __attribute__((ext_vector_type(4)));
typedef u32 u32x2 __attribute__((ext_vector_type(2)));
typedef float f32x4 __attribute__((ext_vector_type(4)));
typedef float f32x2 __attribute__((ext_vector_type(2)));
typedef short s16x4 __attribute__((ext_vector_type(4)));
typedef __bf16 bf16x2 __attribute__((ext_vector_type(2)));
typedef __bf16 bf16x8 __attribute__((ext_vector_type(8)));
typedef __amdgpu_buffer_rsrc_t rsrc_t;
#define GAS __attribute__((address_space(1)))
#define LAS __attribute__((address_space(3)))

constexpr int DM = 640, NH = 32, SK = 8192, DH = 128, FF = 1728;
constexpr int NWG = 256, NTH = 1024;
constexpr int NFFN = 216;
constexpr int CONS0 = 96;
constexpr u32 WS_E = 0;
constexpr u32 WS_ML = 4096;
constexpr u32 WS_Q = 8192;
constexpr u32 WS_H = WS_Q + NH * 8 * 2 * 64 * 16;
constexpr u32 WS_F = WS_H + NFFN * 64 * 16;
constexpr u32 WS_Y = WS_F + NFFN * 80 * 16;
constexpr u32 WS_END = WS_Y + NWG * 320 * 16;
constexpr u32 OOB = 0x7ff00000u;
constexpr int AUX_NT = 2, AUX_SC1 = 16;
constexpr u32 SPIN_CAP = 1u << 16;

#define K_AUX 2
#define V_AUX 2
#define W_AUX 0
#define K_N0 21
#define RING 3
#define BUMP 2
#define Q_DELAY 36
#define FF_WIN 2
#define POLL_SLEEP 16
#define Y_SLEEP 1
#define WO_SLEEP 8
#define WO_WIN 2
#define WO_STAG 1
#define VSIG 40
constexpr int N0 = K_N0, R = RING;
constexpr int target(int i) {
  int t = i + 1 + R;
  const int top = N0 + (i + 1 < R ? i + 1 : R);
  if (t < top) t = top;
  if (i >= 64 - BUMP && i < 64) t = i + 1 + R + (i - (64 - BUMP) + 1);
  if (i >= 64 && t < 64 + R + BUMP) t = 64 + R + BUMP;
  return t > 128 ? 128 : t;
}

struct Params {
  const void* x; const void* k; const void* v; const void* wq; const void* wo;
  const void* w1; const void* b1; const void* w2; const void* b2; void* out; void* ws;
};

__device__ __forceinline__ rsrc_t mk_rsrc(const void* p, u32 bytes) { return __builtin_amdgcn_make_buffer_rsrc((void*)p, 0, (int)bytes, 0x00020000); }
template <int AUX> __device__ __forceinline__ u32x4 bld128(rsrc_t r, u32 voff, u32 soff) { return __builtin_amdgcn_raw_buffer_load_b128(r, (int)voff, (int)soff, AUX); }
template <int AUX> __device__ __forceinline__ u32 bld32(rsrc_t r, u32 voff, u32 soff) { return __builtin_amdgcn_raw_buffer_load_b32(r, (int)voff, (int)soff, AUX); }
__device__ __forceinline__ void publish(rsrc_t r, u32 voff, u32 soff, u32 a, u32 b, u32 tA, u32 tB) { __builtin_amdgcn_raw_buffer_store_b128(u32x4{a, tA, b, tB}, r, (int)voff, (int)soff, AUX_SC1); }
__device__ __forceinline__ float bflo(u32 x) { return __builtin_bit_cast(float, x << 16); }
__device__ __forceinline__ float bfhi(u32 x) { return __builtin_bit_cast(float, x & 0xffff0000u); }
__device__ __forceinline__ u32 pkbf(float lo, float hi) { bf16x2 v = {(__bf16)lo, (__bf16)hi}; return __builtin_bit_cast(u32, v); }
__device__ __forceinline__ float rbf(float x) { return bflo(pkbf(x, 0.f) & 0xffffu); }
__device__ __forceinline__ float dot2(u32 a, u32 b, float c) { return __builtin_amdgcn_fdot2_f32_bf16(__builtin_bit_cast(bf16x2, a), __builtin_bit_cast(bf16x2, b), c, false); }
template <int CTRL> __device__ __forceinline__ float dpp(float x) { return __builtin_bit_cast(float, __builtin_amdgcn_mov_dpp(__builtin_bit_cast(int, x), CTRL, 0xf, 0xf, true)); }
constexpr int XOR1 = 0xB1, XOR2 = 0x4E, ROR4 = 0x124, ROR8 = 0x128, ROR2 = 0x122, ROR1 = 0x121, BCAST = 0x150;
__device__ __forceinline__ float u2f(u32 x) { return __builtin_bit_cast(float, x); }
__device__ __forceinline__ u32 f2u(float x) { return __builtin_bit_cast(u32, x); }
__device__ __forceinline__ float tr32(float a, float b) { auto s = __builtin_amdgcn_permlane32_swap(f2u(a), f2u(b), false, false); return u2f(s[0]) + u2f(s[1]); }
__device__ __forceinline__ float tr16(float a, float b) { auto s = __builtin_amdgcn_permlane16_swap(f2u(a), f2u(b), false, false); return u2f(s[0]) + u2f(s[1]); }
__device__ __forceinline__ float rows_sum(float x) { x = tr16(x, x); return tr32(x, x); }
__device__ __forceinline__ float rows_max(float x) {
  auto s = __builtin_amdgcn_permlane16_swap(f2u(x), f2u(x), false, false); x = fmaxf(u2f(s[0]), u2f(s[1]));
  auto t = __builtin_amdgcn_permlane32_swap(f2u(x), f2u(x), false, false); return fmaxf(u2f(t[0]), u2f(t[1]));
}
__device__ __forceinline__ float row16_sum(float x) { x += dpp<ROR8>(x); x += dpp<ROR4>(x); x += dpp<ROR2>(x); x += dpp<ROR1>(x); return x; }
__device__ __forceinline__ float row16_max(float x) { x = fmaxf(x, dpp<ROR8>(x)); x = fmaxf(x, dpp<ROR4>(x)); x = fmaxf(x, dpp<ROR2>(x)); x = fmaxf(x, dpp<ROR1>(x)); return x; }
__device__ __forceinline__ float wave_sum(float x) { return rows_sum(row16_sum(x)); }
__device__ __forceinline__ float wave_max(float x) { return rows_max(row16_max(x)); }
template <int K> __device__ __forceinline__ float merge(float lo, float hi, int c) {
  const bool bit = (c >> K) & 1;
  const float keep = bit ? hi : lo, send = bit ? lo : hi;
  constexpr int CTRL = K == 0 ? XOR1 : K == 1 ? XOR2 : K == 2 ? ROR4 : ROR8;
  return keep + dpp<CTRL>(send);
}
template <int T> __device__ __forceinline__ float bcast(float x) { return dpp<BCAST + T>(x); }
#define SB() __builtin_amdgcn_sched_barrier(0)
template <int I> struct IC { static constexpr int value = I; };
template <int B, int E, class F> __device__ __forceinline__ void sfor(F&& f) { if constexpr (B < E) { f(IC<B>{}); sfor<B + 1, E>(f); } }
#define INL __attribute__((always_inline))

__device__ __forceinline__ bool tags_ok(const u32x4& g, u32 tA, u32 tB) { return g.y == tA && g.w == tB; }
template <int N, int SLEEP, int DELAY>
__device__ __forceinline__ void gather(u32x4 (&g)[N], rsrc_t rs, u32 off, u32 stride, u32 tA, u32 tB, bool need_last = true) {
  if (DELAY) __builtin_amdgcn_s_sleep(DELAY);
#pragma unroll
  for (int k = 0; k < N; ++k) g[k] = bld128<AUX_SC1>(rs, (k < N - 1 || need_last) ? off : OOB, k * stride);
  if (!need_last) { g[N - 1].x = 0; g[N - 1].y = tA; g[N - 1].z = 0; g[N - 1].w = tB; }
  bool ok = true;
#pragma unroll
  for (int k = 0; k < N; ++k) ok = ok && tags_ok(g[k], tA, tB);
  if (!__all(ok)) {
#pragma unroll 1
    for (u32 spin = 0; spin < SPIN_CAP; ++spin) {
      asm volatile("" ::: "memory");
      u32x4 t[N];
#pragma unroll
      for (int k = 0; k < N; ++k) t[k] = bld128<AUX_SC1>(rs, tags_ok(g[k], tA, tB) ? OOB : off, k * stride);
      ok = true;
#pragma unroll
      for (int k = 0; k < N; ++k) { if (!tags_ok(g[k], tA, tB)) g[k] = t[k]; ok = ok && tags_ok(g[k], tA, tB); }
      if (__all(ok)) break;
      if (SLEEP) __builtin_amdgcn_s_sleep(SLEEP);
    }
  }
}
__device__ __forceinline__ void lds_wait(unsigned char* word, u32 want) {
#pragma unroll 1
  for (u32 spin = 0; spin < (SPIN_CAP << 4); ++spin) {
    if (*(volatile LAS u32*)(LAS unsigned char*)word >= want) break;
    __builtin_amdgcn_s_sleep(2);
  }
  asm volatile("" ::: "memory");
}
__device__ __forceinline__ void lds_signal(unsigned char* word, int lane) {
  asm volatile("s_waitcnt lgkmcnt(0)" ::: "memory");
  if (lane == 0) __hip_atomic_fetch_add((LAS u32*)(LAS unsigned char*)word, 1u, __ATOMIC_RELAXED, __HIP_MEMORY_SCOPE_WORKGROUP);
}

constexpr int L_XN = 0;
constexpr int L_QP = 2560;
constexpr int L_Q = L_QP + 8192;
constexpr int L_ML = L_Q + 256;
constexpr int L_CNT = L_ML + 64;
constexpr int L_TAG = L_CNT + 16;
constexpr int L_FW = L_CNT + 32;
constexpr int L_YP = L_FW + 64;
constexpr int L_H2 = L_YP + 256;
constexpr int L_FR = L_H2 + 512;
constexpr int L_OA = L_FR + 3840;
constexpr int L_WO = L_OA + 16 * 272;
constexpr int L_TRASH = L_WO + 8 * 5120;
constexpr int L_END = L_TRASH + 1024;
static_assert(L_OA % 16 == 0 && L_WO % 16 == 0 && L_FW % 16 == 0 && L_FR % 16 == 0 && L_QP % 16 == 0 && L_YP % 16 == 0, "LDS alignment");

#define STAMP(n) do {} while (0)
#define STAMP_FLUSH() do {} while (0)

__global__ void __launch_bounds__(NTH, 4) fused_block(Params p) {
  __shared__ __attribute__((aligned(16))) unsigned char lds[L_END];
  const int tid = threadIdx.x, lane = tid & 63, w = __builtin_amdgcn_readfirstlane(tid >> 6), g4 = lane >> 4, c = lane & 15;
  const int bid = blockIdx.x, xc = bid & 7, rr = bid >> 3;
  const int h = (rr >> 3) * 8 + xc, b = (rr >> 2) & 1, s = rr & 3;
  const int pair = b * NH + h, prod = pair * 4 + s;
  STAMP(0);

  if (w >= 8) {
    const int pw = w - 8;
    const int fu = xc + 8 * (rr >> 3), fv = rr & 7;
    const bool is_ffn = fu < 27;
    const bool is_cons = bid >= CONS0;
    const int cj = bid - CONS0, cb = is_cons ? cj / 80 : 0, coct = is_cons ? cj % 80 : 0;
    const rsrc_t rsWS = mk_rsrc(p.ws, WS_END);
    const rsrc_t rsX = mk_rsrc(p.x, 2 * DM * 2);
    const rsrc_t rsWQ = mk_rsrc(p.wq, DM * NH * DH * 2);
    if (pw == 0) *(u32x4*)(lds + (lane == 0 ? L_CNT : L_TRASH + lane * 16)) = u32x4{0u, 0u, 0u, 0u};
    __syncthreads();
    const u32x4 x0a = bld128<0>(rsX, lane * 20, 0), x1a = bld128<0>(rsX, 1280 + lane * 20, 0);
    const u32 x0b = bld32<0>(rsX, lane * 20 + 16, 0), x1b = bld32<0>(rsX, 1280 + lane * 20 + 16, 0);
    const int qrow = 80 * (4 * b + s) + 10 * pw + 3 * g4;
    u32x4 WQ[3];
#pragma unroll
    for (int k = 0; k < 3; ++k) WQ[k] = bld128<W_AUX>(rsWQ, 3 * g4 + k < 10 ? (qrow + k) * 8192 + c * 16 : OOB, h * 256);
    const u32 xo = (qrow * 2) & ~3;
    const u32 xr00 = bld32<0>(rsX, xo, 0), xr01 = bld32<0>(rsX, xo + 4 < 1280 ? xo + 4 : OOB, 0), xr10 = bld32<0>(rsX, 1280 + xo, 0), xr11 = bld32<0>(rsX, 1280 + xo + 4, 0);
    const u32x2 E = __builtin_bit_cast(u32x2, __builtin_amdgcn_raw_buffer_load_b64(rsWS, pw < 2 ? (int)WS_E : (int)OOB, 0, 0));
    SB();
    auto ln_stats = [&](const u32x4& ra, u32 rb, float& mb, float& inv) INL {
      float v[10];
#pragma unroll
      for (int k = 0; k < 4; ++k) { v[2 * k] = bflo(ra[k]); v[2 * k + 1] = bfhi(ra[k]); }
      v[8] = bflo(rb); v[9] = bfhi(rb);
      float sum = 0.f;
#pragma unroll
      for (int k = 0; k < 10; ++k) sum += v[k];
      const float mean = wave_sum(sum) * (1.f / DM);
      float sq = 0.f;
#pragma unroll
      for (int k = 0; k < 10; ++k) { const float d = v[k] - mean; sq = fmaf(d, d, sq); }
      const float var = wave_sum(sq) * (1.f / DM);
      mb = rbf(mean);
      inv = 1.f / rbf(sqrtf(rbf(rbf(var) + 1e-5f)));
    };
    float mb0, inv0, mb1, inv1;
    ln_stats(x0a, x0b, mb0, inv0);
    ln_stats(x1a, x1b, mb1, inv1);
    STAMP(9);
    {
      float a[16];
#pragma unroll
      for (int k = 0; k < 16; ++k) a[k] = 0.f;
      const bool odd = qrow & 1;
#pragma unroll
      for (int k = 0; k < 3; ++k) {
        const float r0 = k == 0 ? (odd ? bfhi(xr00) : bflo(xr00)) : k == 1 ? (odd ? bflo(xr01) : bfhi(xr00)) : (odd ? bfhi(xr01) : bflo(xr01));
        const float r1 = k == 0 ? (odd ? bfhi(xr10) : bflo(xr10)) : k == 1 ? (odd ? bflo(xr11) : bfhi(xr10)) : (odd ? bfhi(xr11) : bflo(xr11));
        const float x0 = rbf(rbf(r0 - mb0) * inv0), x1 = rbf(rbf(r1 - mb1) * inv1);
#pragma unroll
        for (int u = 0; u < 4; ++u) {
          const float lo = bflo(WQ[k][u]), hi = bfhi(WQ[k][u]);
          a[2 * u] = fmaf(x0, lo, a[2 * u]); a[2 * u + 1] = fmaf(x0, hi, a[2 * u + 1]);
          a[8 + 2 * u] = fmaf(x1, lo, a[8 + 2 * u]); a[8 + 2 * u + 1] = fmaf(x1, hi, a[8 + 2 * u + 1]);
        }
      }
#pragma unroll
      for (int e = 0; e < 8; ++e) a[e] = tr32(a[e], a[8 + e]);
      const f32x4 z = {tr16(a[0], a[4]), tr16(a[1], a[5]), tr16(a[2], a[6]), tr16(a[3], a[7])};
      *(f32x4*)(lds + L_QP + (pw * 256 + (g4 >> 1) * 128 + 8 * c + 4 * (g4 & 1)) * 4) = z;
      lds_signal(lds + L_CNT + 4, lane);
    }
    const u32 tA = E.x + 0x9E3779B9u, tB = E.y + 0x85EBCA6Bu;
    if (pw == 0) {
      *(u32x2*)(lds + (lane == 0 ? L_TAG : L_TRASH + lane * 8)) = u32x2{tA, tB};
      lds_wait(lds + L_CNT + 4, 8u);
      STAMP(7);
      f32x2 t0 = {0.f, 0.f}, t1 = {0.f, 0.f};
#pragma unroll
      for (int k = 0; k < 8; ++k) { t0 += *(const f32x2*)(lds + L_QP + (k * 256 + 2 * lane) * 4); t1 += *(const f32x2*)(lds + L_QP + (k * 256 + 128 + 2 * lane) * 4); }
      const u32 offP = WS_Q + (((h * 8 + b * 4 + s) * 2) * 64 + lane) * 16;
      publish(rsWS, offP, 0, f2u(t0.x), f2u(t0.y), tA, tB);
      publish(rsWS, offP, 64 * 16, f2u(t1.x), f2u(t1.y), tA, tB);
      u32x4 g[8];
      gather<8, POLL_SLEEP, Q_DELAY>(g, rsWS, WS_Q + ((h * 8 * 2 + b) * 64 + lane) * 16, 2 * 64 * 16, tA, tB);
      float q0 = 0.f, q1 = 0.f;
#pragma unroll
      for (int k = 0; k < 8; ++k) { q0 += u2f(g[k].x); q1 += u2f(g[k].z); }
      *(u32*)(lds + L_Q + lane * 4) = pkbf(q0, q1);
      STAMP(1);
    }
    __syncthreads();

    if (pw == 1 && is_ffn) {
      const rsrc_t rsW1 = mk_rsrc(p.w1, DM * FF * 2), rsW2 = mk_rsrc(p.w2, FF * DM * 2), rsB1 = mk_rsrc(p.b1, FF * 2);
#pragma unroll
      for (int i = 0; i < 2; ++i) {
        const float mb = i ? mb1 : mb0, inv = i ? inv1 : inv0;
#pragma unroll
        for (int k = 0; k < 5; ++k) {
          const u32 r = k < 4 ? (i ? x1a[k & 3] : x0a[k & 3]) : (i ? x1b : x0b);
          const u32 d = pkbf(bflo(r) - mb, bfhi(r) - mb);
          *(u32*)(lds + L_XN + i * 1280 + lane * 20 + 4 * k) = pkbf(bflo(d) * inv, bfhi(d) * inv);
        }
      }
      {
        u32x4 W1[10];
        auto ldW1 = [&](auto K_) INL { constexpr int k = decltype(K_)::value; W1[k] = bld128<W_AUX>(rsW1, (lane >> 3) * (FF * 2) + (lane & 7) * 16, (80 * fv + 8 * k) * (FF * 2) + fu * 128); };
        sfor<0, FF_WIN>(ldW1);
        float hv[16];
#pragma unroll
        for (int k = 0; k < 16; ++k) hv[k] = 0.f;
        sfor<0, 10>([&](auto K_) INL {
          constexpr int k = decltype(K_)::value;
          const int row = 80 * fv + 8 * k + (lane >> 3);
          const float x0 = bflo(*(const u16*)(lds + L_XN + row * 2)), x1 = bflo(*(const u16*)(lds + L_XN + 1280 + row * 2));
          asm volatile("" :: "v"(W1[k][0]));
          SB();
          if constexpr (k + FF_WIN < 10) ldW1(IC<k + FF_WIN>{});
          SB();
#pragma unroll
          for (int u = 0; u < 4; ++u) {
            const float lo = bflo(W1[k][u]), hi = bfhi(W1[k][u]);
            hv[2 * u] = fmaf(x0, lo, hv[2 * u]); hv[2 * u + 1] = fmaf(x0, hi, hv[2 * u + 1]);
            hv[8 + 2 * u] = fmaf(x1, lo, hv[8 + 2 * u]); hv[8 + 2 * u + 1] = fmaf(x1, hi, hv[8 + 2 * u + 1]);
          }
          asm volatile("" : "+v"(hv[0]), "+v"(hv[1]), "+v"(hv[2]), "+v"(hv[3]), "+v"(hv[4]), "+v"(hv[5]), "+v"(hv[6]), "+v"(hv[7]));
          asm volatile("" : "+v"(hv[8]), "+v"(hv[9]), "+v"(hv[10]), "+v"(hv[11]), "+v"(hv[12]), "+v"(hv[13]), "+v"(hv[14]), "+v"(hv[15]));
          SB();
        });
#pragma unroll
        for (int e = 0; e < 8; ++e) hv[e] = merge<3>(hv[e], hv[8 + e], c);
#pragma unroll
        for (int e = 0; e < 4; ++e) hv[e] = tr16(hv[e], hv[e + 4]);
        const float h0 = tr32(hv[0], hv[2]), h1 = tr32(hv[1], hv[3]);
        const u32 gi = ((lane >> 3) & 1) * 32 + 4 * (lane & 7) + 2 * ((lane >> 4) & 1) + (lane >> 5);
        publish(rsWS, WS_H + ((fu * 8 + fv) * 64 + gi) * 16, 0, f2u(h0), f2u(h1), tA, tB);
      }
      SB();
      {
        const u32 B1v = bld32<W_AUX>(rsB1, (lane & 31) * 4, fu * 128);
        float s0 = 0.f, s1 = 0.f;
        if (H_DELAY) for (int i = 0; i < H_DELAY / 16; ++i) __builtin_amdgcn_s_sleep(16);
        sfor<0, 8 / FF_WIN>([&](auto J_) INL {
          constexpr int j = decltype(J_)::value;
          u32x4 g[FF_WIN];
          gather<FF_WIN, POLL_SLEEP, 0>(g, rsWS, WS_H + ((fu * 8 + j * FF_WIN) * 64 + lane) * 16, 64 * 16, tA, tB);
#pragma unroll
          for (int k = 0; k < FF_WIN; ++k) { s0 += u2f(g[k].x); s1 += u2f(g[k].z); }
          asm volatile("" : "+v"(s0), "+v"(s1));
          SB();
        });
        u32x4 W2[11];
        const int rg = lane / 10, ch = lane - 10 * rg;
        auto ldW2 = [&](auto K_) INL { constexpr int k = decltype(K_)::value; W2[k] = bld128<W_AUX>(rsW2, (lane < 60 && 6 * k + rg < 64) ? rg * (DM * 2) + ch * 16 : OOB, (64 * fu + 6 * k) * (DM * 2) + fv * 160); };
        sfor<0, FF_WIN>(ldW2);
        SB();
        s0 = rbf(rbf(s0) + bflo(B1v)); s1 = rbf(rbf(s1) + bfhi(B1v));
        s0 = s0 > 0.f ? s0 : 0.f; s1 = s1 > 0.f ? s1 : 0.f;
        *(f32x2*)(lds + L_H2 + lane * 8) = f32x2{rbf(s0 * s0), rbf(s1 * s1)};
        float ov[16];
#pragma unroll
        for (int k = 0; k < 16; ++k) ov[k] = 0.f;
        sfor<0, 11>([&](auto K_) INL {
          constexpr int k = decltype(K_)::value;
          const int r = (6 * k + rg) & 63;
          const float a0 = *(const float*)(lds + L_H2 + r * 4), a1 = *(const float*)(lds + L_H2 + 256 + r * 4);
          asm volatile("" :: "v"(W2[k][0]));
          SB();
          if constexpr (k + FF_WIN < 11) ldW2(IC<k + FF_WIN>{});
          SB();
#pragma unroll
          for (int u = 0; u < 4; ++u) {
            const float lo = bflo(W2[k][u]), hi = bfhi(W2[k][u]);
            ov[2 * u] = fmaf(a0, lo, ov[2 * u]); ov[2 * u + 1] = fmaf(a0, hi, ov[2 * u + 1]);
            ov[8 + 2 * u] = fmaf(a1, lo, ov[8 + 2 * u]); ov[8 + 2 * u + 1] = fmaf(a1, hi, ov[8 + 2 * u + 1]);
          }
          asm volatile("" : "+v"(ov[0]), "+v"(ov[1]), "+v"(ov[2]), "+v"(ov[3]), "+v"(ov[4]), "+v"(ov[5]), "+v"(ov[6]), "+v"(ov[7]));
          asm volatile("" : "+v"(ov[8]), "+v"(ov[9]), "+v"(ov[10]), "+v"(ov[11]), "+v"(ov[12]), "+v"(ov[13]), "+v"(ov[14]), "+v"(ov[15]));
          SB();
        });
        unsigned char* fr = lds + (lane < 60 ? L_FR + lane * 64 : L_TRASH + (lane & 3) * 64);
#pragma unroll
        for (int k = 0; k < 4; ++k) *(f32x4*)(fr + 16 * k) = f32x4{ov[4 * k], ov[4 * k + 1], ov[4 * k + 2], ov[4 * k + 3]};
#pragma unroll
        for (int j = 0; j < 2; ++j) {
          const int gq = lane + 64 * j, gb = gq >= 40 ? 1 : 0, cp = (gq - 40 * gb) % 40;
          f32x2 t = {0.f, 0.f};
#pragma unroll
          for (int k = 0; k < 6; ++k) t += *(const f32x2*)(lds + L_FR + (k * 10 + (cp >> 2)) * 64 + (gb * 8 + 2 * (cp & 3)) * 4);
          publish(rsWS, gq < 80 ? WS_F + ((fu * 8 + fv) * 80 + gq) * 16 : OOB, 0, f2u(t.x), f2u(t.y), tA, tB);
        }
      }
      SB();
    }

    u32x4 BF[4][5];
    {
      const rsrc_t rsWO = mk_rsrc((const char*)p.wo + (size_t)h * (DH * DM * 2), DH * DM * 2);
      u32 vWO[5];
#pragma unroll
      for (int k = 0; k < 5; ++k) { const int it = lane + 64 * k; vWO[k] = (it / 10) * (DM * 2) + (it % 10) * 16 + pw * 160; }
      unsigned char* st = lds + L_WO + pw * 5120;
      if (pw >= 2) for (int i = 0; i < (pw - 2) * WO_STAG; ++i) __builtin_amdgcn_s_sleep(16);
      sfor<0, 4>([&](auto RHO) INL {
        constexpr int rho = decltype(RHO)::value;
        u32x4 v[5];
        auto ldWO = [&](auto K_) INL {
          constexpr int k = decltype(K_)::value;
          v[k] = bld128<W_AUX>(rsWO, vWO[k], rho * 32 * DM * 2);
        };
        sfor<0, WO_WIN>(ldWO);
        sfor<0, 5>([&](auto K_) INL {
          constexpr int k = decltype(K_)::value;
          *(u32x4*)(st + (lane + 64 * k) * 16) = v[k];
          SB();
          if constexpr (k + WO_WIN < 5) ldWO(IC<k + WO_WIN>{});
          SB();
        });
#pragma unroll
        for (int n = 0; n < 5; ++n) {
          LAS unsigned char* a = (LAS unsigned char*)st + (8 * g4 + (c >> 2)) * 160 + 32 * n + 8 * (c & 3);
          const u32x2 lo = __builtin_bit_cast(u32x2, __builtin_amdgcn_ds_read_tr16_b64_v4i16((LAS s16x4*)a));
          const u32x2 hi = __builtin_bit_cast(u32x2, __builtin_amdgcn_ds_read_tr16_b64_v4i16((LAS s16x4*)(a + 4 * 160)));
          BF[rho][n] = u32x4{lo.x, lo.y, hi.x, hi.y};
          asm volatile("" : "+v"(BF[rho][n]));
        }
        if (rho < 3 && pw >= 2) for (int i = 0; i < WO_SLEEP; ++i) __builtin_amdgcn_s_sleep(16);
        SB();
      });
    }
    STAMP(2);

    float gate0 = 0.f, gate1 = 0.f;
    u32 XV = 0;
    if (pw == 0) {
      lds_wait(lds + L_CNT, 8u);
      const f32x2 ml = *(const f32x2*)(lds + L_ML + (lane & 7) * 8);
      const float mg = wave_max(ml.x);
      const float lg = row16_sum(c < 8 ? ml.y * __builtin_amdgcn_exp2f(ml.x - mg) : 0.f);
      publish(rsWS, lane == 0 ? WS_ML + prod * 16 : OOB, 0, f2u(mg), f2u(lg), tA, tB);
      STAMP(3);
      lds_wait(lds + L_CNT + 8, 1u);
      u32x4 g[1];
      gather<1, POLL_SLEEP, 0>(g, rsWS, WS_ML + (pair * 4 + (lane & 3)) * 16, 0, tA, tB);
      const float mv = u2f(g[0].x), M = row16_max(mv);
      const float L = row16_sum(c < 4 ? u2f(g[0].z) * __builtin_amdgcn_exp2f(mv - M) : 0.f);
      *(float*)(lds + (lane < 16 ? L_FW : L_TRASH) + lane * 4) = __builtin_amdgcn_exp2f(ml.x - M) / L;
      STAMP(4);
      if (bid == CONS0) {
        u32x4 e[4];
        gather<4, POLL_SLEEP, 0>(e, rsWS, WS_ML + lane * 16, 64 * 16, tA, tB);
        if (lane == 0) __hip_atomic_store((GAS u64*)p.ws, (u64)tA | ((u64)tB << 32), __ATOMIC_RELAXED, __HIP_MEMORY_SCOPE_AGENT);
      }
      if (is_cons) {
        const rsrc_t rsB2 = mk_rsrc(p.b2, DM * 2);
        const u32 B2v = bld32<0>(rsB2, (8 * coct + 2 * (lane & 3)) * 2, 0);
        XV = bld32<0>(rsX, cb * 1280 + (8 * coct + 2 * (lane & 3)) * 2, 0);
        const int cv = coct / 10, cp0 = (coct - 10 * cv) * 4;
        u32x4 f[2];
        gather<2, POLL_SLEEP, 0>(f, rsWS, WS_F + (((lane >> 2) * 8 + cv) * 80 + cb * 40 + cp0 + (lane & 3)) * 16, 16 * 8 * 80 * 16, tA, tB, lane < 44);
        float f0 = u2f(f[0].x) + u2f(f[1].x), f1 = u2f(f[0].z) + u2f(f[1].z);
        f0 += dpp<ROR4>(f0); f1 += dpp<ROR4>(f1); f0 += dpp<ROR8>(f0); f1 += dpp<ROR8>(f1);
        f0 = rows_sum(f0); f1 = rows_sum(f1);
        const float o0 = rbf(rbf(f0) + bflo(B2v)), o1 = rbf(rbf(f1) + bfhi(B2v));
        gate0 = rbf(o0 / (1.f + __expf(-o0))); gate1 = rbf(o1 / (1.f + __expf(-o1)));
      }
    }
    STAMP(5);
    __syncthreads();
    const u32x2 tg = *(const u32x2*)(lds + L_TAG);
    {
      f32x4 D[5];
#pragma unroll
      for (int n = 0; n < 5; ++n) D[n] = f32x4{0.f, 0.f, 0.f, 0.f};
      const f32x4 fr = *(const f32x4*)(lds + L_FW + 16 * g4);
#pragma unroll
      for (int rho = 0; rho < 4; ++rho) {
        const u32x4 A = *(const u32x4*)(lds + L_OA + c * 272 + 64 * rho + 16 * g4);
#pragma unroll
        for (int n = 0; n < 5; ++n) D[n] = __builtin_amdgcn_mfma_f32_16x16x32_bf16(__builtin_bit_cast(bf16x8, A), __builtin_bit_cast(bf16x8, BF[rho][n]), D[n], 0, 0, 0);
      }
      float e[5];
#pragma unroll
      for (int n = 0; n < 5; ++n) e[n] = fmaf(D[n][0], fr[0], D[n][1] * fr[1]) + fmaf(D[n][2], fr[2], D[n][3] * fr[3]);
      const float ya = tr16(tr32(e[0], e[2]), tr32(e[1], e[3]));
      const float yb = rows_sum(e[4]);
      const float yan = dpp<XOR1>(ya), ybn = dpp<XOR1>(yb);
      const bool odd = c & 1;
      const u32 cp = odd ? 40 * pw + 32 + (c >> 1) : 40 * pw + 8 * g4 + (c >> 1);
      publish(rsWS, (!odd || g4 == 0) ? WS_Y + (prod * 320 + cp) * 16 : OOB, 0, odd ? f2u(ybn) : f2u(ya), odd ? f2u(yb) : f2u(yan), tg.x, tg.y);
    }
    STAMP(6);
    if (is_cons) {
      u32x4 g[1];
      gather<1, Y_SLEEP, 0>(g, rsWS, WS_Y + ((cb * 128 + 16 * pw + (lane >> 2)) * 320 + 4 * coct + (lane & 3)) * 16, 0, tg.x, tg.y);
      float y0 = u2f(g[0].x), y1 = u2f(g[0].z);
      y0 += dpp<ROR4>(y0); y1 += dpp<ROR4>(y1); y0 += dpp<ROR8>(y0); y1 += dpp<ROR8>(y1);
      y0 = rows_sum(y0); y1 = rows_sum(y1);
      *(f32x2*)(lds + (lane < 4 ? L_YP + pw * 32 : L_TRASH) + lane * 8) = f32x2{y0, y1};
      __syncthreads();
      if (pw == 0) {
        f32x2 y = {0.f, 0.f};
#pragma unroll
        for (int k = 0; k < 8; ++k) y += *(const f32x2*)(lds + L_YP + k * 32 + (lane & 3) * 8);
        const float r0 = bflo(XV) + rbf(rbf(y.x) * gate0), r1 = bfhi(XV) + rbf(rbf(y.y) * gate1);
        if (lane < 4) ((GAS u32*)p.out)[(cb * DM + 8 * coct) / 2 + lane] = pkbf(r0, r1);
        STAMP(8);
      }
    }
    STAMP_FLUSH();
    return;
  }

  const rsrc_t rsK = mk_rsrc((const char*)p.k + (size_t)pair * (SK * DH * 2) + (size_t)s * (2048 * DH * 2), 2048 * DH * 2);
  const rsrc_t rsV = mk_rsrc((const char*)p.v + (size_t)pair * (SK * DH * 2) + (size_t)s * (2048 * DH * 2), 2048 * DH * 2);
  const u32 vKV = w * 4096 + lane * 16;
  u32x4 S[128];
  auto ldS = [&](int i) INL -> u32x4 { const int t = i & 63; return i < 64 ? bld128<K_AUX>(rsK, vKV + (t & 3) * 1024, (t >> 2) * 32768) : bld128<V_AUX>(rsV, vKV + (t & 3) * 1024, (t >> 2) * 32768); };
  sfor<0, R>([&](auto I) INL { S[decltype(I)::value] = ldS(decltype(I)::value); });
  SB();
  __syncthreads();
  sfor<R, N0>([&](auto I) INL {
    constexpr int n = decltype(I)::value;
    asm volatile("" :: "v"(S[n - R][0]));
    SB();
    S[n] = ldS(n);
    SB();
    if constexpr (n == 6) STAMP(9);
    if constexpr (n == 10) STAMP(10);
    if constexpr (n == 14) STAMP(11);
  });
  STAMP(6);
  __syncthreads();
  u32 q[4];
  { const u32x4 qq = *(const u32x4*)(lds + L_Q + c * 16); q[0] = qq[0]; q[1] = qq[1]; q[2] = qq[2]; q[3] = qq[3]; }
  STAMP(2);

  float sc[4], d0 = 0.f, m1a = 0.f, m2a = 0.f, m3a = 0.f, m1 = 0.f, m2 = 0.f;
  sfor<0, 64>([&](auto I) INL {
    constexpr int i = decltype(I)::value;
    float d = dot2(S[i][0], q[0], 0.f);
    asm volatile("" : "+v"(d));
    SB();
    sfor<(i == 0 ? N0 : target(i - 1)), target(i)>([&](auto N) INL { S[decltype(N)::value] = ldS(decltype(N)::value); });
    SB();
    d = dot2(S[i][1], q[1], d); d = dot2(S[i][2], q[2], d); d = dot2(S[i][3], q[3], d);
    asm volatile("" : "+v"(d));
    constexpr int t = i & 15;
    if ((t & 1) == 0) d0 = d;
    else {
      const float a1 = merge<0>(d0, d, c);
      if ((t & 2) == 0) m1a = a1;
      else {
        m1 = merge<1>(m1a, a1, c);
        if ((t & 4) == 0) m2a = m1;
        else {
          m2 = merge<2>(m2a, m1, c);
          if ((t & 8) == 0) m3a = m2;
          else sc[i >> 4] = merge<3>(m3a, m2, c);
        }
      }
    }
    SB();
  });
  STAMP(3);
  constexpr float C2 = 0.08838834764831845f * 1.4426950408889634f;
  float pr[4];
  {
#pragma unroll
    for (int j = 0; j < 4; ++j) sc[j] *= C2;
    const float mw = wave_max(fmaxf(fmaxf(sc[0], sc[1]), fmaxf(sc[2], sc[3])));
#pragma unroll
    for (int j = 0; j < 4; ++j) pr[j] = __builtin_amdgcn_exp2f(sc[j] - mw);
    const float lw = wave_sum((pr[0] + pr[1]) + (pr[2] + pr[3]));
    *(f32x2*)(lds + (lane == 0 ? L_ML + w * 8 : L_TRASH + lane * 8)) = f32x2{mw, lw};
    lds_signal(lds + L_CNT, lane);
  }
  SB();

  float acc[8];
#pragma unroll
  for (int k = 0; k < 8; ++k) acc[k] = 0.f;
  sfor<64, 128>([&](auto I) INL {
    constexpr int i = decltype(I)::value;
    constexpr int vi = i - 64, t = vi & 15;
    const u32x4 V = S[i];
    float v0 = bflo(V[0]);
    asm volatile("" : "+v"(v0));
    SB();
    sfor<target(i - 1), target(i)>([&](auto N) INL { S[decltype(N)::value] = ldS(decltype(N)::value); });
    if constexpr (vi == VSIG) { if (tid == 0) __hip_atomic_fetch_add((LAS u32*)(LAS unsigned char*)(lds + L_CNT + 8), 1u, __ATOMIC_RELAXED, __HIP_MEMORY_SCOPE_WORKGROUP); }
    SB();
    const float pb = bcast<t>(pr[vi >> 4]);
    acc[0] = fmaf(pb, v0, acc[0]); acc[1] = fmaf(pb, bfhi(V[0]), acc[1]);
#pragma unroll
    for (int u = 1; u < 4; ++u) { acc[2 * u] = fmaf(pb, bflo(V[u]), acc[2 * u]); acc[2 * u + 1] = fmaf(pb, bfhi(V[u]), acc[2 * u + 1]); }
    asm volatile("" : "+v"(acc[0]), "+v"(acc[1]), "+v"(acc[2]), "+v"(acc[3]), "+v"(acc[4]), "+v"(acc[5]), "+v"(acc[6]), "+v"(acc[7]));
    SB();
  });
  STAMP(4);
  {
    const float z0 = tr16(tr32(acc[0], acc[4]), tr32(acc[2], acc[6])), z1 = tr16(tr32(acc[1], acc[5]), tr32(acc[3], acc[7]));
    const u32 hi = pkbf(z0, z1), lo = pkbf(z0 - bflo(hi), z1 - bfhi(hi));
    *(u32*)(lds + L_OA + w * 272 + 16 * c + 4 * g4) = hi;
    *(u32*)(lds + L_OA + (8 + w) * 272 + 16 * c + 4 * g4) = lo;
  }
  __syncthreads();
  STAMP(5);
  STAMP_FLUSH();
}

}

extern "C" void kernel_launch(void* const* d_in, const int* in_sizes, int n_in, void* d_out, int out_size, void* d_ws, size_t ws_size, hipStream_t stream) {
  (void)in_sizes; (void)n_in; (void)out_size; (void)ws_size;
  Params p;
  p.x = d_in[0]; p.k = d_in[1]; p.v = d_in[2]; p.wq = d_in[3]; p.wo = d_in[4];
  p.w1 = d_in[6]; p.b1 = d_in[7]; p.w2 = d_in[8]; p.b2 = d_in[9]; p.out = d_out; p.ws = d_ws;
  hipLaunchKernelGGL(fused_block, dim3(NWG), dim3(NTH), 0, stream, p);
}
```

```cpp
#define H_DELAY 200
#define VSIG 46
#include <hip/hip_runtime.h>
#include <hip/hip_bf16.h>
#include <stdint.h>

namespace {

typedef unsigned u32;
typedef unsigned long long u64;
typedef unsigned short u16;
typedef u32 u32x4 __attribute__((ext_vector_type(4)));
typedef u32 u32x2 __attribute__((ext_vector_type(2)));
typedef float f32x4 __attribute__((ext_vector_type(4)));
typedef float f32x2 __attribute__((ext_vector_type(2)));
typedef short s16x4 __attribute__((ext_vector_type(4)));
typedef __bf16 bf16x2 __attribute__((ext_vector_type(2)));
typedef __bf16 bf16x8 __attribute__((ext_vector_type(8)));
typedef __amdgpu_buffer_rsrc_t rsrc_t;
#define GAS __attribute__((address_space(1)))
#define LAS __attribute__((address_space(3)))

constexpr int DM = 640, NH = 32, SK = 8192, DH = 128, FF = 1728;
constexpr int NWG = 256, NTH = 1024;
constexpr int NFFN = 216;
constexpr int CONS0 = 96;
constexpr u32 WS_E = 0;
constexpr u32 WS_ML = 4096;
constexpr u32 WS_Q = 8192;
constexpr u32 WS_H = WS_Q + NH * 8 * 2 * 64 * 16;
constexpr u32 WS_F = WS_H + NFFN * 64 * 16;
constexpr u32 WS_Y = WS_F + NFFN * 80 * 16;
constexpr u32 WS_END = WS_Y + NWG * 320 * 16;
constexpr u32 OOB = 0x7ff00000u;
constexpr int AUX_NT = 2, AUX_SC1 = 16;
constexpr u32 SPIN_CAP = 1u << 16;

#define K_AUX 2
#define V_AUX 2
#define W_AUX 0
#define K_N0 21
#define RING 3
#define BUMP 2
#define Q_DELAY 36
#define FF_WIN 2
#define POLL_SLEEP 16
#define Y_SLEEP 1
#define WO_SLEEP 8
#define WO_WIN 2
#define WO_STAG 1
#define TOUCH 0
constexpr int N0 = K_N0, R = RING;
#define RNUM 0
#define RDEN 4
constexpr int target(int i) {
  int t = i + 1 + R + ((i % RDEN) < RNUM ? 1 : 0);
  const int top = N0 + (i + 1 < R ? i + 1 : R);
  if (t < top) t = top;
  if (i >= 64 - BUMP && i < 64) t = i + 1 + R + (i - (64 - BUMP) + 1);
  if (i >= 64 && t < 64 + R + BUMP) t = 64 + R + BUMP;
  return t > 128 ? 128 : t;
}
constexpr int targetm(int i) { int t = target(0); for (int j = 1; j <= i; ++j) { const int u = target(j); if (u > t) t = u; } return t; }

struct Params {
  const void* x; const void* k; const void* v; const void* wq; const void* wo;
  const void* w1; const void* b1; const void* w2; const void* b2; void* out; void* ws;
};

__device__ __forceinline__ rsrc_t mk_rsrc(const void* p, u32 bytes) { return __builtin_amdgcn_make_buffer_rsrc((void*)p, 0, (int)bytes, 0x00020000); }
template <int AUX> __device__ __forceinline__ u32x4 bld128(rsrc_t r, u32 voff, u32 soff) { return __builtin_amdgcn_raw_buffer_load_b128(r, (int)voff, (int)soff, AUX); }
template <int AUX> __device__ __forceinline__ u32 bld32(rsrc_t r, u32 voff, u32 soff) { return __builtin_amdgcn_raw_buffer_load_b32(r, (int)voff, (int)soff, AUX); }
__device__ __forceinline__ void publish(rsrc_t r, u32 voff, u32 soff, u32 a, u32 b, u32 tA, u32 tB) { __builtin_amdgcn_raw_buffer_store_b128(u32x4{a, tA, b, tB}, r, (int)voff, (int)soff, AUX_SC1); }
__device__ __forceinline__ float bflo(u32 x) { return __builtin_bit_cast(float, x << 16); }
__device__ __forceinline__ float bfhi(u32 x) { return __builtin_bit_cast(float, x & 0xffff0000u); }
__device__ __forceinline__ u32 pkbf(float lo, float hi) { bf16x2 v = {(__bf16)lo, (__bf16)hi}; return __builtin_bit_cast(u32, v); }
__device__ __forceinline__ float rbf(float x) { return bflo(pkbf(x, 0.f) & 0xffffu); }
__device__ __forceinline__ float dot2(u32 a, u32 b, float c) { return __builtin_amdgcn_fdot2_f32_bf16(__builtin_bit_cast(bf16x2, a), __builtin_bit_cast(bf16x2, b), c, false); }
template <int CTRL> __device__ __forceinline__ float dpp(float x) { return __builtin_bit_cast(float, __builtin_amdgcn_mov_dpp(__builtin_bit_cast(int, x), CTRL, 0xf, 0xf, true)); }
constexpr int XOR1 = 0xB1, XOR2 = 0x4E, ROR4 = 0x124, ROR8 = 0x128, ROR2 = 0x122, ROR1 = 0x121, BCAST = 0x150;
__device__ __forceinline__ float u2f(u32 x) { return __builtin_bit_cast(float, x); }
__device__ __forceinline__ u32 f2u(float x) { return __builtin_bit_cast(u32, x); }
__device__ __forceinline__ float tr32(float a, float b) { auto s = __builtin_amdgcn_permlane32_swap(f2u(a), f2u(b), false, false); return u2f(s[0]) + u2f(s[1]); }
__device__ __forceinline__ float tr16(float a, float b) { auto s = __builtin_amdgcn_permlane16_swap(f2u(a), f2u(b), false, false); return u2f(s[0]) + u2f(s[1]); }
__device__ __forceinline__ float rows_sum(float x) { x = tr16(x, x); return tr32(x, x); }
__device__ __forceinline__ float rows_max(float x) {
  auto s = __builtin_amdgcn_permlane16_swap(f2u(x), f2u(x), false, false); x = fmaxf(u2f(s[0]), u2f(s[1]));
  auto t = __builtin_amdgcn_permlane32_swap(f2u(x), f2u(x), false, false); return fmaxf(u2f(t[0]), u2f(t[1]));
}
__device__ __forceinline__ float row16_sum(float x) { x += dpp<ROR8>(x); x += dpp<ROR4>(x); x += dpp<ROR2>(x); x += dpp<ROR1>(x); return x; }
__device__ __forceinline__ float row16_max(float x) { x = fmaxf(x, dpp<ROR8>(x)); x = fmaxf(x, dpp<ROR4>(x)); x = fmaxf(x, dpp<ROR2>(x)); x = fmaxf(x, dpp<ROR1>(x)); return x; }
__device__ __forceinline__ float wave_sum(float x) { return rows_sum(row16_sum(x)); }
__device__ __forceinline__ float wave_max(float x) { return rows_max(row16_max(x)); }
template <int K> __device__ __forceinline__ float merge(float lo, float hi, int c) {
  const bool bit = (c >> K) & 1;
  const float keep = bit ? hi : lo, send = bit ? lo : hi;
  constexpr int CTRL = K == 0 ? XOR1 : K == 1 ? XOR2 : K == 2 ? ROR4 : ROR8;
  return keep + dpp<CTRL>(send);
}
template <int T> __device__ __forceinline__ float bcast(float x) { return dpp<BCAST + T>(x); }
#define SB() __builtin_amdgcn_sched_barrier(0)
template <int I> struct IC { static constexpr int value = I; };
template <int B, int E, class F> __device__ __forceinline__ void sfor(F&& f) { if constexpr (B < E) { f(IC<B>{}); sfor<B + 1, E>(f); } }
#define INL __attribute__((always_inline))

__device__ __forceinline__ bool tags_ok(const u32x4& g, u32 tA, u32 tB) { return g.y == tA && g.w == tB; }
template <int N, int SLEEP, int DELAY>
__device__ __forceinline__ void gather(u32x4 (&g)[N], rsrc_t rs, u32 off, u32 stride, u32 tA, u32 tB, bool need_last = true) {
  if (DELAY) __builtin_amdgcn_s_sleep(DELAY);
#pragma unroll
  for (int k = 0; k < N; ++k) g[k] = bld128<AUX_SC1>(rs, (k < N - 1 || need_last) ? off : OOB, k * stride);
  if (!need_last) { g[N - 1].x = 0; g[N - 1].y = tA; g[N - 1].z = 0; g[N - 1].w = tB; }
  bool ok = true;
#pragma unroll
  for (int k = 0; k < N; ++k) ok = ok && tags_ok(g[k], tA, tB);
  if (!__all(ok)) {
#pragma unroll 1
    for (u32 spin = 0; spin < SPIN_CAP; ++spin) {
      asm volatile("" ::: "memory");
      u32x4 t[N];
#pragma unroll
      for (int k = 0; k < N; ++k) t[k] = bld128<AUX_SC1>(rs, tags_ok(g[k], tA, tB) ? OOB : off, k * stride);
      ok = true;
#pragma unroll
      for (int k = 0; k < N; ++k) { if (!tags_ok(g[k], tA, tB)) g[k] = t[k]; ok = ok && tags_ok(g[k], tA, tB); }
      if (__all(ok)) break;
      if (SLEEP) __builtin_amdgcn_s_sleep(SLEEP);
    }
  }
}
__device__ __forceinline__ void lds_wait(unsigned char* word, u32 want) {
#pragma unroll 1
  for (u32 spin = 0; spin < (SPIN_CAP << 4); ++spin) {
    if (*(volatile LAS u32*)(LAS unsigned char*)word >= want) break;
    __builtin_amdgcn_s_sleep(2);
  }
  asm volatile("" ::: "memory");
}
__device__ __forceinline__ void lds_signal(unsigned char* word, int lane) {
  asm volatile("s_waitcnt lgkmcnt(0)" ::: "memory");
  if (lane == 0) __hip_atomic_fetch_add((LAS u32*)(LAS unsigned char*)word, 1u, __ATOMIC_RELAXED, __HIP_MEMORY_SCOPE_WORKGROUP);
}

constexpr int L_XN = 0;
constexpr int L_QP = 2560;
constexpr int L_Q = L_QP + 8192;
constexpr int L_ML = L_Q + 256;
constexpr int L_CNT = L_ML + 64;
constexpr int L_TAG = L_CNT + 16;
constexpr int L_FW = L_CNT + 32;
constexpr int L_YP = L_FW + 64;
constexpr int L_H2 = L_YP + 256;
constexpr int L_FR = L_H2 + 512;
constexpr int L_OA = L_FR + 3840;
constexpr int L_WO = L_OA + 16 * 272;
constexpr int L_TRASH = L_WO + 8 * 5120;
constexpr int L_END = L_TRASH + 1024;
static_assert(L_OA % 16 == 0 && L_WO % 16 == 0 && L_FW % 16 == 0 && L_FR % 16 == 0 && L_QP % 16 == 0 && L_YP % 16 == 0, "LDS alignment");

#define STAMP(n) do {} while (0)
#define STAMP_FLUSH() do {} while (0)

__global__ void __launch_bounds__(NTH, 4) fused_block(Params p) {
  __shared__ __attribute__((aligned(16))) unsigned char lds[L_END];
  const int tid = threadIdx.x, lane = tid & 63, w = __builtin_amdgcn_readfirstlane(tid >> 6), g4 = lane >> 4, c = lane & 15;
  const int bid = blockIdx.x, xc = bid & 7, rr = bid >> 3;
  const int h = (rr >> 3) * 8 + xc, b = (rr >> 2) & 1, s = rr & 3;
  const int pair = b * NH + h, prod = pair * 4 + s;
  STAMP(0);

  if (w >= 8) {
    const int pw = w - 8;
    const int fu = xc + 8 * (rr >> 3), fv = rr & 7;
    const bool is_ffn = fu < 27;
    const bool is_cons = bid >= CONS0;
    const int cj = bid - CONS0, cb = is_cons ? cj / 80 : 0, coct = is_cons ? cj % 80 : 0;
    const rsrc_t rsWS = mk_rsrc(p.ws, WS_END);
    const rsrc_t rsX = mk_rsrc(p.x, 2 * DM * 2);
    const rsrc_t rsWQ = mk_rsrc(p.wq, DM * NH * DH * 2);
    if (pw == 0) *(u32x4*)(lds + (lane == 0 ? L_CNT : L_TRASH + lane * 16)) = u32x4{0u, 0u, 0u, 0u};
    __syncthreads();
    const u32x4 x0a = bld128<0>(rsX, lane * 20, 0), x1a = bld128<0>(rsX, 1280 + lane * 20, 0);
    const u32 x0b = bld32<0>(rsX, lane * 20 + 16, 0), x1b = bld32<0>(rsX, 1280 + lane * 20 + 16, 0);
    const int qrow = 80 * (4 * b + s) + 10 * pw + 3 * g4;
    u32x4 WQ[3];
#pragma unroll
    for (int k = 0; k < 3; ++k) WQ[k] = bld128<W_AUX>(rsWQ, 3 * g4 + k < 10 ? (qrow + k) * 8192 + c * 16 : OOB, h * 256);
    const u32 xo = (qrow * 2) & ~3;
    const u32 xr00 = bld32<0>(rsX, xo, 0), xr01 = bld32<0>(rsX, xo + 4 < 1280 ? xo + 4 : OOB, 0), xr10 = bld32<0>(rsX, 1280 + xo, 0), xr11 = bld32<0>(rsX, 1280 + xo + 4, 0);
    const u32x2 E = __builtin_bit_cast(u32x2, __builtin_amdgcn_raw_buffer_load_b64(rsWS, pw < 2 ? (int)WS_E : (int)OOB, 0, AUX_SC1));
    SB();
    auto ln_stats = [&](const u32x4& ra, u32 rb, float& mb, float& inv) INL {
      float v[10];
#pragma unroll
      for (int k = 0; k < 4; ++k) { v[2 * k] = bflo(ra[k]); v[2 * k + 1] = bfhi(ra[k]); }
      v[8] = bflo(rb); v[9] = bfhi(rb);
      float sum = 0.f;
#pragma unroll
      for (int k = 0; k < 10; ++k) sum += v[k];
      const float mean = wave_sum(sum) * (1.f / DM);
      float sq = 0.f;
#pragma unroll
      for (int k = 0; k < 10; ++k) { const float d = v[k] - mean; sq = fmaf(d, d, sq); }
      const float var = wave_sum(sq) * (1.f / DM);
      mb = rbf(mean);
      inv = 1.f / rbf(sqrtf(rbf(rbf(var) + 1e-5f)));
    };
    float mb0, inv0, mb1, inv1;
    ln_stats(x0a, x0b, mb0, inv0);
    ln_stats(x1a, x1b, mb1, inv1);
    STAMP(9);
    {
      float a[16];
#pragma unroll
      for (int k = 0; k < 16; ++k) a[k] = 0.f;
      const bool odd = qrow & 1;
#pragma unroll
      for (int k = 0; k < 3; ++k) {
        const float r0 = k == 0 ? (odd ? bfhi(xr00) : bflo(xr00)) : k == 1 ? (odd ? bflo(xr01) : bfhi(xr00)) : (odd ? bfhi(xr01) : bflo(xr01));
        const float r1 = k == 0 ? (odd ? bfhi(xr10) : bflo(xr10)) : k == 1 ? (odd ? bflo(xr11) : bfhi(xr10)) : (odd ? bfhi(xr11) : bflo(xr11));
        const float x0 = rbf(rbf(r0 - mb0) * inv0), x1 = rbf(rbf(r1 - mb1) * inv1);
#pragma unroll
        for (int u = 0; u < 4; ++u) {
          const float lo = bflo(WQ[k][u]), hi = bfhi(WQ[k][u]);
          a[2 * u] = fmaf(x0, lo, a[2 * u]); a[2 * u + 1] = fmaf(x0, hi, a[2 * u + 1]);
          a[8 + 2 * u] = fmaf(x1, lo, a[8 + 2 * u]); a[8 + 2 * u + 1] = fmaf(x1, hi, a[8 + 2 * u + 1]);
        }
      }
#pragma unroll
      for (int e = 0; e < 8; ++e) a[e] = tr32(a[e], a[8 + e]);
      const f32x4 z = {tr16(a[0], a[4]), tr16(a[1], a[5]), tr16(a[2], a[6]), tr16(a[3], a[7])};
      *(f32x4*)(lds + L_QP + (pw * 256 + (g4 >> 1) * 128 + 8 * c + 4 * (g4 & 1)) * 4) = z;
      lds_signal(lds + L_CNT + 4, lane);
    }
    const u32 tA = E.x + 0x9E3779B9u, tB = E.y + 0x85EBCA6Bu;
    if (pw == 0) {
      *(u32x2*)(lds + (lane == 0 ? L_TAG : L_TRASH + lane * 8)) = u32x2{tA, tB};
      lds_wait(lds + L_CNT + 4, 8u);
      STAMP(7);
      f32x2 t0 = {0.f, 0.f}, t1 = {0.f, 0.f};
#pragma unroll
      for (int k = 0; k < 8; ++k) { t0 += *(const f32x2*)(lds + L_QP + (k * 256 + 2 * lane) * 4); t1 += *(const f32x2*)(lds + L_QP + (k * 256 + 128 + 2 * lane) * 4); }
      const u32 offP = WS_Q + (((h * 8 + b * 4 + s) * 2) * 64 + lane) * 16;
      publish(rsWS, offP, 0, f2u(t0.x), f2u(t0.y), tA, tB);
      publish(rsWS, offP, 64 * 16, f2u(t1.x), f2u(t1.y), tA, tB);
      u32x4 g[8];
      gather<8, POLL_SLEEP, Q_DELAY>(g, rsWS, WS_Q + ((h * 8 * 2 + b) * 64 + lane) * 16, 2 * 64 * 16, tA, tB);
      float q0 = 0.f, q1 = 0.f;
#pragma unroll
      for (int k = 0; k < 8; ++k) { q0 += u2f(g[k].x); q1 += u2f(g[k].z); }
      *(u32*)(lds + L_Q + lane * 4) = pkbf(q0, q1);
      STAMP(1);
    }
    __syncthreads();

    if (pw == 1 && is_ffn) {
      const rsrc_t rsW1 = mk_rsrc(p.w1, DM * FF * 2), rsW2 = mk_rsrc(p.w2, FF * DM * 2), rsB1 = mk_rsrc(p.b1, FF * 2);
#pragma unroll
      for (int i = 0; i < 2; ++i) {
        const float mb = i ? mb1 : mb0, inv = i ? inv1 : inv0;
#pragma unroll
        for (int k = 0; k < 5; ++k) {
          const u32 r = k < 4 ? (i ? x1a[k & 3] : x0a[k & 3]) : (i ? x1b : x0b);
          const u32 d = pkbf(bflo(r) - mb, bfhi(r) - mb);
          *(u32*)(lds + L_XN + i * 1280 + lane * 20 + 4 * k) = pkbf(bflo(d) * inv, bfhi(d) * inv);
        }
      }
      {
        u32x4 W1[10];
        auto ldW1 = [&](auto K_) INL { constexpr int k = decltype(K_)::value; W1[k] = bld128<W_AUX>(rsW1, (lane >> 3) * (FF * 2) + (lane & 7) * 16, (80 * fv + 8 * k) * (FF * 2) + fu * 128); };
        sfor<0, FF_WIN>(ldW1);
        float hv[16];
#pragma unroll
        for (int k = 0; k < 16; ++k) hv[k] = 0.f;
        sfor<0, 10>([&](auto K_) INL {
          constexpr int k = decltype(K_)::value;
          const int row = 80 * fv + 8 * k + (lane >> 3);
          const float x0 = bflo(*(const u16*)(lds + L_XN + row * 2)), x1 = bflo(*(const u16*)(lds + L_XN + 1280 + row * 2));
          asm volatile("" :: "v"(W1[k][0]));
          SB();
          if constexpr (k + FF_WIN < 10) ldW1(IC<k + FF_WIN>{});
          SB();
#pragma unroll
          for (int u = 0; u < 4; ++u) {
            const float lo = bflo(W1[k][u]), hi = bfhi(W1[k][u]);
            hv[2 * u] = fmaf(x0, lo, hv[2 * u]); hv[2 * u + 1] = fmaf(x0, hi, hv[2 * u + 1]);
            hv[8 + 2 * u] = fmaf(x1, lo, hv[8 + 2 * u]); hv[8 + 2 * u + 1] = fmaf(x1, hi, hv[8 + 2 * u + 1]);
          }
          asm volatile("" : "+v"(hv[0]), "+v"(hv[1]), "+v"(hv[2]), "+v"(hv[3]), "+v"(hv[4]), "+v"(hv[5]), "+v"(hv[6]), "+v"(hv[7]));
          asm volatile("" : "+v"(hv[8]), "+v"(hv[9]), "+v"(hv[10]), "+v"(hv[11]), "+v"(hv[12]), "+v"(hv[13]), "+v"(hv[14]), "+v"(hv[15]));
          SB();
        });
#pragma unroll
        for (int e = 0; e < 8; ++e) hv[e] = merge<3>(hv[e], hv[8 + e], c);
#pragma unroll
        for (int e = 0; e < 4; ++e) hv[e] = tr16(hv[e], hv[e + 4]);
        const float h0 = tr32(hv[0], hv[2]), h1 = tr32(hv[1], hv[3]);
        const u32 gi = ((lane >> 3) & 1) * 32 + 4 * (lane & 7) + 2 * ((lane >> 4) & 1) + (lane >> 5);
        publish(rsWS, WS_H + ((fu * 8 + fv) * 64 + gi) * 16, 0, f2u(h0), f2u(h1), tA, tB);
      }
      SB();
      {
        const u32 B1v = bld32<W_AUX>(rsB1, (lane & 31) * 4, fu * 128);
        float s0 = 0.f, s1 = 0.f;
        if (H_DELAY) for (int i = 0; i < H_DELAY / 16; ++i) __builtin_amdgcn_s_sleep(16);
        sfor<0, 8 / FF_WIN>([&](auto J_) INL {
          constexpr int j = decltype(J_)::value;
          u32x4 g[FF_WIN];
          gather<FF_WIN, POLL_SLEEP, 0>(g, rsWS, WS_H + ((fu * 8 + j * FF_WIN) * 64 + lane) * 16, 64 * 16, tA, tB);
#pragma unroll
          for (int k = 0; k < FF_WIN; ++k) { s0 += u2f(g[k].x); s1 += u2f(g[k].z); }
          asm volatile("" : "+v"(s0), "+v"(s1));
          SB();
        });
        u32x4 W2[11];
        const int rg = lane / 10, ch = lane - 10 * rg;
        auto ldW2 = [&](auto K_) INL { constexpr int k = decltype(K_)::value; W2[k] = bld128<W_AUX>(rsW2, (lane < 60 && 6 * k + rg < 64) ? rg * (DM * 2) + ch * 16 : OOB, (64 * fu + 6 * k) * (DM * 2) + fv * 160); };
        sfor<0, FF_WIN>(ldW2);
        SB();
        s0 = rbf(rbf(s0) + bflo(B1v)); s1 = rbf(rbf(s1) + bfhi(B1v));
        s0 = s0 > 0.f ? s0 : 0.f; s1 = s1 > 0.f ? s1 : 0.f;
        *(f32x2*)(lds + L_H2 + lane * 8) = f32x2{rbf(s0 * s0), rbf(s1 * s1)};
        float ov[16];
#pragma unroll
        for (int k = 0; k < 16; ++k) ov[k] = 0.f;
        sfor<0, 11>([&](auto K_) INL {
          constexpr int k = decltype(K_)::value;
          const int r = (6 * k + rg) & 63;
          const float a0 = *(const float*)(lds + L_H2 + r * 4), a1 = *(const float*)(lds + L_H2 + 256 + r * 4);
          asm volatile("" :: "v"(W2[k][0]));
          SB();
          if constexpr (k + FF_WIN < 11) ldW2(IC<k + FF_WIN>{});
          SB();
#pragma unroll
          for (int u = 0; u < 4; ++u) {
            const float lo = bflo(W2[k][u]), hi = bfhi(W2[k][u]);
            ov[2 * u] = fmaf(a0, lo, ov[2 * u]); ov[2 * u + 1] = fmaf(a0, hi, ov[2 * u + 1]);
            ov[8 + 2 * u] = fmaf(a1, lo, ov[8 + 2 * u]); ov[8 + 2 * u + 1] = fmaf(a1, hi, ov[8 + 2 * u + 1]);
          }
          asm volatile("" : "+v"(ov[0]), "+v"(ov[1]), "+v"(ov[2]), "+v"(ov[3]), "+v"(ov[4]), "+v"(ov[5]), "+v"(ov[6]), "+v"(ov[7]));
          asm volatile("" : "+v"(ov[8]), "+v"(ov[9]), "+v"(ov[10]), "+v"(ov[11]), "+v"(ov[12]), "+v"(ov[13]), "+v"(ov[14]), "+v"(ov[15]));
          SB();
        });
        unsigned char* fr = lds + (lane < 60 ? L_FR + lane * 64 : L_TRASH + (lane & 3) * 64);
#pragma unroll
        for (int k = 0; k < 4; ++k) *(f32x4*)(fr + 16 * k) = f32x4{ov[4 * k], ov[4 * k + 1], ov[4 * k + 2], ov[4 * k + 3]};
#pragma unroll
        for (int j = 0; j < 2; ++j) {
          const int gq = lane + 64 * j, gb = gq >= 40 ? 1 : 0, cp = (gq - 40 * gb) % 40;
          f32x2 t = {0.f, 0.f};
#pragma unroll
          for (int k = 0; k < 6; ++k) t += *(const f32x2*)(lds + L_FR + (k * 10 + (cp >> 2)) * 64 + (gb * 8 + 2 * (cp & 3)) * 4);
          publish(rsWS, gq < 80 ? WS_F + ((fu * 8 + fv) * 80 + gq) * 16 : OOB, 0, f2u(t.x), f2u(t.y), tA, tB);
        }
      }
      SB();
    }

    u32x4 BF[4][5];
    {
      const rsrc_t rsWO = mk_rsrc((const char*)p.wo + (size_t)h * (DH * DM * 2), DH * DM * 2);
      u32 vWO[5];
#pragma unroll
      for (int k = 0; k < 5; ++k) { const int it = lane + 64 * k; vWO[k] = (it / 10) * (DM * 2) + (it % 10) * 16 + pw * 160; }
      unsigned char* st = lds + L_WO + pw * 5120;
      if (pw >= 2) for (int i = 0; i < (pw - 2) * WO_STAG; ++i) __builtin_amdgcn_s_sleep(16);
      sfor<0, 4>([&](auto RHO) INL {
        constexpr int rho = decltype(RHO)::value;
        u32x4 v[5];
        auto ldWO = [&](auto K_) INL {
          constexpr int k = decltype(K_)::value;
          v[k] = bld128<W_AUX>(rsWO, vWO[k], rho * 32 * DM * 2);
        };
        sfor<0, WO_WIN>(ldWO);
        sfor<0, 5>([&](auto K_) INL {
          constexpr int k = decltype(K_)::value;
          *(u32x4*)(st + (lane + 64 * k) * 16) = v[k];
          SB();
          if constexpr (k + WO_WIN < 5) ldWO(IC<k + WO_WIN>{});
          SB();
        });
#pragma unroll
        for (int n = 0; n < 5; ++n) {
          LAS unsigned char* a = (LAS unsigned char*)st + (8 * g4 + (c >> 2)) * 160 + 32 * n + 8 * (c & 3);
          const u32x2 lo = __builtin_bit_cast(u32x2, __builtin_amdgcn_ds_read_tr16_b64_v4i16((LAS s16x4*)a));
          const u32x2 hi = __builtin_bit_cast(u32x2, __builtin_amdgcn_ds_read_tr16_b64_v4i16((LAS s16x4*)(a + 4 * 160)));
          BF[rho][n] = u32x4{lo.x, lo.y, hi.x, hi.y};
          asm volatile("" : "+v"(BF[rho][n]));
        }
        if (rho < 3 && pw >= 2) for (int i = 0; i < WO_SLEEP; ++i) __builtin_amdgcn_s_sleep(16);
        SB();
      });
    }
    STAMP(2);

    float gate0 = 0.f, gate1 = 0.f;
    u32 XV = 0;
    if (pw == 0) {
      lds_wait(lds + L_CNT, 8u);
      const f32x2 ml = *(const f32x2*)(lds + L_ML + (lane & 7) * 8);
      const float mg = wave_max(ml.x);
      const float lg = row16_sum(c < 8 ? ml.y * __builtin_amdgcn_exp2f(ml.x - mg) : 0.f);
      publish(rsWS, lane == 0 ? WS_ML + prod * 16 : OOB, 0, f2u(mg), f2u(lg), tA, tB);
      STAMP(3);
      lds_wait(lds + L_CNT + 8, 1u);
      u32x4 g[1];
      gather<1, POLL_SLEEP, 0>(g, rsWS, WS_ML + (pair * 4 + (lane & 3)) * 16, 0, tA, tB);
      const float mv = u2f(g[0].x), M = row16_max(mv);
      const float L = row16_sum(c < 4 ? u2f(g[0].z) * __builtin_amdgcn_exp2f(mv - M) : 0.f);
      *(float*)(lds + (lane < 16 ? L_FW : L_TRASH) + lane * 4) = __builtin_amdgcn_exp2f(ml.x - M) / L;
      STAMP(4);
      if (bid == CONS0) {
        u32x4 e[4];
        gather<4, POLL_SLEEP, 0>(e, rsWS, WS_ML + lane * 16, 64 * 16, tA, tB);
        if (lane == 0) __hip_atomic_store((GAS u64*)p.ws, (u64)tA | ((u64)tB << 32), __ATOMIC_RELAXED, __HIP_MEMORY_SCOPE_AGENT);
      }
      if (is_cons) {
        const rsrc_t rsB2 = mk_rsrc(p.b2, DM * 2);
        const u32 B2v = bld32<0>(rsB2, (8 * coct + 2 * (lane & 3)) * 2, 0);
        XV = bld32<0>(rsX, cb * 1280 + (8 * coct + 2 * (lane & 3)) * 2, 0);
        const int cv = coct / 10, cp0 = (coct - 10 * cv) * 4;
        u32x4 f[2];
        gather<2, POLL_SLEEP, 0>(f, rsWS, WS_F + (((lane >> 2) * 8 + cv) * 80 + cb * 40 + cp0 + (lane & 3)) * 16, 16 * 8 * 80 * 16, tA, tB, lane < 44);
        float f0 = u2f(f[0].x) + u2f(f[1].x), f1 = u2f(f[0].z) + u2f(f[1].z);
        f0 += dpp<ROR4>(f0); f1 += dpp<ROR4>(f1); f0 += dpp<ROR8>(f0); f1 += dpp<ROR8>(f1);
        f0 = rows_sum(f0); f1 = rows_sum(f1);
        const float o0 = rbf(rbf(f0) + bflo(B2v)), o1 = rbf(rbf(f1) + bfhi(B2v));
        gate0 = rbf(o0 / (1.f + __expf(-o0))); gate1 = rbf(o1 / (1.f + __expf(-o1)));
      }
    }
    STAMP(5);
    __syncthreads();
    const u32x2 tg = *(const u32x2*)(lds + L_TAG);
    {
      f32x4 D[5];
#pragma unroll
      for (int n = 0; n < 5; ++n) D[n] = f32x4{0.f, 0.f, 0.f, 0.f};
      const f32x4 fr = *(const f32x4*)(lds + L_FW + 16 * g4);
#pragma unroll
      for (int rho = 0; rho < 4; ++rho) {
        const u32x4 A = *(const u32x4*)(lds + L_OA + c * 272 + 64 * rho + 16 * g4);
#pragma unroll
        for (int n = 0; n < 5; ++n) D[n] = __builtin_amdgcn_mfma_f32_16x16x32_bf16(__builtin_bit_cast(bf16x8, A), __builtin_bit_cast(bf16x8, BF[rho][n]), D[n], 0, 0, 0);
      }
      float e[5];
#pragma unroll
      for (int n = 0; n < 5; ++n) e[n] = fmaf(D[n][0], fr[0], D[n][1] * fr[1]) + fmaf(D[n][2], fr[2], D[n][3] * fr[3]);
      const float ya = tr16(tr32(e[0], e[2]), tr32(e[1], e[3]));
      const float yb = rows_sum(e[4]);
      const float yan = dpp<XOR1>(ya), ybn = dpp<XOR1>(yb);
      const bool odd = c & 1;
      const u32 cp = odd ? 40 * pw + 32 + (c >> 1) : 40 * pw + 8 * g4 + (c >> 1);
      publish(rsWS, (!odd || g4 == 0) ? WS_Y + (prod * 320 + cp) * 16 : OOB, 0, odd ? f2u(ybn) : f2u(ya), odd ? f2u(yb) : f2u(yan), tg.x, tg.y);
    }
    STAMP(6);
    if (is_cons) {
      u32x4 g[1];
      gather<1, Y_SLEEP, 0>(g, rsWS, WS_Y + ((cb * 128 + 16 * pw + (lane >> 2)) * 320 + 4 * coct + (lane & 3)) * 16, 0, tg.x, tg.y);
      float y0 = u2f(g[0].x), y1 = u2f(g[0].z);
      y0 += dpp<ROR4>(y0); y1 += dpp<ROR4>(y1); y0 += dpp<ROR8>(y0); y1 += dpp<ROR8>(y1);
      y0 = rows_sum(y0); y1 = rows_sum(y1);
      *(f32x2*)(lds + (lane < 4 ? L_YP + pw * 32 : L_TRASH) + lane * 8) = f32x2{y0, y1};
      __syncthreads();
      if (pw == 0) {
        f32x2 y = {0.f, 0.f};
#pragma unroll
        for (int k = 0; k < 8; ++k) y += *(const f32x2*)(lds + L_YP + k * 32 + (lane & 3) * 8);
        const float r0 = bflo(XV) + rbf(rbf(y.x) * gate0), r1 = bfhi(XV) + rbf(rbf(y.y) * gate1);
        if (lane < 4) ((GAS u32*)p.out)[(cb * DM + 8 * coct) / 2 + lane] = pkbf(r0, r1);
        STAMP(8);
      }
    }
    STAMP_FLUSH();
    return;
  }

  const rsrc_t rsK = mk_rsrc((const char*)p.k + (size_t)pair * (SK * DH * 2) + (size_t)s * (2048 * DH * 2), 2048 * DH * 2);
  const rsrc_t rsV = mk_rsrc((const char*)p.v + (size_t)pair * (SK * DH * 2) + (size_t)s * (2048 * DH * 2), 2048 * DH * 2);
  const u32 vKV = w * 4096 + lane * 16;
  u32x4 S[128];
  auto ldS = [&](int i) INL -> u32x4 { const int t = i & 63; return i < 64 ? bld128<K_AUX>(rsK, vKV + (t & 3) * 1024, (t >> 2) * 32768) : bld128<V_AUX>(rsV, vKV + (t & 3) * 1024, (t >> 2) * 32768); };
  sfor<0, R>([&](auto I) INL { S[decltype(I)::value] = ldS(decltype(I)::value); });
  SB();
  __syncthreads();
  sfor<R, N0>([&](auto I) INL {
    constexpr int n = decltype(I)::value;
    asm volatile("" :: "v"(S[n - R][0]));
    SB();
    S[n] = ldS(n);
    SB();
    if constexpr (n == 6) STAMP(9);
    if constexpr (n == 10) STAMP(10);
    if constexpr (n == 14) STAMP(11);
  });
  STAMP(6);
  __syncthreads();
  u32 q[4];
  { const u32x4 qq = *(const u32x4*)(lds + L_Q + c * 16); q[0] = qq[0]; q[1] = qq[1]; q[2] = qq[2]; q[3] = qq[3]; }
  STAMP(2);

  float sc[4], d0 = 0.f, m1a = 0.f, m2a = 0.f, m3a = 0.f, m1 = 0.f, m2 = 0.f;
  sfor<0, 64>([&](auto I) INL {
    constexpr int i = decltype(I)::value;
    float d = dot2(S[i][0], q[0], 0.f);
    asm volatile("" : "+v"(d));
    SB();
    sfor<(i == 0 ? N0 : targetm(i - 1)), targetm(i)>([&](auto N) INL { S[decltype(N)::value] = ldS(decltype(N)::value); });
    SB();
    d = dot2(S[i][1], q[1], d); d = dot2(S[i][2], q[2], d); d = dot2(S[i][3], q[3], d);
    asm volatile("" : "+v"(d));
    constexpr int t = i & 15;
    if ((t & 1) == 0) d0 = d;
    else {
      const float a1 = merge<0>(d0, d, c);
      if ((t & 2) == 0) m1a = a1;
      else {
        m1 = merge<1>(m1a, a1, c);
        if ((t & 4) == 0) m2a = m1;
        else {
          m2 = merge<2>(m2a, m1, c);
          if ((t & 8) == 0) m3a = m2;
          else sc[i >> 4] = merge<3>(m3a, m2, c);
        }
      }
    }
    SB();
  });
  STAMP(3);
  constexpr float C2 = 0.08838834764831845f * 1.4426950408889634f;
  float pr[4];
  {
#pragma unroll
    for (int j = 0; j < 4; ++j) sc[j] *= C2;
    const float mw = wave_max(fmaxf(fmaxf(sc[0], sc[1]), fmaxf(sc[2], sc[3])));
#pragma unroll
    for (int j = 0; j < 4; ++j) pr[j] = __builtin_amdgcn_exp2f(sc[j] - mw);
    const float lw = wave_sum((pr[0] + pr[1]) + (pr[2] + pr[3]));
    *(f32x2*)(lds + (lane == 0 ? L_ML + w * 8 : L_TRASH + lane * 8)) = f32x2{mw, lw};
    lds_signal(lds + L_CNT, lane);
  }
  SB();

  float acc[8];
#pragma unroll
  for (int k = 0; k < 8; ++k) acc[k] = 0.f;
  sfor<64, 128>([&](auto I) INL {
    constexpr int i = decltype(I)::value;
    constexpr int vi = i - 64, t = vi & 15;
    const u32x4 V = S[i];
    float v0 = bflo(V[0]);
    asm volatile("" : "+v"(v0));
    SB();
    sfor<targetm(i - 1), targetm(i)>([&](auto N) INL { S[decltype(N)::value] = ldS(decltype(N)::value); });
    if constexpr (vi == VSIG) { if (tid == 0) __hip_atomic_fetch_add((LAS u32*)(LAS unsigned char*)(lds + L_CNT + 8), 1u, __ATOMIC_RELAXED, __HIP_MEMORY_SCOPE_WORKGROUP); }
    SB();
    const float pb = bcast<t>(pr[vi >> 4]);
    acc[0] = fmaf(pb, v0, acc[0]); acc[1] = fmaf(pb, bfhi(V[0]), acc[1]);
#pragma unroll
    for (int u = 1; u < 4; ++u) { acc[2 * u] = fmaf(pb, bflo(V[u]), acc[2 * u]); acc[2 * u + 1] = fmaf(pb, bfhi(V[u]), acc[2 * u + 1]); }
    asm volatile("" : "+v"(acc[0]), "+v"(acc[1]), "+v"(acc[2]), "+v"(acc[3]), "+v"(acc[4]), "+v"(acc[5]), "+v"(acc[6]), "+v"(acc[7]));
    SB();
  });
  STAMP(4);
  {
    const float z0 = tr16(tr32(acc[0], acc[4]), tr32(acc[2], acc[6])), z1 = tr16(tr32(acc[1], acc[5]), tr32(acc[3], acc[7]));
    const u32 hi = pkbf(z0, z1), lo = pkbf(z0 - bflo(hi), z1 - bfhi(hi));
    *(u32*)(lds + L_OA + w * 272 + 16 * c + 4 * g4) = hi;
    *(u32*)(lds + L_OA + (8 + w) * 272 + 16 * c + 4 * g4) = lo;
  }
  __syncthreads();
  STAMP(5);
  STAMP_FLUSH();
}

}

extern "C" void kernel_launch(void* const* d_in, const int* in_sizes, int n_in, void* d_out, int out_size, void* d_ws, size_t ws_size, hipStream_t stream) {
  (void)in_sizes; (void)n_in; (void)out_size; (void)ws_size;
  Params p;
  p.x = d_in[0]; p.k = d_in[1]; p.v = d_in[2]; p.wq = d_in[3]; p.wo = d_in[4];
  p.w1 = d_in[6]; p.b1 = d_in[7]; p.w2 = d_in[8]; p.b2 = d_in[9]; p.out = d_out; p.ws = d_ws;
  hipLaunchKernelGGL(fused_block, dim3(NWG), dim3(NTH), 0, stream, p);
}
```

```cpp
#include <hip/hip_runtime.h>
#include <hip/hip_bf16.h>
#include <stdint.h>

namespace {

typedef unsigned u32;
typedef unsigned long long u64;
typedef unsigned short u16;
typedef u32 u32x4 __attribute__((ext_vector_type(4)));
typedef u32 u32x2 __attribute__((ext_vector_type(2)));
typedef float f32x4 __attribute__((ext_vector_type(4)));
typedef float f32x2 __attribute__((ext_vector_type(2)));
typedef short s16x4 __attribute__((ext_vector_type(4)));
typedef __bf16 bf16x2 __attribute__((ext_vector_type(2)));
typedef __bf16 bf16x8 __attribute__((ext_vector_type(8)));
typedef __amdgpu_buffer_rsrc_t rsrc_t;
#define GAS __attribute__((address_space(1)))
#define LAS __attribute__((address_space(3)))

constexpr int DM = 640, NH = 32, SK = 8192, DH = 128, FF = 1728;
constexpr int NWG = 256, NTH = 1024;
constexpr int NFFN = 216;
constexpr int CONS0 = 96;
constexpr u32 WS_E = 0;
constexpr u32 WS_ML = 4096;
constexpr u32 WS_Q = 8192;
constexpr u32 WS_H = WS_Q + NH * 8 * 2 * 64 * 16;
constexpr u32 WS_F = WS_H + NFFN * 64 * 16;
constexpr u32 WS_Y = WS_F + NFFN * 80 * 16;
constexpr u32 WS_END = WS_Y + NWG * 320 * 16;
constexpr u32 OOB = 0x7ff00000u;
constexpr int AUX_NT = 2, AUX_SC1 = 16;
constexpr u32 SPIN_CAP = 1u << 16;

constexpr int KV_AUX = AUX_NT;
constexpr int R = 3;
constexpr int N0 = 21;
constexpr int BUMP = 2;
constexpr int Q_DELAY = 32;
constexpr int H_DELAY = 12;
constexpr int POLL_SLEEP = 16, Y_SLEEP = 1;
constexpr int WIN = 2;
constexpr int WO_SLEEP = 8, WO_STAG = 1;
constexpr int VSIG = 46;
constexpr int YTH = 4;
constexpr int target1(int i) {
  int t = i + 1 + R;
  const int top = N0 + (i + 1 < R ? i + 1 : R);
  if (t < top) t = top;
  if (i >= 64 - BUMP && i < 64) t = i + 1 + R + (i - (64 - BUMP) + 1);
  if (i >= 64 && t < 64 + R + BUMP) t = 64 + R + BUMP;
  return t > 128 ? 128 : t;
}
constexpr int target(int i) {
  int t = target1(0);
  for (int j = 1; j <= i; ++j) if (target1(j) > t) t = target1(j);
  return t;
}

struct Params {
  const void* x; const void* k; const void* v; const void* wq; const void* wo;
  const void* w1; const void* b1; const void* w2; const void* b2; void* out; void* ws;
};

__device__ __forceinline__ rsrc_t mk_rsrc(const void* p, u32 bytes) { return __builtin_amdgcn_make_buffer_rsrc((void*)p, 0, (int)bytes, 0x00020000); }
template <int AUX> __device__ __forceinline__ u32x4 bld128(rsrc_t r, u32 voff, u32 soff) { return __builtin_amdgcn_raw_buffer_load_b128(r, (int)voff, (int)soff, AUX); }
template <int AUX> __device__ __forceinline__ u32 bld32(rsrc_t r, u32 voff, u32 soff) { return __builtin_amdgcn_raw_buffer_load_b32(r, (int)voff, (int)soff, AUX); }
__device__ __forceinline__ void publish(rsrc_t r, u32 voff, u32 soff, u32 a, u32 b, u32 tA, u32 tB) { __builtin_amdgcn_raw_buffer_store_b128(u32x4{a, tA, b, tB}, r, (int)voff, (int)soff, AUX_SC1); }
__device__ __forceinline__ float bflo(u32 x) { return __builtin_bit_cast(float, x << 16); }
__device__ __forceinline__ float bfhi(u32 x) { return __builtin_bit_cast(float, x & 0xffff0000u); }
__device__ __forceinline__ u32 pkbf(float lo, float hi) { bf16x2 v = {(__bf16)lo, (__bf16)hi}; return __builtin_bit_cast(u32, v); }
__device__ __forceinline__ float rbf(float x) { return bflo(pkbf(x, 0.f) & 0xffffu); }
__device__ __forceinline__ float dot2(u32 a, u32 b, float c) { return __builtin_amdgcn_fdot2_f32_bf16(__builtin_bit_cast(bf16x2, a), __builtin_bit_cast(bf16x2, b), c, false); }
template <int CTRL> __device__ __forceinline__ float dpp(float x) { return __builtin_bit_cast(float, __builtin_amdgcn_mov_dpp(__builtin_bit_cast(int, x), CTRL, 0xf, 0xf, true)); }
constexpr int XOR1 = 0xB1, XOR2 = 0x4E, ROR4 = 0x124, ROR8 = 0x128, ROR2 = 0x122, ROR1 = 0x121, BCAST = 0x150;
__device__ __forceinline__ float u2f(u32 x) { return __builtin_bit_cast(float, x); }
__device__ __forceinline__ u32 f2u(float x) { return __builtin_bit_cast(u32, x); }
__device__ __forceinline__ float tr32(float a, float b) { auto s = __builtin_amdgcn_permlane32_swap(f2u(a), f2u(b), false, false); return u2f(s[0]) + u2f(s[1]); }
__device__ __forceinline__ float tr16(float a, float b) { auto s = __builtin_amdgcn_permlane16_swap(f2u(a), f2u(b), false, false); return u2f(s[0]) + u2f(s[1]); }
__device__ __forceinline__ float rows_sum(float x) { x = tr16(x, x); return tr32(x, x); }
__device__ __forceinline__ float rows_max(float x) {
  auto s = __builtin_amdgcn_permlane16_swap(f2u(x), f2u(x), false, false); x = fmaxf(u2f(s[0]), u2f(s[1]));
  auto t = __builtin_amdgcn_permlane32_swap(f2u(x), f2u(x), false, false); return fmaxf(u2f(t[0]), u2f(t[1]));
}
__device__ __forceinline__ float row16_sum(float x) { x += dpp<ROR8>(x); x += dpp<ROR4>(x); x += dpp<ROR2>(x); x += dpp<ROR1>(x); return x; }
__device__ __forceinline__ float row16_max(float x) { x = fmaxf(x, dpp<ROR8>(x)); x = fmaxf(x, dpp<ROR4>(x)); x = fmaxf(x, dpp<ROR2>(x)); x = fmaxf(x, dpp<ROR1>(x)); return x; }
__device__ __forceinline__ float wave_sum(float x) { return rows_sum(row16_sum(x)); }
__device__ __forceinline__ float wave_max(float x) { return rows_max(row16_max(x)); }
template <int K> __device__ __forceinline__ float merge(float lo, float hi, int c) {
  const bool bit = (c >> K) & 1;
  const float keep = bit ? hi : lo, send = bit ? lo : hi;
  constexpr int CTRL = K == 0 ? XOR1 : K == 1 ? XOR2 : K == 2 ? ROR4 : ROR8;
  return keep + dpp<CTRL>(send);
}
template <int T> __device__ __forceinline__ float bcast(float x) { return dpp<BCAST + T>(x); }
#define SB() __builtin_amdgcn_sched_barrier(0)
template <int I> struct IC { static constexpr int value = I; };
template <int B, int E, class F> __device__ __forceinline__ void sfor(F&& f) { if constexpr (B < E) { f(IC<B>{}); sfor<B + 1, E>(f); } }
#define INL __attribute__((always_inline))

__device__ __forceinline__ bool tags_ok(const u32x4& g, u32 tA, u32 tB) { return g.y == tA && g.w == tB; }
template <int N, int SLEEP, int DELAY>
__device__ __forceinline__ void gather(u32x4 (&g)[N], rsrc_t rs, u32 off, u32 stride, u32 tA, u32 tB, bool need_last = true) {
  if (DELAY) __builtin_amdgcn_s_sleep(DELAY);
#pragma unroll
  for (int k = 0; k < N; ++k) g[k] = bld128<AUX_SC1>(rs, (k < N - 1 || need_last) ? off : OOB, k * stride);
  if (!need_last) { g[N - 1].x = 0; g[N - 1].y = tA; g[N - 1].z = 0; g[N - 1].w = tB; }
  bool ok = true;
#pragma unroll
  for (int k = 0; k < N; ++k) ok = ok && tags_ok(g[k], tA, tB);
  if (!__all(ok)) {
#pragma unroll 1
    for (u32 spin = 0; spin < SPIN_CAP; ++spin) {
      asm volatile("" ::: "memory");
      u32x4 t[N];
#pragma unroll
      for (int k = 0; k < N; ++k) t[k] = bld128<AUX_SC1>(rs, tags_ok(g[k], tA, tB) ? OOB : off, k * stride);
      ok = true;
#pragma unroll
      for (int k = 0; k < N; ++k) { if (!tags_ok(g[k], tA, tB)) g[k] = t[k]; ok = ok && tags_ok(g[k], tA, tB); }
      if (__all(ok)) break;
      if (SLEEP) __builtin_amdgcn_s_sleep(SLEEP);
    }
  }
}
__device__ __forceinline__ void lds_wait(unsigned char* word, u32 want) {
#pragma unroll 1
  for (u32 spin = 0; spin < (SPIN_CAP << 4); ++spin) {
    if (*(volatile LAS u32*)(LAS unsigned char*)word >= want) break;
    __builtin_amdgcn_s_sleep(2);
  }
  asm volatile("" ::: "memory");
}
__device__ __forceinline__ void lds_signal(unsigned char* word, int lane) {
  asm volatile("s_waitcnt lgkmcnt(0)" ::: "memory");
  if (lane == 0) __hip_atomic_fetch_add((LAS u32*)(LAS unsigned char*)word, 1u, __ATOMIC_RELAXED, __HIP_MEMORY_SCOPE_WORKGROUP);
}

constexpr int L_XN = 0;
constexpr int L_QP = 2560;
constexpr int L_Q = L_QP + 8192;
constexpr int L_ML = L_Q + 256;
constexpr int L_CNT = L_ML + 64;
constexpr int L_TAG = L_CNT + 16;
constexpr int L_FW = L_CNT + 32;
constexpr int L_YP = L_FW + 64;
constexpr int L_H2 = L_YP + 256;
constexpr int L_FR = L_H2 + 512;
constexpr int L_OA = L_FR + 3840;
constexpr int L_WO = L_OA + 16 * 272;
constexpr int L_TRASH = L_WO + 8 * 5120;
constexpr int L_END = L_TRASH + 1024;
typedef char lds_regions_are_16_byte_aligned[(L_OA % 16 == 0 && L_WO % 16 == 0 && L_FW % 16 == 0 && L_FR % 16 == 0 && L_QP % 16 == 0 && L_YP % 16 == 0) ? 1 : -1];

__global__ void __launch_bounds__(NTH, 4) fused_block(Params p) {
  __shared__ __attribute__((aligned(16))) unsigned char lds[L_END];
  const int tid = threadIdx.x, lane = tid & 63, w = __builtin_amdgcn_readfirstlane(tid >> 6), g4 = lane >> 4, c = lane & 15;
  const int bid = blockIdx.x, xc = bid & 7, rr = bid >> 3;
  const int h = (rr >> 3) * 8 + xc, b = (rr >> 2) & 1, s = rr & 3;
  const int pair = b * NH + h, prod = pair * 4 + s;

  if (w >= 8) {
    const int pw = w - 8;
    const int fu = xc + 8 * (rr >> 3), fv = rr & 7;
    const bool is_ffn = fu < 27;
    const bool is_cons = bid >= CONS0;
    const int cj = bid - CONS0, cb = is_cons ? cj / 80 : 0, coct = is_cons ? cj % 80 : 0;
    const rsrc_t rsWS = mk_rsrc(p.ws, WS_END);
    const rsrc_t rsX = mk_rsrc(p.x, 2 * DM * 2);
    const rsrc_t rsWQ = mk_rsrc(p.wq, DM * NH * DH * 2);
    if (pw == 0) *(u32x4*)(lds + (lane == 0 ? L_CNT : L_TRASH + lane * 16)) = u32x4{0u, 0u, 0u, 0u};
    __syncthreads();
    const u32x4 x0a = bld128<0>(rsX, lane * 20, 0), x1a = bld128<0>(rsX, 1280 + lane * 20, 0);
    const u32 x0b = bld32<0>(rsX, lane * 20 + 16, 0), x1b = bld32<0>(rsX, 1280 + lane * 20 + 16, 0);
    const int qrow = 80 * (4 * b + s) + 10 * pw + 3 * g4;
    u32x4 WQ[3];
#pragma unroll
    for (int k = 0; k < 3; ++k) WQ[k] = bld128<0>(rsWQ, 3 * g4 + k < 10 ? (qrow + k) * 8192 + c * 16 : OOB, h * 256);
    const u32 xo = (qrow * 2) & ~3;
    const u32 xr00 = bld32<0>(rsX, xo, 0), xr01 = bld32<0>(rsX, xo + 4 < 1280 ? xo + 4 : OOB, 0), xr10 = bld32<0>(rsX, 1280 + xo, 0), xr11 = bld32<0>(rsX, 1280 + xo + 4, 0);
    const u32x2 E = __builtin_bit_cast(u32x2, __builtin_amdgcn_raw_buffer_load_b64(rsWS, pw == 0 ? (int)WS_E : (int)OOB, 0, AUX_SC1));
    SB();
    auto ln_stats = [&](const u32x4& ra, u32 rb, float& mb, float& inv) INL {
      float v[10];
#pragma unroll
      for (int k = 0; k < 4; ++k) { v[2 * k] = bflo(ra[k]); v[2 * k + 1] = bfhi(ra[k]); }
      v[8] = bflo(rb); v[9] = bfhi(rb);
      float sum = 0.f;
#pragma unroll
      for (int k = 0; k < 10; ++k) sum += v[k];
      const float mean = wave_sum(sum) * (1.f / DM);
      float sq = 0.f;
#pragma unroll
      for (int k = 0; k < 10; ++k) { const float d = v[k] - mean; sq = fmaf(d, d, sq); }
      const float var = wave_sum(sq) * (1.f / DM);
      mb = rbf(mean);
      inv = 1.f / rbf(sqrtf(rbf(rbf(var) + 1e-5f)));
    };
    float mb0, inv0, mb1, inv1;
    ln_stats(x0a, x0b, mb0, inv0);
    ln_stats(x1a, x1b, mb1, inv1);
    {
      float a[16];
#pragma unroll
      for (int k = 0; k < 16; ++k) a[k] = 0.f;
      const bool odd = qrow & 1;
#pragma unroll
      for (int k = 0; k < 3; ++k) {
        const float r0 = k == 0 ? (odd ? bfhi(xr00) : bflo(xr00)) : k == 1 ? (odd ? bflo(xr01) : bfhi(xr00)) : (odd ? bfhi(xr01) : bflo(xr01));
        const float r1 = k == 0 ? (odd ? bfhi(xr10) : bflo(xr10)) : k == 1 ? (odd ? bflo(xr11) : bfhi(xr10)) : (odd ? bfhi(xr11) : bflo(xr11));
        const float x0 = rbf(rbf(r0 - mb0) * inv0), x1 = rbf(rbf(r1 - mb1) * inv1);
#pragma unroll
        for (int u = 0; u < 4; ++u) {
          const float lo = bflo(WQ[k][u]), hi = bfhi(WQ[k][u]);
          a[2 * u] = fmaf(x0, lo, a[2 * u]); a[2 * u + 1] = fmaf(x0, hi, a[2 * u + 1]);
          a[8 + 2 * u] = fmaf(x1, lo, a[8 + 2 * u]); a[8 + 2 * u + 1] = fmaf(x1, hi, a[8 + 2 * u + 1]);
        }
      }
#pragma unroll
      for (int e = 0; e < 8; ++e) a[e] = tr32(a[e], a[8 + e]);
      const f32x4 z = {tr16(a[0], a[4]), tr16(a[1], a[5]), tr16(a[2], a[6]), tr16(a[3], a[7])};
      *(f32x4*)(lds + L_QP + (pw * 256 + (g4 >> 1) * 128 + 8 * c + 4 * (g4 & 1)) * 4) = z;
      lds_signal(lds + L_CNT + 4, lane);
    }
    if (pw == 0) {
      const u32 tA = E.x + 0x9E3779B9u, tB = E.y + 0x85EBCA6Bu;
      *(u32x2*)(lds + (lane == 0 ? L_TAG : L_TRASH + lane * 8)) = u32x2{tA, tB};
      lds_wait(lds + L_CNT + 4, 8u);
      f32x2 t0 = {0.f, 0.f}, t1 = {0.f, 0.f};
#pragma unroll
      for (int k = 0; k < 8; ++k) { t0 += *(const f32x2*)(lds + L_QP + (k * 256 + 2 * lane) * 4); t1 += *(const f32x2*)(lds + L_QP + (k * 256 + 128 + 2 * lane) * 4); }
      const u32 offP = WS_Q + (((h * 8 + b * 4 + s) * 2) * 64 + lane) * 16;
      publish(rsWS, offP, 0, f2u(t0.x), f2u(t0.y), tA, tB);
      publish(rsWS, offP, 64 * 16, f2u(t1.x), f2u(t1.y), tA, tB);
      u32x4 g[8];
      gather<8, POLL_SLEEP, Q_DELAY>(g, rsWS, WS_Q + ((h * 8 * 2 + b) * 64 + lane) * 16, 2 * 64 * 16, tA, tB);
      float q0 = 0.f, q1 = 0.f;
#pragma unroll
      for (int k = 0; k < 8; ++k) { q0 += u2f(g[k].x); q1 += u2f(g[k].z); }
      *(u32*)(lds + L_Q + lane * 4) = pkbf(q0, q1);
    }
    __syncthreads();
    const u32x2 tg = *(const u32x2*)(lds + L_TAG);
    const u32 tA = tg.x, tB = tg.y;

    if (pw == 1 && is_ffn) {
      const rsrc_t rsW1 = mk_rsrc(p.w1, DM * FF * 2), rsW2 = mk_rsrc(p.w2, FF * DM * 2), rsB1 = mk_rsrc(p.b1, FF * 2);
#pragma unroll
      for (int i = 0; i < 2; ++i) {
        const float mb = i ? mb1 : mb0, inv = i ? inv1 : inv0;
#pragma unroll
        for (int k = 0; k < 5; ++k) {
          const u32 r = k < 4 ? (i ? x1a[k & 3] : x0a[k & 3]) : (i ? x1b : x0b);
          const u32 d = pkbf(bflo(r) - mb, bfhi(r) - mb);
          *(u32*)(lds + L_XN + i * 1280 + lane * 20 + 4 * k) = pkbf(bflo(d) * inv, bfhi(d) * inv);
        }
      }
      {
        u32x4 W1[10];
        auto ldW1 = [&](auto K_) INL { constexpr int k = decltype(K_)::value; W1[k] = bld128<0>(rsW1, (lane >> 3) * (FF * 2) + (lane & 7) * 16, (80 * fv + 8 * k) * (FF * 2) + fu * 128); };
        sfor<0, WIN>(ldW1);
        float hv[16];
#pragma unroll
        for (int k = 0; k < 16; ++k) hv[k] = 0.f;
        sfor<0, 10>([&](auto K_) INL {
          constexpr int k = decltype(K_)::value;
          const int row = 80 * fv + 8 * k + (lane >> 3);
          const float x0 = bflo(*(const u16*)(lds + L_XN + row * 2)), x1 = bflo(*(const u16*)(lds + L_XN + 1280 + row * 2));
          asm volatile("" :: "v"(W1[k][0]));
          SB();
          if constexpr (k + WIN < 10) ldW1(IC<k + WIN>{});
          SB();
#pragma unroll
          for (int u = 0; u < 4; ++u) {
            const float lo = bflo(W1[k][u]), hi = bfhi(W1[k][u]);
            hv[2 * u] = fmaf(x0, lo, hv[2 * u]); hv[2 * u + 1] = fmaf(x0, hi, hv[2 * u + 1]);
            hv[8 + 2 * u] = fmaf(x1, lo, hv[8 + 2 * u]); hv[8 + 2 * u + 1] = fmaf(x1, hi, hv[8 + 2 * u + 1]);
          }
          asm volatile("" : "+v"(hv[0]), "+v"(hv[1]), "+v"(hv[2]), "+v"(hv[3]), "+v"(hv[4]), "+v"(hv[5]), "+v"(hv[6]), "+v"(hv[7]));
          asm volatile("" : "+v"(hv[8]), "+v"(hv[9]), "+v"(hv[10]), "+v"(hv[11]), "+v"(hv[12]), "+v"(hv[13]), "+v"(hv[14]), "+v"(hv[15]));
          SB();
        });
#pragma unroll
        for (int e = 0; e < 8; ++e) hv[e] = merge<3>(hv[e], hv[8 + e], c);
#pragma unroll
        for (int e = 0; e < 4; ++e) hv[e] = tr16(hv[e], hv[e + 4]);
        const float h0 = tr32(hv[0], hv[2]), h1 = tr32(hv[1], hv[3]);
        const u32 gi = ((lane >> 3) & 1) * 32 + 4 * (lane & 7) + 2 * ((lane >> 4) & 1) + (lane >> 5);
        publish(rsWS, WS_H + ((fu * 8 + fv) * 64 + gi) * 16, 0, f2u(h0), f2u(h1), tA, tB);
      }
      SB();
      {
        const u32 B1v = bld32<0>(rsB1, (lane & 31) * 4, fu * 128);
        float s0 = 0.f, s1 = 0.f;
        for (int i = 0; i < H_DELAY; ++i) __builtin_amdgcn_s_sleep(16);
        sfor<0, 8 / WIN>([&](auto J_) INL {
          constexpr int j = decltype(J_)::value;
          u32x4 g[WIN];
          gather<WIN, POLL_SLEEP, 0>(g, rsWS, WS_H + ((fu * 8 + j * WIN) * 64 + lane) * 16, 64 * 16, tA, tB);
#pragma unroll
          for (int k = 0; k < WIN; ++k) { s0 += u2f(g[k].x); s1 += u2f(g[k].z); }
          asm volatile("" : "+v"(s0), "+v"(s1));
          SB();
        });
        u32x4 W2[11];
        const int rg = lane / 10, ch = lane - 10 * rg;
        auto ldW2 = [&](auto K_) INL { constexpr int k = decltype(K_)::value; W2[k] = bld128<0>(rsW2, (lane < 60 && 6 * k + rg < 64) ? rg * (DM * 2) + ch * 16 : OOB, (64 * fu + 6 * k) * (DM * 2) + fv * 160); };
        sfor<0, WIN>(ldW2);
        SB();
        s0 = rbf(rbf(s0) + bflo(B1v)); s1 = rbf(rbf(s1) + bfhi(B1v));
        s0 = s0 > 0.f ? s0 : 0.f; s1 = s1 > 0.f ? s1 : 0.f;
        *(f32x2*)(lds + L_H2 + lane * 8) = f32x2{rbf(s0 * s0), rbf(s1 * s1)};
        float ov[16];
#pragma unroll
        for (int k = 0; k < 16; ++k) ov[k] = 0.f;
        sfor<0, 11>([&](auto K_) INL {
          constexpr int k = decltype(K_)::value;
          const int r = (6 * k + rg) & 63;
          const float a0 = *(const float*)(lds + L_H2 + r * 4), a1 = *(const float*)(lds + L_H2 + 256 + r * 4);
          asm volatile("" :: "v"(W2[k][0]));
          SB();
          if constexpr (k + WIN < 11) ldW2(IC<k + WIN>{});
          SB();
#pragma unroll
          for (int u = 0; u < 4; ++u) {
            const float lo = bflo(W2[k][u]), hi = bfhi(W2[k][u]);
            ov[2 * u] = fmaf(a0, lo, ov[2 * u]); ov[2 * u + 1] = fmaf(a0, hi, ov[2 * u + 1]);
            ov[8 + 2 * u] = fmaf(a1, lo, ov[8 + 2 * u]); ov[8 + 2 * u + 1] = fmaf(a1, hi, ov[8 + 2 * u + 1]);
          }
          asm volatile("" : "+v"(ov[0]), "+v"(ov[1]), "+v"(ov[2]), "+v"(ov[3]), "+v"(ov[4]), "+v"(ov[5]), "+v"(ov[6]), "+v"(ov[7]));
          asm volatile("" : "+v"(ov[8]), "+v"(ov[9]), "+v"(ov[10]), "+v"(ov[11]), "+v"(ov[12]), "+v"(ov[13]), "+v"(ov[14]), "+v"(ov[15]));
          SB();
        });
        unsigned char* fr = lds + (lane < 60 ? L_FR + lane * 64 : L_TRASH + (lane & 3) * 64);
#pragma unroll
        for (int k = 0; k < 4; ++k) *(f32x4*)(fr + 16 * k) = f32x4{ov[4 * k], ov[4 * k + 1], ov[4 * k + 2], ov[4 * k + 3]};
#pragma unroll
        for (int j = 0; j < 2; ++j) {
          const int gq = lane + 64 * j, gb = gq >= 40 ? 1 : 0, cp = (gq - 40 * gb) % 40;
          f32x2 t = {0.f, 0.f};
#pragma unroll
          for (int k = 0; k < 6; ++k) t += *(const f32x2*)(lds + L_FR + (k * 10 + (cp >> 2)) * 64 + (gb * 8 + 2 * (cp & 3)) * 4);
          publish(rsWS, gq < 80 ? WS_F + ((fu * 8 + fv) * 80 + gq) * 16 : OOB, 0, f2u(t.x), f2u(t.y), tA, tB);
        }
      }
      SB();
    }

    u32x4 BF[4][5];
    {
      const rsrc_t rsWO = mk_rsrc((const char*)p.wo + (size_t)h * (DH * DM * 2), DH * DM * 2);
      u32 vWO[5];
#pragma unroll
      for (int k = 0; k < 5; ++k) { const int it = lane + 64 * k; vWO[k] = (it / 10) * (DM * 2) + (it % 10) * 16 + pw * 160; }
      unsigned char* st = lds + L_WO + pw * 5120;
      if (pw >= 2) for (int i = 0; i < (pw - 2) * WO_STAG; ++i) __builtin_amdgcn_s_sleep(16);
      sfor<0, 4>([&](auto RHO) INL {
        constexpr int rho = decltype(RHO)::value;
        u32x4 v[5];
        auto ldWO = [&](auto K_) INL {
          constexpr int k = decltype(K_)::value;
          v[k] = bld128<0>(rsWO, vWO[k], rho * 32 * DM * 2);
        };
        sfor<0, WIN>(ldWO);
        sfor<0, 5>([&](auto K_) INL {
          constexpr int k = decltype(K_)::value;
          *(u32x4*)(st + (lane + 64 * k) * 16) = v[k];
          SB();
          if constexpr (k + WIN < 5) ldWO(IC<k + WIN>{});
          SB();
        });
#pragma unroll
        for (int n = 0; n < 5; ++n) {
          LAS unsigned char* a = (LAS unsigned char*)st + (8 * g4 + (c >> 2)) * 160 + 32 * n + 8 * (c & 3);
          const u32x2 lo = __builtin_bit_cast(u32x2, __builtin_amdgcn_ds_read_tr16_b64_v4i16((LAS s16x4*)a));
          const u32x2 hi = __builtin_bit_cast(u32x2, __builtin_amdgcn_ds_read_tr16_b64_v4i16((LAS s16x4*)(a + 4 * 160)));
          BF[rho][n] = u32x4{lo.x, lo.y, hi.x, hi.y};
          asm volatile("" : "+v"(BF[rho][n]));
        }
        if (rho < 3 && pw >= 2) for (int i = 0; i < WO_SLEEP; ++i) __builtin_amdgcn_s_sleep(16);
        SB();
      });
    }

    float gate0 = 0.f, gate1 = 0.f;
    u32 XV = 0;
    if (pw == 0) {
      lds_wait(lds + L_CNT, 8u);
      const f32x2 ml = *(const f32x2*)(lds + L_ML + (lane & 7) * 8);
      const float mg = wave_max(ml.x);
      const float lg = row16_sum(c < 8 ? ml.y * __builtin_amdgcn_exp2f(ml.x - mg) : 0.f);
      publish(rsWS, lane == 0 ? WS_ML + prod * 16 : OOB, 0, f2u(mg), f2u(lg), tA, tB);
      lds_wait(lds + L_CNT + 8, 1u);
      u32x4 g[1];
      gather<1, POLL_SLEEP, 0>(g, rsWS, WS_ML + (pair * 4 + (lane & 3)) * 16, 0, tA, tB);
      const float mv = u2f(g[0].x), M = row16_max(mv);
      const float L = row16_sum(c < 4 ? u2f(g[0].z) * __builtin_amdgcn_exp2f(mv - M) : 0.f);
      *(float*)(lds + (lane < 16 ? L_FW : L_TRASH) + lane * 4) = __builtin_amdgcn_exp2f(ml.x - M) / L;
      if (bid == CONS0) {
        u32x4 e[4];
        gather<4, POLL_SLEEP, 0>(e, rsWS, WS_ML + lane * 16, 64 * 16, tA, tB);
        if (lane == 0) __hip_atomic_store((GAS u64*)p.ws, (u64)tA | ((u64)tB << 32), __ATOMIC_RELAXED, __HIP_MEMORY_SCOPE_AGENT);
      }
      if (is_cons) {
        const rsrc_t rsB2 = mk_rsrc(p.b2, DM * 2);
        const u32 B2v = bld32<0>(rsB2, (8 * coct + 2 * (lane & 3)) * 2, 0);
        XV = bld32<0>(rsX, cb * 1280 + (8 * coct + 2 * (lane & 3)) * 2, 0);
        const int cv = coct / 10, cp0 = (coct - 10 * cv) * 4;
        u32x4 f[2];
        gather<2, POLL_SLEEP, 0>(f, rsWS, WS_F + (((lane >> 2) * 8 + cv) * 80 + cb * 40 + cp0 + (lane & 3)) * 16, 16 * 8 * 80 * 16, tA, tB, lane < 44);
        float f0 = u2f(f[0].x) + u2f(f[1].x), f1 = u2f(f[0].z) + u2f(f[1].z);
        f0 += dpp<ROR4>(f0); f1 += dpp<ROR4>(f1); f0 += dpp<ROR8>(f0); f1 += dpp<ROR8>(f1);
        f0 = rows_sum(f0); f1 = rows_sum(f1);
        const float o0 = rbf(rbf(f0) + bflo(B2v)), o1 = rbf(rbf(f1) + bfhi(B2v));
        gate0 = rbf(o0 / (1.f + __expf(-o0))); gate1 = rbf(o1 / (1.f + __expf(-o1)));
      }
    }
    u32 offP = (!(c & 1) || g4 == 0) ? WS_Y + (prod * 320 + ((c & 1) ? 40 * pw + 32 + (c >> 1) : 40 * pw + 8 * g4 + (c >> 1))) * 16 : OOB;
    asm volatile("" : "+v"(offP));
    __builtin_amdgcn_s_setprio(3);
    __syncthreads();
    {
      f32x4 D[5];
#pragma unroll
      for (int n = 0; n < 5; ++n) D[n] = f32x4{0.f, 0.f, 0.f, 0.f};
      const f32x4 fr = *(const f32x4*)(lds + L_FW + 16 * g4);
#pragma unroll
      for (int rho = 0; rho < 4; ++rho) {
        const u32x4 A = *(const u32x4*)(lds + L_OA + c * 272 + 64 * rho + 16 * g4);
#pragma unroll
        for (int n = 0; n < 5; ++n) D[n] = __builtin_amdgcn_mfma_f32_16x16x32_bf16(__builtin_bit_cast(bf16x8, A), __builtin_bit_cast(bf16x8, BF[rho][n]), D[n], 0, 0, 0);
      }
      float e[5];
#pragma unroll
      for (int n = 0; n < 5; ++n) e[n] = fmaf(D[n][0], fr[0], D[n][1] * fr[1]) + fmaf(D[n][2], fr[2], D[n][3] * fr[3]);
      const float ya = tr16(tr32(e[0], e[2]), tr32(e[1], e[3]));
      const float yb = rows_sum(e[4]);
      const float yan = dpp<XOR1>(ya), ybn = dpp<XOR1>(yb);
      const bool odd = c & 1;
      publish(rsWS, offP, 0, odd ? f2u(ybn) : f2u(ya), odd ? f2u(yb) : f2u(yan), tg.x, tg.y);
    }
    if (is_cons) {
      u32x4 g[1];
      const int pj = lane >> 2;
      const u32 offY = WS_Y + ((cb * 128 + 8 * pj + ((pw + pj) & 7)) * 320 + 4 * coct + (lane & 3)) * 16;
      if (pw == 0) {
        g[0] = bld128<AUX_SC1>(rsWS, offY, 0);
        bool released = false;
#pragma unroll 1
        for (u32 spin = 0; spin < SPIN_CAP; ++spin) {
          asm volatile("" ::: "memory");
          const bool ok = tags_ok(g[0], tg.x, tg.y);
          const int missing = __builtin_popcountll(__ballot(!ok));
          if (!released && missing <= 4 * YTH) { released = true; *(volatile LAS u32*)(LAS unsigned char*)(lds + L_CNT + 12) = 1u; }
          if (missing == 0) break;
          const u32x4 t = bld128<AUX_SC1>(rsWS, ok ? OOB : offY, 0);
          if (!ok) g[0] = t;
          if (Y_SLEEP) __builtin_amdgcn_s_sleep(Y_SLEEP);
        }
        if (!released) *(volatile LAS u32*)(LAS unsigned char*)(lds + L_CNT + 12) = 1u;
      } else {
        lds_wait(lds + L_CNT + 12, 1u);
        gather<1, Y_SLEEP, 0>(g, rsWS, offY, 0, tg.x, tg.y);
      }
      float y0 = u2f(g[0].x), y1 = u2f(g[0].z);
      y0 += dpp<ROR4>(y0); y1 += dpp<ROR4>(y1); y0 += dpp<ROR8>(y0); y1 += dpp<ROR8>(y1);
      y0 = rows_sum(y0); y1 = rows_sum(y1);
      *(f32x2*)(lds + (lane < 4 ? L_YP + pw * 32 : L_TRASH) + lane * 8) = f32x2{y0, y1};
      __syncthreads();
      if (pw == 0) {
        f32x2 y = {0.f, 0.f};
#pragma unroll
        for (int k = 0; k < 8; ++k) y += *(const f32x2*)(lds + L_YP + k * 32 + (lane & 3) * 8);
        const float r0 = bflo(XV) + rbf(rbf(y.x) * gate0), r1 = bfhi(XV) + rbf(rbf(y.y) * gate1);
        if (lane < 4) ((GAS u32*)p.out)[(cb * DM + 8 * coct) / 2 + lane] = pkbf(r0, r1);
      }
    }
    return;
  }

  __builtin_amdgcn_s_setprio(1);
  const rsrc_t rsK = mk_rsrc((const char*)p.k + (size_t)pair * (SK * DH * 2) + (size_t)s * (2048 * DH * 2), 2048 * DH * 2);
  const rsrc_t rsV = mk_rsrc((const char*)p.v + (size_t)pair * (SK * DH * 2) + (size_t)s * (2048 * DH * 2), 2048 * DH * 2);
  const u32 vKV = w * 4096 + lane * 16;
  u32x4 S[128];
  auto ldS = [&](int i) INL -> u32x4 { const int t = i & 63; return i < 64 ? bld128<KV_AUX>(rsK, vKV + (t & 3) * 1024, (t >> 2) * 32768) : bld128<KV_AUX>(rsV, vKV + (t & 3) * 1024, (t >> 2) * 32768); };
  sfor<0, R>([&](auto I) INL { S[decltype(I)::value] = ldS(decltype(I)::value); });
  SB();
  __syncthreads();
  sfor<R, N0>([&](auto I) INL {
    constexpr int n = decltype(I)::value;
    asm volatile("" :: "v"(S[n - R][0]));
    SB();
    S[n] = ldS(n);
    SB();
  });
  __syncthreads();
  u32 q[4];
  { const u32x4 qq = *(const u32x4*)(lds + L_Q + c * 16); q[0] = qq[0]; q[1] = qq[1]; q[2] = qq[2]; q[3] = qq[3]; }

  float sc[4], d0 = 0.f, m1a = 0.f, m2a = 0.f, m3a = 0.f, m1 = 0.f, m2 = 0.f;
  sfor<0, 64>([&](auto I) INL {
    constexpr int i = decltype(I)::value;
    float d = dot2(S[i][0], q[0], 0.f);
    asm volatile("" : "+v"(d));
    SB();
    sfor<(i == 0 ? N0 : target(i - 1)), target(i)>([&](auto N) INL { S[decltype(N)::value] = ldS(decltype(N)::value); });
    SB();
    d = dot2(S[i][1], q[1], d); d = dot2(S[i][2], q[2], d); d = dot2(S[i][3], q[3], d);
    asm volatile("" : "+v"(d));
    constexpr int t = i & 15;
    if ((t & 1) == 0) d0 = d;
    else {
      const float a1 = merge<0>(d0, d, c);
      if ((t & 2) == 0) m1a = a1;
      else {
        m1 = merge<1>(m1a, a1, c);
        if ((t & 4) == 0) m2a = m1;
        else {
          m2 = merge<2>(m2a, m1, c);
          if ((t & 8) == 0) m3a = m2;
          else sc[i >> 4] = merge<3>(m3a, m2, c);
        }
      }
    }
    SB();
  });
  constexpr float C2 = 0.08838834764831845f * 1.4426950408889634f;
  float pr[4];
  {
#pragma unroll
    for (int j = 0; j < 4; ++j) sc[j] *= C2;
    const float mw = wave_max(fmaxf(fmaxf(sc[0], sc[1]), fmaxf(sc[2], sc[3])));
#pragma unroll
    for (int j = 0; j < 4; ++j) pr[j] = __builtin_amdgcn_exp2f(sc[j] - mw);
    const float lw = wave_sum((pr[0] + pr[1]) + (pr[2] + pr[3]));
    *(f32x2*)(lds + (lane == 0 ? L_ML + w * 8 : L_TRASH + lane * 8)) = f32x2{mw, lw};
    lds_signal(lds + L_CNT, lane);
  }
  SB();

  float acc[8];
#pragma unroll
  for (int k = 0; k < 8; ++k) acc[k] = 0.f;
  sfor<64, 128>([&](auto I) INL {
    constexpr int i = decltype(I)::value;
    constexpr int vi = i - 64, t = vi & 15;
    const u32x4 V = S[i];
    float v0 = bflo(V[0]);
    asm volatile("" : "+v"(v0));
    SB();
    sfor<target(i - 1), target(i)>([&](auto N) INL { S[decltype(N)::value] = ldS(decltype(N)::value); });
    if constexpr (vi == VSIG) { if (tid == 0) __hip_atomic_fetch_add((LAS u32*)(LAS unsigned char*)(lds + L_CNT + 8), 1u, __ATOMIC_RELAXED, __HIP_MEMORY_SCOPE_WORKGROUP); }
    SB();
    const float pb = bcast<t>(pr[vi >> 4]);
    acc[0] = fmaf(pb, v0, acc[0]); acc[1] = fmaf(pb, bfhi(V[0]), acc[1]);
#pragma unroll
    for (int u = 1; u < 4; ++u) { acc[2 * u] = fmaf(pb, bflo(V[u]), acc[2 * u]); acc[2 * u + 1] = fmaf(pb, bfhi(V[u]), acc[2 * u + 1]); }
    asm volatile("" : "+v"(acc[0]), "+v"(acc[1]), "+v"(acc[2]), "+v"(acc[3]), "+v"(acc[4]), "+v"(acc[5]), "+v"(acc[6]), "+v"(acc[7]));
    SB();
  });
  {
    const float z0 = tr16(tr32(acc[0], acc[4]), tr32(acc[2], acc[6])), z1 = tr16(tr32(acc[1], acc[5]), tr32(acc[3], acc[7]));
    const u32 hi = pkbf(z0, z1), lo = pkbf(z0 - bflo(hi), z1 - bfhi(hi));
    *(u32*)(lds + L_OA + w * 272 + 16 * c + 4 * g4) = hi;
    *(u32*)(lds + L_OA + (8 + w) * 272 + 16 * c + 4 * g4) = lo;
  }
  __syncthreads();
}

}

extern "C" void kernel_launch(void* const* d_in, const int* in_sizes, int n_in, void* d_out, int out_size, void* d_ws, size_t ws_size, hipStream_t stream) {
  (void)in_sizes; (void)n_in; (void)out_size; (void)ws_size;
  Params p;
  p.x = d_in[0]; p.k = d_in[1]; p.v = d_in[2]; p.wq = d_in[3]; p.wo = d_in[4];
  p.w1 = d_in[6]; p.b1 = d_in[7]; p.w2 = d_in[8]; p.b2 = d_in[9]; p.out = d_out; p.ws = d_ws;
  hipLaunchKernelGGL(fused_block, dim3(NWG), dim3(NTH), 0, stream, p);
}
```

```cpp
#include <hip/hip_runtime.h>
#include <hip/hip_bf16.h>
#include <stdint.h>

namespace {

typedef unsigned u32;
typedef unsigned long long u64;
typedef unsigned short u16;
typedef u32 u32x4 __attribute__((ext_vector_type(4)));
typedef u32 u32x2 __attribute__((ext_vector_type(2)));
typedef float f32x4 __attribute__((ext_vector_type(4)));
typedef float f32x2 __attribute__((ext_vector_type(2)));
typedef short s16x4 __attribute__((ext_vector_type(4)));
typedef __bf16 bf16x2 __attribute__((ext_vector_type(2)));
typedef __bf16 bf16x8 __attribute__((ext_vector_type(8)));
typedef __amdgpu_buffer_rsrc_t rsrc_t;
#define GAS __attribute__((address_space(1)))
#define LAS __attribute__((address_space(3)))

constexpr int DM = 640, NH = 32, SK = 8192, DH = 128, FF = 1728;
constexpr int NWG = 256, NTH = 1024;
constexpr int NFFN = 216;
constexpr int CONS0 = 96;
constexpr u32 WS_E = 0;
constexpr u32 WS_ML = 4096;
constexpr u32 WS_Q = 8192;
constexpr u32 WS_H = WS_Q + NH * 8 * 2 * 64 * 16;
constexpr u32 WS_F = WS_H + NFFN * 64 * 16;
constexpr u32 WS_Y = WS_F + NFFN * 80 * 16;
constexpr u32 WS_END = WS_Y + NWG * 320 * 16;
constexpr u32 OOB = 0x7ff00000u;
constexpr int AUX_NT = 2, AUX_SC1 = 16;
constexpr u32 SPIN_CAP = 1u << 16;

constexpr int KV_AUX = AUX_NT;
constexpr int R = 3;
constexpr int N0 = 25;
constexpr int BUMP = 2;
constexpr int Q_DELAY = 32;
constexpr int H_DELAY = 8;
constexpr int POLL_SLEEP = 16, Y_SLEEP = 1;
constexpr int WIN = 2;
constexpr int WO_SLEEP = 8, WO_STAG = 1;
constexpr int VSIG = 40;
constexpr int YTH = 4;
constexpr int target1(int i) {
  int t = i + 1 + R;
  const int top = N0 + (i + 1 < R ? i + 1 : R);
  if (t < top) t = top;
  if (i >= 64 - BUMP && i < 64) t = i + 1 + R + (i - (64 - BUMP) + 1);
  if (i >= 64 && t < 64 + R + BUMP) t = 64 + R + BUMP;
  return t > 128 ? 128 : t;
}
constexpr int target(int i) {
  int t = target1(0);
  for (int j = 1; j <= i; ++j) if (target1(j) > t) t = target1(j);
  return t;
}

struct Params {
  const void* x; const void* k; const void* v; const void* wq; const void* wo;
  const void* w1; const void* b1; const void* w2; const void* b2; void* out; void* ws;
};

__device__ __forceinline__ rsrc_t mk_rsrc(const void* p, u32 bytes) { return __builtin_amdgcn_make_buffer_rsrc((void*)p, 0, (int)bytes, 0x00020000); }
template <int AUX> __device__ __forceinline__ u32x4 bld128(rsrc_t r, u32 voff, u32 soff) { return __builtin_amdgcn_raw_buffer_load_b128(r, (int)voff, (int)soff, AUX); }
template <int AUX> __device__ __forceinline__ u32 bld32(rsrc_t r, u32 voff, u32 soff) { return __builtin_amdgcn_raw_buffer_load_b32(r, (int)voff, (int)soff, AUX); }
__device__ __forceinline__ void publish(rsrc_t r, u32 voff, u32 soff, u32 a, u32 b, u32 tA, u32 tB) { __builtin_amdgcn_raw_buffer_store_b128(u32x4{a, tA, b, tB}, r, (int)voff, (int)soff, AUX_SC1); }
__device__ __forceinline__ float bflo(u32 x) { return __builtin_bit_cast(float, x << 16); }
__device__ __forceinline__ float bfhi(u32 x) { return __builtin_bit_cast(float, x & 0xffff0000u); }
__device__ __forceinline__ u32 pkbf(float lo, float hi) { bf16x2 v = {(__bf16)lo, (__bf16)hi}; return __builtin_bit_cast(u32, v); }
__device__ __forceinline__ float rbf(float x) { return bflo(pkbf(x, 0.f) & 0xffffu); }
__device__ __forceinline__ float dot2(u32 a, u32 b, float c) { return __builtin_amdgcn_fdot2_f32_bf16(__builtin_bit_cast(bf16x2, a), __builtin_bit_cast(bf16x2, b), c, false); }
template <int CTRL> __device__ __forceinline__ float dpp(float x) { return __builtin_bit_cast(float, __builtin_amdgcn_mov_dpp(__builtin_bit_cast(int, x), CTRL, 0xf, 0xf, true)); }
constexpr int XOR1 = 0xB1, XOR2 = 0x4E, ROR4 = 0x124, ROR8 = 0x128, ROR2 = 0x122, ROR1 = 0x121, BCAST = 0x150;
__device__ __forceinline__ float u2f(u32 x) { return __builtin_bit_cast(float, x); }
__device__ __forceinline__ u32 f2u(float x) { return __builtin_bit_cast(u32, x); }
__device__ __forceinline__ float tr32(float a, float b) { auto s = __builtin_amdgcn_permlane32_swap(f2u(a), f2u(b), false, false); return u2f(s[0]) + u2f(s[1]); }
__device__ __forceinline__ float tr16(float a, float b) { auto s = __builtin_amdgcn_permlane16_swap(f2u(a), f2u(b), false, false); return u2f(s[0]) + u2f(s[1]); }
__device__ __forceinline__ float rows_sum(float x) { x = tr16(x, x); return tr32(x, x); }
__device__ __forceinline__ float rows_max(float x) {
  auto s = __builtin_amdgcn_permlane16_swap(f2u(x), f2u(x), false, false); x = fmaxf(u2f(s[0]), u2f(s[1]));
  auto t = __builtin_amdgcn_permlane32_swap(f2u(x), f2u(x), false, false); return fmaxf(u2f(t[0]), u2f(t[1]));
}
__device__ __forceinline__ float row16_sum(float x) { x += dpp<ROR8>(x); x += dpp<ROR4>(x); x += dpp<ROR2>(x); x += dpp<ROR1>(x); return x; }
__device__ __forceinline__ float row16_max(float x) { x = fmaxf(x, dpp<ROR8>(x)); x = fmaxf(x, dpp<ROR4>(x)); x = fmaxf(x, dpp<ROR2>(x)); x = fmaxf(x, dpp<ROR1>(x)); return x; }
__device__ __forceinline__ float wave_sum(float x) { return rows_sum(row16_sum(x)); }
__device__ __forceinline__ float wave_max(float x) { return rows_max(row16_max(x)); }
template <int K> __device__ __forceinline__ float merge(float lo, float hi, int c) {
  const bool bit = (c >> K) & 1;
  const float keep = bit ? hi : lo, send = bit ? lo : hi;
  constexpr int CTRL = K == 0 ? XOR1 : K == 1 ? XOR2 : K == 2 ? ROR4 : ROR8;
  return keep + dpp<CTRL>(send);
}
template <int T> __device__ __forceinline__ float bcast(float x) { return dpp<BCAST + T>(x); }
#define SB() __builtin_amdgcn_sched_barrier(0)
template <int I> struct IC { static constexpr int value = I; };
template <int B, int E, class F> __device__ __forceinline__ void sfor(F&& f) { if constexpr (B < E) { f(IC<B>{}); sfor<B + 1, E>(f); } }
#define INL __attribute__((always_inline))

__device__ __forceinline__ bool tags_ok(const u32x4& g, u32 tA, u32 tB) { return g.y == tA && g.w == tB; }
template <int N, int SLEEP, int DELAY>
__device__ __forceinline__ void gather(u32x4 (&g)[N], rsrc_t rs, u32 off, u32 stride, u32 tA, u32 tB, bool need_last = true) {
  if (DELAY) __builtin_amdgcn_s_sleep(DELAY);
#pragma unroll
  for (int k = 0; k < N; ++k) g[k] = bld128<AUX_SC1>(rs, (k < N - 1 || need_last) ? off : OOB, k * stride);
  if (!need_last) { g[N - 1].x = 0; g[N - 1].y = tA; g[N - 1].z = 0; g[N - 1].w = tB; }
  bool ok = true;
#pragma unroll
  for (int k = 0; k < N; ++k) ok = ok && tags_ok(g[k], tA, tB);
  if (!__all(ok)) {
#pragma unroll 1
    for (u32 spin = 0; spin < SPIN_CAP; ++spin) {
      asm volatile("" ::: "memory");
      u32x4 t[N];
#pragma unroll
      for (int k = 0; k < N; ++k) t[k] = bld128<AUX_SC1>(rs, tags_ok(g[k], tA, tB) ? OOB : off, k * stride);
      ok = true;
#pragma unroll
      for (int k = 0; k < N; ++k) { if (!tags_ok(g[k], tA, tB)) g[k] = t[k]; ok = ok && tags_ok(g[k], tA, tB); }
      if (__all(ok)) break;
      if (SLEEP) __builtin_amdgcn_s_sleep(SLEEP);
    }
  }
}
__device__ __forceinline__ void lds_wait(unsigned char* word, u32 want) {
#pragma unroll 1
  for (u32 spin = 0; spin < (SPIN_CAP << 4); ++spin) {
    if (*(volatile LAS u32*)(LAS unsigned char*)word >= want) break;
    __builtin_amdgcn_s_sleep(2);
  }
  asm volatile("" ::: "memory");
}
__device__ __forceinline__ void lds_signal(unsigned char* word, int lane) {
  asm volatile("s_waitcnt lgkmcnt(0)" ::: "memory");
  if (lane == 0) __hip_atomic_fetch_add((LAS u32*)(LAS unsigned char*)word, 1u, __ATOMIC_RELAXED, __HIP_MEMORY_SCOPE_WORKGROUP);
}

constexpr int L_XN = 0;
constexpr int L_QP = 2560;
constexpr int L_Q = L_QP + 8192;
constexpr int L_ML = L_Q + 256;
constexpr int L_CNT = L_ML + 64;
constexpr int L_TAG = L_CNT + 16;
constexpr int L_FW = L_CNT + 32;
constexpr int L_YP = L_FW + 64;
constexpr int L_H2 = L_YP + 256;
constexpr int L_FR = L_H2 + 512;
constexpr int L_OA = L_FR + 3840;
constexpr int L_WO = L_OA + 16 * 272;
constexpr int L_TRASH = L_WO + 8 * 5120;
constexpr int L_END = L_TRASH + 1024;
typedef char lds_regions_are_16_byte_aligned[(L_OA % 16 == 0 && L_WO % 16 == 0 && L_FW % 16 == 0 && L_FR % 16 == 0 && L_QP % 16 == 0 && L_YP % 16 == 0) ? 1 : -1];

__global__ void __launch_bounds__(NTH, 4) fused_block(Params p) {
  __shared__ __attribute__((aligned(16))) unsigned char lds[L_END];
  const int tid = threadIdx.x, lane = tid & 63, w = __builtin_amdgcn_readfirstlane(tid >> 6), g4 = lane >> 4, c = lane & 15;
  const int bid = blockIdx.x, xc = bid & 7, rr = bid >> 3;
  const int h = (rr >> 3) * 8 + xc, b = (rr >> 2) & 1, s = rr & 3;
  const int pair = b * NH + h, prod = pair * 4 + s;

  if (w >= 8) {
    const int pw = w - 8;
    const int fu = xc + 8 * (rr >> 3), fv = rr & 7;
    const bool is_ffn = fu < 27;
    const bool is_cons = bid >= CONS0;
    const int cj = bid - CONS0, cb = is_cons ? cj / 80 : 0, coct = is_cons ? cj % 80 : 0;
    const rsrc_t rsWS = mk_rsrc(p.ws, WS_END);
    const rsrc_t rsX = mk_rsrc(p.x, 2 * DM * 2);
    const rsrc_t rsWQ = mk_rsrc(p.wq, DM * NH * DH * 2);
    if (pw == 0) *(u32x4*)(lds + (lane == 0 ? L_CNT : L_TRASH + lane * 16)) = u32x4{0u, 0u, 0u, 0u};
    __syncthreads();
    const u32x4 x0a = bld128<0>(rsX, lane * 20, 0), x1a = bld128<0>(rsX, 1280 + lane * 20, 0);
    const u32 x0b = bld32<0>(rsX, lane * 20 + 16, 0), x1b = bld32<0>(rsX, 1280 + lane * 20 + 16, 0);
    const int qrow = 80 * (4 * b + s) + 10 * pw + 3 * g4;
    u32x4 WQ[3];
#pragma unroll
    for (int k = 0; k < 3; ++k) WQ[k] = bld128<0>(rsWQ, 3 * g4 + k < 10 ? (qrow + k) * 8192 + c * 16 : OOB, h * 256);
    const u32 xo = (qrow * 2) & ~3;
    const u32 xr00 = bld32<0>(rsX, xo, 0), xr01 = bld32<0>(rsX, xo + 4 < 1280 ? xo + 4 : OOB, 0), xr10 = bld32<0>(rsX, 1280 + xo, 0), xr11 = bld32<0>(rsX, xo + 4 < 1280 ? 1280 + xo + 4 : OOB, 0);
    const u32x2 E = __builtin_bit_cast(u32x2, __builtin_amdgcn_raw_buffer_load_b64(rsWS, pw == 0 ? (int)WS_E : (int)OOB, 0, AUX_SC1));
    SB();
    auto ln_stats = [&](const u32x4& ra, u32 rb, float& mb, float& inv) INL {
      float v[10];
#pragma unroll
      for (int k = 0; k < 4; ++k) { v[2 * k] = bflo(ra[k]); v[2 * k + 1] = bfhi(ra[k]); }
      v[8] = bflo(rb); v[9] = bfhi(rb);
      float sum = 0.f;
#pragma unroll
      for (int k = 0; k < 10; ++k) sum += v[k];
      const float mean = wave_sum(sum) * (1.f / DM);
      float sq = 0.f;
#pragma unroll
      for (int k = 0; k < 10; ++k) { const float d = v[k] - mean; sq = fmaf(d, d, sq); }
      const float var = wave_sum(sq) * (1.f / DM);
      mb = rbf(mean);
      inv = 1.f / rbf(sqrtf(rbf(rbf(var) + 1e-5f)));
    };
    float mb0, inv0, mb1, inv1;
    ln_stats(x0a, x0b, mb0, inv0);
    ln_stats(x1a, x1b, mb1, inv1);
    {
      float a[16];
#pragma unroll
      for (int k = 0; k < 16; ++k) a[k] = 0.f;
      const bool odd = qrow & 1;
#pragma unroll
      for (int k = 0; k < 3; ++k) {
        const float r0 = k == 0 ? (odd ? bfhi(xr00) : bflo(xr00)) : k == 1 ? (odd ? bflo(xr01) : bfhi(xr00)) : (odd ? bfhi(xr01) : bflo(xr01));
        const float r1 = k == 0 ? (odd ? bfhi(xr10) : bflo(xr10)) : k == 1 ? (odd ? bflo(xr11) : bfhi(xr10)) : (odd ? bfhi(xr11) : bflo(xr11));
        const float x0 = rbf(rbf(r0 - mb0) * inv0), x1 = rbf(rbf(r1 - mb1) * inv1);
#pragma unroll
        for (int u = 0; u < 4; ++u) {
          const float lo = bflo(WQ[k][u]), hi = bfhi(WQ[k][u]);
          a[2 * u] = fmaf(x0, lo, a[2 * u]); a[2 * u + 1] = fmaf(x0, hi, a[2 * u + 1]);
          a[8 + 2 * u] = fmaf(x1, lo, a[8 + 2 * u]); a[8 + 2 * u + 1] = fmaf(x1, hi, a[8 + 2 * u + 1]);
        }
      }
#pragma unroll
      for (int e = 0; e < 8; ++e) a[e] = tr32(a[e], a[8 + e]);
      const f32x4 z = {tr16(a[0], a[4]), tr16(a[1], a[5]), tr16(a[2], a[6]), tr16(a[3], a[7])};
      *(f32x4*)(lds + L_QP + (pw * 256 + (g4 >> 1) * 128 + 8 * c + 4 * (g4 & 1)) * 4) = z;
      lds_signal(lds + L_CNT + 4, lane);
    }
    if (pw == 0) {
      const u32 tA = E.x + 0x9E3779B9u, tB = E.y + 0x85EBCA6Bu;
      *(u32x2*)(lds + (lane == 0 ? L_TAG : L_TRASH + lane * 8)) = u32x2{tA, tB};
      lds_wait(lds + L_CNT + 4, 8u);
      f32x2 t0 = {0.f, 0.f}, t1 = {0.f, 0.f};
#pragma unroll
      for (int k = 0; k < 8; ++k) { t0 += *(const f32x2*)(lds + L_QP + (k * 256 + 2 * lane) * 4); t1 += *(const f32x2*)(lds + L_QP + (k * 256 + 128 + 2 * lane) * 4); }
      const u32 offP = WS_Q + (((h * 8 + b * 4 + s) * 2) * 64 + lane) * 16;
      publish(rsWS, offP, 0, f2u(t0.x), f2u(t0.y), tA, tB);
      publish(rsWS, offP, 64 * 16, f2u(t1.x), f2u(t1.y), tA, tB);
      u32x4 g[8];
      gather<8, POLL_SLEEP, Q_DELAY>(g, rsWS, WS_Q + ((h * 8 * 2 + b) * 64 + lane) * 16, 2 * 64 * 16, tA, tB);
      float q0 = 0.f, q1 = 0.f;
#pragma unroll
      for (int k = 0; k < 8; ++k) { q0 += u2f(g[k].x); q1 += u2f(g[k].z); }
      *(u32*)(lds + L_Q + lane * 4) = pkbf(q0, q1);
    }
    __syncthreads();
    const u32x2 tg = *(const u32x2*)(lds + L_TAG);
    const u32 tA = tg.x, tB = tg.y;

    if (pw == 1 && is_ffn) {
      const rsrc_t rsW1 = mk_rsrc(p.w1, DM * FF * 2), rsW2 = mk_rsrc(p.w2, FF * DM * 2), rsB1 = mk_rsrc(p.b1, FF * 2);
#pragma unroll
      for (int i = 0; i < 2; ++i) {
        const float mb = i ? mb1 : mb0, inv = i ? inv1 : inv0;
#pragma unroll
        for (int k = 0; k < 5; ++k) {
          const u32 r = k < 4 ? (i ? x1a[k & 3] : x0a[k & 3]) : (i ? x1b : x0b);
          const u32 d = pkbf(bflo(r) - mb, bfhi(r) - mb);
          *(u32*)(lds + L_XN + i * 1280 + lane * 20 + 4 * k) = pkbf(bflo(d) * inv, bfhi(d) * inv);
        }
      }
      {
        u32x4 W1[10];
        auto ldW1 = [&](auto K_) INL { constexpr int k = decltype(K_)::value; W1[k] = bld128<0>(rsW1, (lane >> 3) * (FF * 2) + (lane & 7) * 16, (80 * fv + 8 * k) * (FF * 2) + fu * 128); };
        sfor<0, WIN>(ldW1);
        float hv[16];
#pragma unroll
        for (int k = 0; k < 16; ++k) hv[k] = 0.f;
        sfor<0, 10>([&](auto K_) INL {
          constexpr int k = decltype(K_)::value;
          const int row = 80 * fv + 8 * k + (lane >> 3);
          const float x0 = bflo(*(const u16*)(lds + L_XN + row * 2)), x1 = bflo(*(const u16*)(lds + L_XN + 1280 + row * 2));
          asm volatile("" :: "v"(W1[k][0]));
          SB();
          if constexpr (k + WIN < 10) ldW1(IC<k + WIN>{});
          SB();
#pragma unroll
          for (int u = 0; u < 4; ++u) {
            const float lo = bflo(W1[k][u]), hi = bfhi(W1[k][u]);
            hv[2 * u] = fmaf(x0, lo, hv[2 * u]); hv[2 * u + 1] = fmaf(x0, hi, hv[2 * u + 1]);
            hv[8 + 2 * u] = fmaf(x1, lo, hv[8 + 2 * u]); hv[8 + 2 * u + 1] = fmaf(x1, hi, hv[8 + 2 * u + 1]);
          }
          asm volatile("" : "+v"(hv[0]), "+v"(hv[1]), "+v"(hv[2]), "+v"(hv[3]), "+v"(hv[4]), "+v"(hv[5]), "+v"(hv[6]), "+v"(hv[7]));
          asm volatile("" : "+v"(hv[8]), "+v"(hv[9]), "+v"(hv[10]), "+v"(hv[11]), "+v"(hv[12]), "+v"(hv[13]), "+v"(hv[14]), "+v"(hv[15]));
          SB();
        });
#pragma unroll
        for (int e = 0; e < 8; ++e) hv[e] = merge<3>(hv[e], hv[8 + e], c);
#pragma unroll
        for (int e = 0; e < 4; ++e) hv[e] = tr16(hv[e], hv[e + 4]);
        const float h0 = tr32(hv[0], hv[2]), h1 = tr32(hv[1], hv[3]);
        const u32 gi = ((lane >> 3) & 1) * 32 + 4 * (lane & 7) + 2 * ((lane >> 4) & 1) + (lane >> 5);
        publish(rsWS, WS_H + ((fu * 8 + fv) * 64 + gi) * 16, 0, f2u(h0), f2u(h1), tA, tB);
      }
      SB();
      {
        const u32 B1v = bld32<0>(rsB1, (lane & 31) * 4, fu * 128);
        float s0 = 0.f, s1 = 0.f;
        for (int i = 0; i < H_DELAY; ++i) __builtin_amdgcn_s_sleep(16);
        sfor<0, 8 / WIN>([&](auto J_) INL {
          constexpr int j = decltype(J_)::value;
          u32x4 g[WIN];
          gather<WIN, POLL_SLEEP, 0>(g, rsWS, WS_H + ((fu * 8 + j * WIN) * 64 + lane) * 16, 64 * 16, tA, tB);
#pragma unroll
          for (int k = 0; k < WIN; ++k) { s0 += u2f(g[k].x); s1 += u2f(g[k].z); }
          asm volatile("" : "+v"(s0), "+v"(s1));
          SB();
        });
        u32x4 W2[11];
        const int rg = lane / 10, ch = lane - 10 * rg;
        auto ldW2 = [&](auto K_) INL { constexpr int k = decltype(K_)::value; W2[k] = bld128<0>(rsW2, (lane < 60 && 6 * k + rg < 64) ? rg * (DM * 2) + ch * 16 : OOB, (64 * fu + 6 * k) * (DM * 2) + fv * 160); };
        sfor<0, WIN>(ldW2);
        SB();
        s0 = rbf(rbf(s0) + bflo(B1v)); s1 = rbf(rbf(s1) + bfhi(B1v));
        s0 = s0 > 0.f ? s0 : 0.f; s1 = s1 > 0.f ? s1 : 0.f;
        *(f32x2*)(lds + L_H2 + lane * 8) = f32x2{rbf(s0 * s0), rbf(s1 * s1)};
        float ov[16];
#pragma unroll
        for (int k = 0; k < 16; ++k) ov[k] = 0.f;
        sfor<0, 11>([&](auto K_) INL {
          constexpr int k = decltype(K_)::value;
          const int r = (6 * k + rg) & 63;
          const float a0 = *(const float*)(lds + L_H2 + r * 4), a1 = *(const float*)(lds + L_H2 + 256 + r * 4);
          asm volatile("" :: "v"(W2[k][0]));
          SB();
          if constexpr (k + WIN < 11) ldW2(IC<k + WIN>{});
          SB();
#pragma unroll
          for (int u = 0; u < 4; ++u) {
            const float lo = bflo(W2[k][u]), hi = bfhi(W2[k][u]);
            ov[2 * u] = fmaf(a0, lo, ov[2 * u]); ov[2 * u + 1] = fmaf(a0, hi, ov[2 * u + 1]);
            ov[8 + 2 * u] = fmaf(a1, lo, ov[8 + 2 * u]); ov[8 + 2 * u + 1] = fmaf(a1, hi, ov[8 + 2 * u + 1]);
          }
          asm volatile("" : "+v"(ov[0]), "+v"(ov[1]), "+v"(ov[2]), "+v"(ov[3]), "+v"(ov[4]), "+v"(ov[5]), "+v"(ov[6]), "+v"(ov[7]));
          asm volatile("" : "+v"(ov[8]), "+v"(ov[9]), "+v"(ov[10]), "+v"(ov[11]), "+v"(ov[12]), "+v"(ov[13]), "+v"(ov[14]), "+v"(ov[15]));
          SB();
        });
        unsigned char* fr = lds + (lane < 60 ? L_FR + lane * 64 : L_TRASH + (lane & 3) * 64);
#pragma unroll
        for (int k = 0; k < 4; ++k) *(f32x4*)(fr + 16 * k) = f32x4{ov[4 * k], ov[4 * k + 1], ov[4 * k + 2], ov[4 * k + 3]};
#pragma unroll
        for (int j = 0; j < 2; ++j) {
          const int gq = lane + 64 * j, gb = gq >= 40 ? 1 : 0, cp = (gq - 40 * gb) % 40;
          f32x2 t = {0.f, 0.f};
#pragma unroll
          for (int k = 0; k < 6; ++k) t += *(const f32x2*)(lds + L_FR + (k * 10 + (cp >> 2)) * 64 + (gb * 8 + 2 * (cp & 3)) * 4);
          publish(rsWS, gq < 80 ? WS_F + ((fu * 8 + fv) * 80 + gq) * 16 : OOB, 0, f2u(t.x), f2u(t.y), tA, tB);
        }
      }
      SB();
    }

    u32x4 BF[4][5];
    {
      const rsrc_t rsWO = mk_rsrc((const char*)p.wo + (size_t)h * (DH * DM * 2), DH * DM * 2);
      u32 vWO[5];
#pragma unroll
      for (int k = 0; k < 5; ++k) { const int it = lane + 64 * k; vWO[k] = (it / 10) * (DM * 2) + (it % 10) * 16 + pw * 160; }
      unsigned char* st = lds + L_WO + pw * 5120;
      if (pw >= 2) for (int i = 0; i < (pw - 2) * WO_STAG; ++i) __builtin_amdgcn_s_sleep(16);
      auto rounds = [&](auto WW_) INL {
        constexpr int WW = decltype(WW_)::value;
        sfor<0, 4>([&](auto RHO) INL {
          constexpr int rho = decltype(RHO)::value;
          u32x4 v[5];
          auto ldWO = [&](auto K_) INL {
            constexpr int k = decltype(K_)::value;
            v[k] = bld128<0>(rsWO, vWO[k], rho * 32 * DM * 2);
          };
          sfor<0, WW>(ldWO);
          sfor<0, 5>([&](auto K_) INL {
            constexpr int k = decltype(K_)::value;
            *(u32x4*)(st + (lane + 64 * k) * 16) = v[k];
            SB();
            if constexpr (k + WW < 5) ldWO(IC<k + WW>{});
            SB();
          });
#pragma unroll
          for (int n = 0; n < 5; ++n) {
            LAS unsigned char* a = (LAS unsigned char*)st + (8 * g4 + (c >> 2)) * 160 + 32 * n + 8 * (c & 3);
            const u32x2 lo = __builtin_bit_cast(u32x2, __builtin_amdgcn_ds_read_tr16_b64_v4i16((LAS s16x4*)a));
            const u32x2 hi = __builtin_bit_cast(u32x2, __builtin_amdgcn_ds_read_tr16_b64_v4i16((LAS s16x4*)(a + 4 * 160)));
            BF[rho][n] = u32x4{lo.x, lo.y, hi.x, hi.y};
            asm volatile("" : "+v"(BF[rho][n]));
          }
          if (rho < 3 && pw >= 2) for (int i = 0; i < WO_SLEEP; ++i) __builtin_amdgcn_s_sleep(16);
          SB();
        });
      };
      if (pw == 1) rounds(IC<5>{}); else rounds(IC<WIN>{});
    }

    float gate0 = 0.f, gate1 = 0.f;
    u32 XV = 0;
    if (pw == 0) {
      lds_wait(lds + L_CNT, 8u);
      const f32x2 ml = *(const f32x2*)(lds + L_ML + (lane & 7) * 8);
      const float mg = wave_max(ml.x);
      const float lg = row16_sum(c < 8 ? ml.y * __builtin_amdgcn_exp2f(ml.x - mg) : 0.f);
      publish(rsWS, lane == 0 ? WS_ML + prod * 16 : OOB, 0, f2u(mg), f2u(lg), tA, tB);
      lds_wait(lds + L_CNT + 8, 1u);
      u32x4 g[1];
      gather<1, POLL_SLEEP, 0>(g, rsWS, WS_ML + (pair * 4 + (lane & 3)) * 16, 0, tA, tB);
      const float mv = u2f(g[0].x), M = row16_max(mv);
      const float L = row16_sum(c < 4 ? u2f(g[0].z) * __builtin_amdgcn_exp2f(mv - M) : 0.f);
      *(float*)(lds + (lane < 16 ? L_FW : L_TRASH) + lane * 4) = __builtin_amdgcn_exp2f(ml.x - M) / L;
      if (bid == CONS0) {
        u32x4 e[4];
        gather<4, POLL_SLEEP, 0>(e, rsWS, WS_ML + lane * 16, 64 * 16, tA, tB);
        if (lane == 0) __hip_atomic_store((GAS u64*)p.ws, (u64)tA | ((u64)tB << 32), __ATOMIC_RELAXED, __HIP_MEMORY_SCOPE_AGENT);
      }
      if (is_cons) {
        const rsrc_t rsB2 = mk_rsrc(p.b2, DM * 2);
        const u32 B2v = bld32<0>(rsB2, (8 * coct + 2 * (lane & 3)) * 2, 0);
        XV = bld32<0>(rsX, cb * 1280 + (8 * coct + 2 * (lane & 3)) * 2, 0);
        const int cv = coct / 10, cp0 = (coct - 10 * cv) * 4;
        u32x4 f[2];
        gather<2, POLL_SLEEP, 0>(f, rsWS, WS_F + (((lane >> 2) * 8 + cv) * 80 + cb * 40 + cp0 + (lane & 3)) * 16, 16 * 8 * 80 * 16, tA, tB, lane < 44);
        float f0 = u2f(f[0].x) + u2f(f[1].x), f1 = u2f(f[0].z) + u2f(f[1].z);
        f0 += dpp<ROR4>(f0); f1 += dpp<ROR4>(f1); f0 += dpp<ROR8>(f0); f1 += dpp<ROR8>(f1);
        f0 = rows_sum(f0); f1 = rows_sum(f1);
        const float o0 = rbf(rbf(f0) + bflo(B2v)), o1 = rbf(rbf(f1) + bfhi(B2v));
        gate0 = rbf(o0 / (1.f + __expf(-o0))); gate1 = rbf(o1 / (1.f + __expf(-o1)));
      }
    }
    u32 offP = (!(c & 1) || g4 == 0) ? WS_Y + (prod * 320 + ((c & 1) ? 40 * pw + 32 + (c >> 1) : 40 * pw + 8 * g4 + (c >> 1))) * 16 : OOB;
    asm volatile("" : "+v"(offP));
    __builtin_amdgcn_s_setprio(3);
    __syncthreads();
    {
      f32x4 D[5];
#pragma unroll
      for (int n = 0; n < 5; ++n) D[n] = f32x4{0.f, 0.f, 0.f, 0.f};
      const f32x4 fr = *(const f32x4*)(lds + L_FW + 16 * g4);
#pragma unroll
      for (int rho = 0; rho < 4; ++rho) {
        const u32x4 A = *(const u32x4*)(lds + L_OA + c * 272 + 64 * rho + 16 * g4);
#pragma unroll
        for (int n = 0; n < 5; ++n) D[n] = __builtin_amdgcn_mfma_f32_16x16x32_bf16(__builtin_bit_cast(bf16x8, A), __builtin_bit_cast(bf16x8, BF[rho][n]), D[n], 0, 0, 0);
      }
      float e[5];
#pragma unroll
      for (int n = 0; n < 5; ++n) e[n] = fmaf(D[n][0], fr[0], D[n][1] * fr[1]) + fmaf(D[n][2], fr[2], D[n][3] * fr[3]);
      const float ya = tr16(tr32(e[0], e[2]), tr32(e[1], e[3]));
      const float yb = rows_sum(e[4]);
      const float yan = dpp<XOR1>(ya), ybn = dpp<XOR1>(yb);
      const bool odd = c & 1;
      publish(rsWS, offP, 0, odd ? f2u(ybn) : f2u(ya), odd ? f2u(yb) : f2u(yan), tg.x, tg.y);
    }
    if (is_cons) {
      u32x4 g[1];
      const int pj = lane >> 2;
      const u32 offY = WS_Y + ((cb * 128 + 8 * pj + ((pw + pj) & 7)) * 320 + 4 * coct + (lane & 3)) * 16;
      if (pw == 0) {
        g[0] = bld128<AUX_SC1>(rsWS, offY, 0);
        bool released = false;
#pragma unroll 1
        for (u32 spin = 0; spin < SPIN_CAP; ++spin) {
          asm volatile("" ::: "memory");
          const bool ok = tags_ok(g[0], tg.x, tg.y);
          const int missing = __builtin_popcountll(__ballot(!ok));
          if (!released && missing <= 4 * YTH) { released = true; *(volatile LAS u32*)(LAS unsigned char*)(lds + L_CNT + 12) = 1u; }
          if (missing == 0) break;
          const u32x4 t = bld128<AUX_SC1>(rsWS, ok ? OOB : offY, 0);
          if (!ok) g[0] = t;
          if (Y_SLEEP) __builtin_amdgcn_s_sleep(Y_SLEEP);
        }
        if (!released) *(volatile LAS u32*)(LAS unsigned char*)(lds + L_CNT + 12) = 1u;
      } else {
        lds_wait(lds + L_CNT + 12, 1u);
        gather<1, Y_SLEEP, 0>(g, rsWS, offY, 0, tg.x, tg.y);
      }
      float y0 = u2f(g[0].x), y1 = u2f(g[0].z);
      y0 += dpp<ROR4>(y0); y1 += dpp<ROR4>(y1); y0 += dpp<ROR8>(y0); y1 += dpp<ROR8>(y1);
      y0 = rows_sum(y0); y1 = rows_sum(y1);
      *(f32x2*)(lds + (lane < 4 ? L_YP + pw * 32 : L_TRASH) + lane * 8) = f32x2{y0, y1};
      __syncthreads();
      if (pw == 0) {
        f32x2 y = {0.f, 0.f};
#pragma unroll
        for (int k = 0; k < 8; ++k) y += *(const f32x2*)(lds + L_YP + k * 32 + (lane & 3) * 8);
        const float r0 = bflo(XV) + rbf(rbf(y.x) * gate0), r1 = bfhi(XV) + rbf(rbf(y.y) * gate1);
        if (lane < 4) ((GAS u32*)p.out)[(cb * DM + 8 * coct) / 2 + lane] = pkbf(r0, r1);
      }
    }
    return;
  }

  __builtin_amdgcn_s_setprio(1);
  const rsrc_t rsK = mk_rsrc((const char*)p.k + (size_t)pair * (SK * DH * 2) + (size_t)s * (2048 * DH * 2), 2048 * DH * 2);
  const rsrc_t rsV = mk_rsrc((const char*)p.v + (size_t)pair * (SK * DH * 2) + (size_t)s * (2048 * DH * 2), 2048 * DH * 2);
  const u32 vKV = w * 4096 + lane * 16;
  u32x4 S[128];
  auto ldS = [&](int i) INL -> u32x4 { const int t = i & 63; return i < 64 ? bld128<KV_AUX>(rsK, vKV + (t & 3) * 1024, (t >> 2) * 32768) : bld128<KV_AUX>(rsV, vKV + (t & 3) * 1024, (t >> 2) * 32768); };
  sfor<0, R>([&](auto I) INL { S[decltype(I)::value] = ldS(decltype(I)::value); });
  SB();
  __syncthreads();
  sfor<R, N0>([&](auto I) INL {
    constexpr int n = decltype(I)::value;
    asm volatile("" :: "v"(S[n - R][0]));
    SB();
    S[n] = ldS(n);
    SB();
  });
  __syncthreads();
  u32 q[4];
  { const u32x4 qq = *(const u32x4*)(lds + L_Q + c * 16); q[0] = qq[0]; q[1] = qq[1]; q[2] = qq[2]; q[3] = qq[3]; }

  float sc[4], d0 = 0.f, m1a = 0.f, m2a = 0.f, m3a = 0.f, m1 = 0.f, m2 = 0.f;
  sfor<0, 64>([&](auto I) INL {
    constexpr int i = decltype(I)::value;
    float d = dot2(S[i][0], q[0], 0.f);
    asm volatile("" : "+v"(d));
    SB();
    sfor<(i == 0 ? N0 : target(i - 1)), target(i)>([&](auto N) INL { S[decltype(N)::value] = ldS(decltype(N)::value); });
    SB();
    d = dot2(S[i][1], q[1], d); d = dot2(S[i][2], q[2], d); d = dot2(S[i][3], q[3], d);
    asm volatile("" : "+v"(d));
    constexpr int t = i & 15;
    if ((t & 1) == 0) d0 = d;
    else {
      const float a1 = merge<0>(d0, d, c);
      if ((t & 2) == 0) m1a = a1;
      else {
        m1 = merge<1>(m1a, a1, c);
        if ((t & 4) == 0) m2a = m1;
        else {
          m2 = merge<2>(m2a, m1, c);
          if ((t & 8) == 0) m3a = m2;
          else sc[i >> 4] = merge<3>(m3a, m2, c);
        }
      }
    }
    SB();
  });
  constexpr float C2 = 0.08838834764831845f * 1.4426950408889634f;
  float pr[4];
  {
#pragma unroll
    for (int j = 0; j < 4; ++j) sc[j] *= C2;
    const float mw = wave_max(fmaxf(fmaxf(sc[0], sc[1]), fmaxf(sc[2], sc[3])));
#pragma unroll
    for (int j = 0; j < 4; ++j) pr[j] = __builtin_amdgcn_exp2f(sc[j] - mw);
    const float lw = wave_sum((pr[0] + pr[1]) + (pr[2] + pr[3]));
    *(f32x2*)(lds + (lane == 0 ? L_ML + w * 8 : L_TRASH + lane * 8)) = f32x2{mw, lw};
    lds_signal(lds + L_CNT, lane);
  }
  SB();

  float acc[8];
#pragma unroll
  for (int k = 0; k < 8; ++k) acc[k] = 0.f;
  sfor<64, 128>([&](auto I) INL {
    constexpr int i = decltype(I)::value;
    constexpr int vi = i - 64, t = vi & 15;
    const u32x4 V = S[i];
    float v0 = bflo(V[0]);
    asm volatile("" : "+v"(v0));
    SB();
    sfor<target(i - 1), target(i)>([&](auto N) INL { S[decltype(N)::value] = ldS(decltype(N)::value); });
    if constexpr (vi == VSIG) { if (tid == 0) __hip_atomic_fetch_add((LAS u32*)(LAS unsigned char*)(lds + L_CNT + 8), 1u, __ATOMIC_RELAXED, __HIP_MEMORY_SCOPE_WORKGROUP); }
    SB();
    const float pb = bcast<t>(pr[vi >> 4]);
    acc[0] = fmaf(pb, v0, acc[0]); acc[1] = fmaf(pb, bfhi(V[0]), acc[1]);
#pragma unroll
    for (int u = 1; u < 4; ++u) { acc[2 * u] = fmaf(pb, bflo(V[u]), acc[2 * u]); acc[2 * u + 1] = fmaf(pb, bfhi(V[u]), acc[2 * u + 1]); }
    asm volatile("" : "+v"(acc[0]), "+v"(acc[1]), "+v"(acc[2]), "+v"(acc[3]), "+v"(acc[4]), "+v"(acc[5]), "+v"(acc[6]), "+v"(acc[7]));
    SB();
  });
  {
    const float z0 = tr16(tr32(acc[0], acc[4]), tr32(acc[2], acc[6])), z1 = tr16(tr32(acc[1], acc[5]), tr32(acc[3], acc[7]));
    const u32 hi = pkbf(z0, z1), lo = pkbf(z0 - bflo(hi), z1 - bfhi(hi));
    *(u32*)(lds + L_OA + w * 272 + 16 * c + 4 * g4) = hi;
    *(u32*)(lds + L_OA + (8 + w) * 272 + 16 * c + 4 * g4) = lo;
  }
  __syncthreads();
}

}

extern "C" void kernel_launch(void* const* d_in, const int* in_sizes, int n_in, void* d_out, int out_size, void* d_ws, size_t ws_size, hipStream_t stream) {
  (void)in_sizes; (void)n_in; (void)out_size; (void)ws_size;
  Params p;
  p.x = d_in[0]; p.k = d_in[1]; p.v = d_in[2]; p.wq = d_in[3]; p.wo = d_in[4];
  p.w1 = d_in[6]; p.b1 = d_in[7]; p.w2 = d_in[8]; p.b2 = d_in[9]; p.out = d_out; p.ws = d_ws;
  hipLaunchKernelGGL(fused_block, dim3(NWG), dim3(NTH), 0, stream, p);
}
```
